```python
import torch

def _softmax(x, axis=-1):
    return torch.softmax(x, dim=axis)
def _gelu(x):
    return torch.nn.functional.gelu(x, approximate="tanh")
def _silu(x):
    return torch.nn.functional.silu(x)
def _sigmoid(x):
    return torch.sigmoid(x)
def _layernorm(x, axis=-1, eps=1e-5):
    m = x.mean(dim=axis, keepdim=True)
    v = x.var(dim=axis, keepdim=True, unbiased=False)
    return (x - m) / torch.sqrt(v + eps)
def _scatter_add(tgt, idx, src, axis=0):
    return tgt.clone().index_add_(axis, idx, src)
def _segment_sum(src, seg, n):
    out = torch.zeros((n,) + src.shape[1:], dtype=src.dtype)
    return out.index_add_(0, seg, src)
def _pad(x, pw):
    flat = tuple(v for pair in reversed(pw) for v in pair)
    return torch.nn.functional.pad(x, flat)

kernel_name = 'concat_heads__gqa_block_cached__gqa_block_cached__near_ridge'

def setup_inputs():

    _w = (lambda a: (lambda t: t.to(torch.bfloat16) if t.is_floating_point() else t)(torch.as_tensor(a)))
    def _A_setup():
        B, Sq, D = 32, 96, 576
        Hq, Hkv, Sk, Dh = 32, 16, 512, 128
        assert Hq % Hkv == 0, (Hq, Hkv)
        return {
            "x": _w((lambda *s: torch.randn(*s, dtype=torch.bfloat16) * 0.8)(B, Sq, D)),
            "k_cache": _w((lambda *s: torch.randn(*s, dtype=torch.bfloat16) * 0.8)(B, Hkv, Sk, Dh)),
            "v_cache": _w((lambda *s: torch.randn(*s, dtype=torch.bfloat16) * 0.05)(B, Hkv, Sk, Dh)),
            "wq": _w((lambda *s: torch.randn(*s, dtype=torch.bfloat16) * 0.8)(D, Hq * Dh) * (D ** -0.5)),
            "wo": _w((lambda *s: torch.randn(*s, dtype=torch.bfloat16) * 0.05)(Hq * Dh, D) * ((Hq * Dh) ** -0.5)),
        }
    _A_in = _A_setup()

    def _B_setup():
        B, Sq, D = 32, 96, 576
        Hq, Hkv, Sk, Dh = 32, 16, 512, 128
        assert Hq % Hkv == 0, (Hq, Hkv)
        return {
            "x": _w((lambda *s: torch.randn(*s, dtype=torch.bfloat16) * 0.8)(B, Sq, D)),
            "k_cache": _w((lambda *s: torch.randn(*s, dtype=torch.bfloat16) * 0.8)(B, Hkv, Sk, Dh)),
            "v_cache": _w((lambda *s: torch.randn(*s, dtype=torch.bfloat16) * 0.05)(B, Hkv, Sk, Dh)),
            "wq": _w((lambda *s: torch.randn(*s, dtype=torch.bfloat16) * 0.8)(D, Hq * Dh) * (D ** -0.5)),
            "wo": _w((lambda *s: torch.randn(*s, dtype=torch.bfloat16) * 0.05)(Hq * Dh, D) * ((Hq * Dh) ** -0.5)),
        }
    _B_in = _B_setup()

    return {
        "A_x": _A_in["x"],
        "A_k_cache": _A_in["k_cache"],
        "A_v_cache": _A_in["v_cache"],
        "A_wq": _A_in["wq"],
        "A_wo": _A_in["wo"],
        "B_x": _B_in["x"],
        "B_k_cache": _B_in["k_cache"],
        "B_v_cache": _B_in["v_cache"],
        "B_wq": _B_in["wq"],
        "B_wo": _B_in["wo"],
    }

def reference(A_x, A_k_cache, A_v_cache, A_wq, A_wo, B_x, B_k_cache, B_v_cache, B_wq, B_wo):
    def _A_ref(x, k_cache, v_cache, wq, wo):
        B, Sq, D = x.shape
        Hq, Dh = 32, 128
        Hkv = k_cache.shape[1]
        rep = Hq // Hkv
        q = (lambda x, *d: x.permute(*d))((x @ wq).reshape(B, Sq, Hq, Dh), 0, 2, 1, 3)
        k = (lambda x, n, axis: torch.repeat_interleave(x, n, dim=axis))(k_cache, rep, axis=1)
        v = (lambda x, n, axis: torch.repeat_interleave(x, n, dim=axis))(v_cache, rep, axis=1)
        scores = torch.einsum("bhqd,bhkd->bhqk", q, k) * (Dh ** -0.5)
        p = _softmax(scores, axis=-1)
        o = torch.einsum("bhqk,bhkd->bhqd", p, v)
        return (lambda x, *d: x.permute(*d))(o, 0, 2, 1, 3).reshape(B, Sq, Hq * Dh) @ wo

    def _B_ref(x, k_cache, v_cache, wq, wo):
        B, Sq, D = x.shape
        Hq, Dh = 32, 128
        Hkv = k_cache.shape[1]
        rep = Hq // Hkv
        q = (lambda x, *d: x.permute(*d))((x @ wq).reshape(B, Sq, Hq, Dh), 0, 2, 1, 3)
        k = (lambda x, n, axis: torch.repeat_interleave(x, n, dim=axis))(k_cache, rep, axis=1)
        v = (lambda x, n, axis: torch.repeat_interleave(x, n, dim=axis))(v_cache, rep, axis=1)
        scores = torch.einsum("bhqd,bhkd->bhqk", q, k) * (Dh ** -0.5)
        p = _softmax(scores, axis=-1)
        o = torch.einsum("bhqk,bhkd->bhqd", p, v)
        return (lambda x, *d: x.permute(*d))(o, 0, 2, 1, 3).reshape(B, Sq, Hq * Dh) @ wo

    a = _A_ref(A_x, A_k_cache, A_v_cache, A_wq, A_wo)
    b = _B_ref(B_x, B_k_cache, B_v_cache, B_wq, B_wo)
    return torch.concatenate([a, b], axis=-1)
```

```cpp
#include <hip/hip_runtime.h>
#include <hip/hip_bf16.h>
typedef unsigned short u16;
__device__ __forceinline__ float bf2f(u16 v){ return __uint_as_float(((unsigned)v)<<16); }
__device__ __forceinline__ u16 f2bf(float f){ unsigned u=__float_as_uint(f); u += 0x7fffu + ((u>>16)&1u); return (u16)(u>>16); }

__global__ __launch_bounds__(256) void gemm_naive(const u16* __restrict__ A, const u16* __restrict__ B, u16* __restrict__ C, int M, int N, int K, int ldc){
  __shared__ float As[16][65]; __shared__ float Bs[16][65];
  int bm=blockIdx.y*64, bn=blockIdx.x*64; int tx=threadIdx.x&15, ty=threadIdx.x>>4;
  float acc[4][4]={};
  for(int k0=0;k0<K;k0+=16){
    for(int i=threadIdx.x;i<1024;i+=256){ int r=i>>4,c=i&15; As[c][r]=bf2f(A[(size_t)(bm+r)*K+k0+c]); }
    for(int i=threadIdx.x;i<1024;i+=256){ int r=i>>6,c=i&63; Bs[r][c]=bf2f(B[(size_t)(k0+r)*N+bn+c]); }
    __syncthreads();
    for(int k=0;k<16;k++){ float a[4],b[4]; for(int i=0;i<4;i++){a[i]=As[k][ty*4+i]; b[i]=Bs[k][tx*4+i];}
      for(int i=0;i<4;i++)for(int j=0;j<4;j++)acc[i][j]+=a[i]*b[j]; }
    __syncthreads();
  }
  for(int i=0;i<4;i++)for(int j=0;j<4;j++) C[(size_t)(bm+ty*4+i)*ldc+bn+tx*4+j]=f2bf(acc[i][j]);
}
__global__ __launch_bounds__(256) void attn_naive(const u16* __restrict__ Q, const u16* __restrict__ Kc, const u16* __restrict__ Vc, u16* __restrict__ O){
  int rb=blockIdx.x, h=blockIdx.y, b=blockIdx.z; int kvh=h>>1;
  __shared__ float q[8][128]; __shared__ float s[8][512]; __shared__ float red[8];
  for(int i=threadIdx.x;i<1024;i+=256){ int r=i>>7,d=i&127; q[r][d]=bf2f(Q[(size_t)(b*96+rb*8+r)*4096+h*128+d]); }
  __syncthreads();
  const u16* Kp=Kc+(size_t)(b*16+kvh)*512*128; const u16* Vp=Vc+(size_t)(b*16+kvh)*512*128;
  for(int k=threadIdx.x;k<512;k+=256){ float a[8]={}; for(int d=0;d<128;d++){ float kv=bf2f(Kp[k*128+d]); for(int r=0;r<8;r++)a[r]+=q[r][d]*kv; }
    for(int r=0;r<8;r++) s[r][k]=a[r]*0.08838834764831845f; }
  __syncthreads();
  { int r=threadIdx.x>>5, l=threadIdx.x&31; float m=-1e30f; for(int k=l;k<512;k+=32)m=fmaxf(m,s[r][k]);
    for(int o=16;o;o>>=1)m=fmaxf(m,__shfl_xor(m,o,32)); float su=0; for(int k=l;k<512;k+=32){float e=__expf(s[r][k]-m); s[r][k]=e; su+=e;}
    for(int o=16;o;o>>=1)su+=__shfl_xor(su,o,32); if(l==0)red[r]=1.f/su; }
  __syncthreads();
  { int d=threadIdx.x&127, rh=threadIdx.x>>7; float a[4]={}; for(int k=0;k<512;k++){ float v=bf2f(Vp[k*128+d]); for(int r=0;r<4;r++)a[r]+=s[rh*4+r][k]*v; }
    for(int r=0;r<4;r++) O[(size_t)(b*96+rb*8+rh*4+r)*4096+h*128+d]=f2bf(a[r]*red[rh*4+r]); }
}
extern "C" void kernel_launch(void* const* d_in, const int* in_sizes, int n_in, void* d_out, int out_size, void* d_ws, size_t ws_size, hipStream_t stream){
  u16* Q=(u16*)d_ws; u16* O=Q+(size_t)3072*4096;
  for(int br=0;br<2;br++){
    const u16* x=(const u16*)d_in[br*5+0]; const u16* k=(const u16*)d_in[br*5+1]; const u16* v=(const u16*)d_in[br*5+2];
    const u16* wq=(const u16*)d_in[br*5+3]; const u16* wo=(const u16*)d_in[br*5+4];
    gemm_naive<<<dim3(64,48),256,0,stream>>>(x,wq,Q,3072,4096,576,4096);
    attn_naive<<<dim3(12,32,32),256,0,stream>>>(Q,k,v,O);
    gemm_naive<<<dim3(9,48),256,0,stream>>>(O,wo,(u16*)d_out+br*576,3072,576,4096,1152);
  }
}
```

```cpp
#include <hip/hip_runtime.h>
#include <hip/hip_bf16.h>
typedef const char* cp;

__global__ __launch_bounds__(256, 1) void gqa_fused(cp xA, cp kA, cp vA, cp wqA, cp woA, cp xB, cp kB, cp vB, cp wqB, cp woB, char* ws, char* out) {
  __shared__ __attribute__((aligned(16))) char lds[163840];
  unsigned z;
  asm volatile("s_getpc_b64 s[80:81]\n v_mbcnt_lo_u32_b32 v254, -1, 0\n v_mbcnt_hi_u32_b32 v254, -1, v254\n v_lshlrev_b32 v254, 7, v254\n global_load_dword v255, v254, s[80:81]\n s_mov_b32 %0, 0"
               : "=s"(z) :: "s80", "s81", "v254", "v255", "memory");
  const unsigned wg = blockIdx.x + z, xcd = wg & 7u, idx = wg >> 3;
  const unsigned w = __builtin_amdgcn_readfirstlane(threadIdx.x >> 6);
  const unsigned br = xcd >> 2, bp = ((xcd >> 1) & 1u) * 8u + (idx >> 2), kp2 = (xcd & 1u) * 4u + (idx & 3u);
  const unsigned b = 2u * bp + (w >> 1), h = w & 1u;
  cp x = br ? xB : xA; cp k = br ? kB : kA; cp v = br ? vB : vA; cp wq = br ? wqB : wqA; cp wo = br ? woB : woA;
  char* ows = ws + 65536;
  cp kp = k + (size_t)(b * 16u + 2u * kp2) * 131072u;
  cp vp = v + (size_t)(b * 16u + 2u * kp2) * 131072u;
  cp xp = x + (size_t)b * 110592u;
  cp wp = wq + (size_t)kp2 * 1024u;
  char* op = ows + (size_t)((br * 32u + b) * 32u + 4u * kp2 + h) * 24576u;
  const unsigned wrec = 576u * 8192u - kp2 * 1024u;
  const unsigned ldsb = (unsigned)(size_t)lds;
  const unsigned wwarm = (8u * (bp & 7u) + 2u * w) * 8192u;
  const unsigned xwarm = (24u * (kp2 & 3u) + 12u * h) * 1152u;
  const unsigned rot = ((idx + 5u * xcd + 4u * (w >> 1)) & 7u) * 16384u;
  const unsigned nq4 = kp2 & 3u, kH = kp2 >> 2, beta = w >> 1;
  const unsigned wooff = (2048u * kH + 1024u * h + 32u * beta) * 1152u + 288u * nq4;
  cp wop = wo + wooff;
  const unsigned worec = 4096u * 1152u - wooff;
  cp olp = ows + (size_t)((br * 32u + b) * 32u + 16u * kH + 8u * h) * 24576u;
  char* outp = out + (size_t)b * 96u * 2304u + (size_t)(576u * br + 144u * nq4) * 2u;
  const unsigned cl = br * 16u + bp;
  char* myflag = ws + (size_t)((cl * 4u + (kp2 >> 1)) * 128u + (kp2 & 1u) * 4u);
  cp pollflag = ws + (size_t)((cl * 4u + 2u * kH + h) * 128u);
  cp wowarm = wo + (size_t)(2048u * kH + 16u * (idx * 4u + w)) * 1152u;
  const unsigned mwg = cl * 8u + kp2, pwg = cl * 8u + (kp2 ^ 4u);
  char* xsend = ws + ((size_t)64u << 20) + (size_t)(pwg * 4u + w) * 14336u;
  cp xrecv = ws + ((size_t)64u << 20) + (size_t)(mwg * 4u + w) * 14336u;
  char* fsend = ws + 16384u + (size_t)(pwg * 128u + w * 4u);
  cp frecv = ws + 16384u + (size_t)(mwg * 128u + w * 4u);
  char* eptr = ws + 49152u + (size_t)cl * 128u;
  const unsigned cwarm = ((idx * 4u + w) % 7u) * 8192u;
  asm volatile(
"s_mov_b64 s[48:49], %3\n"
"s_mov_b32 s50, %6\n"
"s_mov_b32 s51, 0x20000\n"
"s_mov_b64 s[44:45], %2\n"
"s_mov_b32 s46, 110592\n"
"s_mov_b32 s47, 0x20000\n"
"s_mov_b64 s[36:37], %0\n"
"s_mov_b32 s38, 0x20000\n"
"s_mov_b32 s39, 0x20000\n"
"s_mov_b64 s[40:41], %1\n"
"s_mov_b32 s42, 0x20000\n"
"s_mov_b32 s43, 0x20000\n"
"s_mov_b64 s[52:53], %4\n"
"s_mov_b32 s54, 0\n"
"s_mov_b32 s55, 0x20000\n"
"s_lshr_b32 s73, %5, 1\n"
"s_and_b32 s65, %5, 1\n"
"s_lshl_b32 s56, s65, 12\n"
"s_lshl_b32 s57, s73, 13\n"
"s_add_u32 s57, s57, s56\n"
"s_add_u32 s57, s57, %7\n"
"s_add_u32 s58, s57, 65536\n"
"s_add_u32 s56, s56, %11\n"
"s_mul_i32 s71, s65, 55296\n"
"s_mov_b32 s79, s56\n"
"s_add_u32 s78, s56, 8192\n"
"s_and_b32 s78, s78, 0x1ffff\n"
"s_mul_i32 s63, s73, 12288\n"
"s_mul_i32 s67, s65, 6144\n"
"s_add_u32 s63, s63, s67\n"
"s_add_u32 s63, s63, %7\n"
"s_lshl_b32 s64, %5, 13\n"
"s_add_u32 s64, s64, %7\n"
"s_lshl_b32 s72, %5, 17\n"
"s_mov_b32 s59, s71\n"
"s_add_u32 s60, s71, 36864\n"
"s_mov_b32 s61, s72\n"
"s_add_u32 s62, s72, 65536\n"
"s_mov_b32 s68, 0x3e0293ee\n"
"s_mov_b32 s69, 0x3e0293ee\n"
"s_mov_b32 s70, 0x53800000\n"
"s_mov_b32 s66, 0\n"
"s_mov_b32 s67, 0\n"
"s_mov_b32 s76, %8\n"
"s_mov_b32 s77, %9\n"
"v_mbcnt_lo_u32_b32 v0, -1, 0\n"
"v_mbcnt_hi_u32_b32 v0, -1, v0\n"
"v_lshlrev_b32 v254, 7, v0\n"
"v_lshrrev_b32 v1, 4, v0\n"
"v_and_b32 v2, 15, v0\n"
"v_bfe_u32 v3, v0, 2, 2\n"
"v_and_b32 v4, 3, v0\n"
"v_lshrrev_b32 v5, 5, v0\n"
"v_and_b32 v6, 31, v0\n"
"v_add_u32 v7, 0, v5\n"
"v_lshlrev_b32 v7, 2, v7\n"
"v_xor_b32 v7, v7, v6\n"
"v_lshlrev_b32 v7, 4, v7\n"
"v_lshl_add_u32 v7, v5, 13, v7\n"
"v_add_u32 v244, 0, v7\n"
"v_add_u32 v7, 2, v5\n"
"v_lshlrev_b32 v7, 2, v7\n"
"v_xor_b32 v7, v7, v6\n"
"v_lshlrev_b32 v7, 4, v7\n"
"v_lshl_add_u32 v7, v5, 13, v7\n"
"v_add_u32 v245, 15360, v7\n"
"v_add_u32 v7, 0, v5\n"
"v_lshlrev_b32 v7, 2, v7\n"
"v_xor_b32 v7, v7, v6\n"
"v_lshlrev_b32 v7, 4, v7\n"
"v_lshl_add_u32 v7, v5, 13, v7\n"
"v_add_u32 v246, 30720, v7\n"
"v_add_u32 v7, 2, v5\n"
"v_lshlrev_b32 v7, 2, v7\n"
"v_xor_b32 v7, v7, v6\n"
"v_lshlrev_b32 v7, 4, v7\n"
"v_lshl_add_u32 v7, v5, 13, v7\n"
"v_add_u32 v247, 46080, v7\n"
"s_add_u32 m0, s64, 131072\n"
"s_nop 0\n"
"buffer_load_dwordx4 v244, s[48:51], s61 offen offset:0 lds\n"
"buffer_load_dwordx4 v245, s[48:51], s61 offen offset:1024 lds\n"
"buffer_load_dwordx4 v246, s[48:51], s61 offen offset:2048 lds\n"
"buffer_load_dwordx4 v247, s[48:51], s61 offen offset:3072 lds\n"
"s_add_u32 m0, s64, 135168\n"
"s_nop 0\n"
"buffer_load_dwordx4 v244, s[48:51], s62 offen offset:0 lds\n"
"buffer_load_dwordx4 v245, s[48:51], s62 offen offset:1024 lds\n"
"buffer_load_dwordx4 v246, s[48:51], s62 offen offset:2048 lds\n"
"buffer_load_dwordx4 v247, s[48:51], s62 offen offset:3072 lds\n"
"s_add_u32 s61, s61, 524288\n"
"s_add_u32 s62, s62, 524288\n"
"v_lshrrev_b32 v5, 3, v0\n"
"v_mul_u32_u24 v5, 1152, v5\n"
"v_and_b32 v6, 7, v0\n"
"v_add_u32 v7, 0, v1\n"
"v_xor_b32 v7, v7, v6\n"
"v_lshl_add_u32 v7, v7, 4, v5\n"
"v_add_u32 v248, 0, v7\n"
"v_add_u32 v7, 4, v1\n"
"v_xor_b32 v7, v7, v6\n"
"v_lshl_add_u32 v7, v7, 4, v5\n"
"v_add_u32 v249, 8192, v7\n"
"v_add_u32 v7, 0, v1\n"
"v_xor_b32 v7, v7, v6\n"
"v_lshl_add_u32 v7, v7, 4, v5\n"
"v_add_u32 v250, 16384, v7\n"
"v_add_u32 v7, 4, v1\n"
"v_xor_b32 v7, v7, v6\n"
"v_lshl_add_u32 v7, v7, 4, v5\n"
"v_add_u32 v251, 24576, v7\n"
"s_add_u32 m0, s63, 106496\n"
"s_nop 0\n"
"buffer_load_dwordx4 v248, s[44:47], s59 offen offset:0 lds\n"
"buffer_load_dwordx4 v249, s[44:47], s59 offen offset:1024 lds\n"
"buffer_load_dwordx4 v250, s[44:47], s59 offen offset:2048 lds\n"
"buffer_load_dwordx4 v251, s[44:47], s59 offen offset:3072 lds\n"
"s_add_u32 m0, s63, 110592\n"
"s_nop 0\n"
"buffer_load_dwordx4 v248, s[44:47], s60 offen offset:0 lds\n"
"buffer_load_dwordx4 v249, s[44:47], s60 offen offset:1024 lds\n"
"s_add_u32 s59, s59, 128\n"
"s_add_u32 s60, s60, 128\n"
"s_add_u32 m0, s64, 32768\n"
"s_nop 0\n"
"buffer_load_dwordx4 v244, s[48:51], s61 offen offset:0 lds\n"
"buffer_load_dwordx4 v245, s[48:51], s61 offen offset:1024 lds\n"
"buffer_load_dwordx4 v246, s[48:51], s61 offen offset:2048 lds\n"
"buffer_load_dwordx4 v247, s[48:51], s61 offen offset:3072 lds\n"
"s_add_u32 m0, s64, 36864\n"
"s_nop 0\n"
"buffer_load_dwordx4 v244, s[48:51], s62 offen offset:0 lds\n"
"buffer_load_dwordx4 v245, s[48:51], s62 offen offset:1024 lds\n"
"buffer_load_dwordx4 v246, s[48:51], s62 offen offset:2048 lds\n"
"buffer_load_dwordx4 v247, s[48:51], s62 offen offset:3072 lds\n"
"s_add_u32 s61, s61, 524288\n"
"s_add_u32 s62, s62, 524288\n"
"s_add_u32 m0, s63, 81920\n"
"s_nop 0\n"
"buffer_load_dwordx4 v248, s[44:47], s59 offen offset:0 lds\n"
"buffer_load_dwordx4 v249, s[44:47], s59 offen offset:1024 lds\n"
"buffer_load_dwordx4 v250, s[44:47], s59 offen offset:2048 lds\n"
"buffer_load_dwordx4 v251, s[44:47], s59 offen offset:3072 lds\n"
"s_add_u32 m0, s63, 86016\n"
"s_nop 0\n"
"buffer_load_dwordx4 v248, s[44:47], s60 offen offset:0 lds\n"
"buffer_load_dwordx4 v249, s[44:47], s60 offen offset:1024 lds\n"
"s_add_u32 s59, s59, 128\n"
"s_add_u32 s60, s60, 128\n"
"v_and_b32 v8, 7, v0\n"
"v_lshlrev_b32 v8, 2, v8\n"
"global_load_dword v221, v8, %23 sc1\n"
"v_mbcnt_lo_u32_b32 v253, -1, 0\n"
"v_mbcnt_hi_u32_b32 v253, -1, v253\n"
"v_lshrrev_b32 v252, 3, v253\n"
"v_lshlrev_b32 v252, 19, v252\n"
"v_and_b32 v253, 7, v253\n"
"v_lshl_add_u32 v252, v253, 7, v252\n"
"v_lshrrev_b32 v253, 2, v253\n"
"v_mul_u32_u24 v253, 7680, v253\n"
"v_add_u32 v252, v252, v253\n"
"v_mbcnt_lo_u32_b32 v8, -1, 0\n"
"v_mbcnt_hi_u32_b32 v8, -1, v8\n"
"v_mul_u32_u24 v253, 43, v8\n"
"v_lshrrev_b32 v253, 9, v253\n"
"v_mul_u32_u24 v8, 1152, v8\n"
"v_mul_u32_u24 v253, 13696, v253\n"
"v_sub_u32 v253, v8, v253\n"
"s_add_u32 s73, s76, 1048576\n"
"s_bfm_b64 exec, 16, 0\n"
"buffer_load_dword v255, v252, s[48:51], s73 offen\n"
"s_mov_b64 exec, -1\n"
"s_add_u32 s73, s77, 256\n"
"s_bfm_b64 exec, 24, 0\n"
"buffer_load_dword v255, v253, s[44:47], s73 offen\n"
"s_mov_b64 exec, -1\n"
"s_getpc_b64 s[80:81]\n"
"v_add_u32 v8, %22, v254\n"
"global_load_dword v255, v8, s[80:81]\n"
"s_bfm_b64 exec, 1, 0\n"
"buffer_load_dword v255, v254, s[36:39], s56 offen\n"
"buffer_load_dword v255, v254, s[40:43], s56 offen\n"
"s_mov_b64 exec, -1\n"
"s_lshl_b32 s74, s65, 3\n"
"v_add_u32 v7, 0, v1\n"
"v_add_u32 v7, s74, v7\n"
"v_xor_b32 v7, v7, v2\n"
"v_lshlrev_b32 v7, 4, v7\n"
"v_lshl_add_u32 v234, v1, 8, v7\n"
"v_add_u32 v7, 4, v1\n"
"v_add_u32 v7, s74, v7\n"
"v_xor_b32 v7, v7, v2\n"
"v_lshlrev_b32 v7, 4, v7\n"
"v_lshl_add_u32 v235, v1, 8, v7\n"
"v_or_b32 v7, 0, v1\n"
"v_lshlrev_b32 v7, 1, v7\n"
"v_xor_b32 v7, v7, v2\n"
"v_lshlrev_b32 v7, 4, v7\n"
"v_lshl_add_u32 v236, v1, 8, v7\n"
"v_or_b32 v7, 4, v1\n"
"v_lshlrev_b32 v7, 1, v7\n"
"v_xor_b32 v7, v7, v2\n"
"v_lshlrev_b32 v7, 4, v7\n"
"v_lshl_add_u32 v237, v1, 8, v7\n"
"s_lshr_b32 s73, %5, 1\n"
"s_lshl_b32 s74, s73, 13\n"
"s_add_u32 s74, s74, %7\n"
"v_lshl_add_u32 v5, v3, 3, v4\n"
"v_lshlrev_b32 v5, 8, v5\n"
"v_add_u32 v5, s74, v5\n"
"v_add_u32 v7, 0, v1\n"
"v_xor_b32 v7, v7, v2\n"
"v_lshl_add_u32 v222, v7, 4, v5\n"
"v_add_u32 v7, 4, v1\n"
"v_xor_b32 v7, v7, v2\n"
"v_lshl_add_u32 v223, v7, 4, v5\n"
"v_add_u32 v7, 8, v1\n"
"v_xor_b32 v7, v7, v2\n"
"v_lshl_add_u32 v224, v7, 4, v5\n"
"v_add_u32 v7, 12, v1\n"
"v_xor_b32 v7, v7, v2\n"
"v_lshl_add_u32 v225, v7, 4, v5\n"
"v_lshl_add_u32 v5, v1, 3, v3\n"
"v_lshlrev_b32 v5, 8, v5\n"
"v_lshl_add_u32 v5, v4, 3, v5\n"
"v_add_u32 v5, s74, v5\n"
"v_add_u32 v5, 65536, v5\n"
"v_and_b32 v6, 1, v1\n"
"v_lshl_or_b32 v6, v6, 2, v3\n"
"v_xor_b32 v7, 0, v6\n"
"v_lshl_add_u32 v226, v7, 5, v5\n"
"v_xor_b32 v7, 1, v6\n"
"v_lshl_add_u32 v227, v7, 5, v5\n"
"v_xor_b32 v7, 2, v6\n"
"v_lshl_add_u32 v228, v7, 5, v5\n"
"v_xor_b32 v7, 3, v6\n"
"v_lshl_add_u32 v229, v7, 5, v5\n"
"v_xor_b32 v7, 4, v6\n"
"v_lshl_add_u32 v230, v7, 5, v5\n"
"v_xor_b32 v7, 5, v6\n"
"v_lshl_add_u32 v231, v7, 5, v5\n"
"v_xor_b32 v7, 6, v6\n"
"v_lshl_add_u32 v232, v7, 5, v5\n"
"v_xor_b32 v7, 7, v6\n"
"v_lshl_add_u32 v233, v7, 5, v5\n"
"s_mul_i32 s74, s73, 12288\n"
"s_add_u32 s74, s74, %7\n"
"s_add_u32 s74, s74, 81920\n"
"v_lshlrev_b32 v5, 7, v2\n"
"v_add_u32 v5, s74, v5\n"
"v_lshrrev_b32 v6, 1, v2\n"
"v_xor_b32 v7, v1, v6\n"
"v_lshl_add_u32 v242, v7, 4, v5\n"
"s_lshl_b32 s74, s65, 8\n"
"s_add_u32 s74, s74, %7\n"
"s_add_u32 s74, s74, 131072\n"
"v_lshl_add_u32 v5, v1, 3, v3\n"
"v_lshlrev_b32 v5, 9, v5\n"
"v_lshl_add_u32 v5, v4, 4, v5\n"
"v_add_u32 v5, s74, v5\n"
"v_xor_b32 v7, 0, v3\n"
"v_lshl_add_u32 v238, v7, 6, v5\n"
"v_xor_b32 v7, 1, v3\n"
"v_lshl_add_u32 v239, v7, 6, v5\n"
"v_xor_b32 v7, 2, v3\n"
"v_lshl_add_u32 v240, v7, 6, v5\n"
"v_xor_b32 v7, 3, v3\n"
"v_lshl_add_u32 v241, v7, 6, v5\n"
".Lf_round:\n"
"s_cmp_lg_u32 s66, 0\n"
"s_cbranch_scc1 .Lf_npc\n"
"s_waitcnt vmcnt(20) lgkmcnt(0)\n"
".Lf_npc:\n"
"s_cmp_eq_u32 s66, 0\n"
"s_cbranch_scc1 .Lf_npw\n"
"s_waitcnt vmcnt(26) lgkmcnt(0)\n"
".Lf_npw:\n"
"s_barrier\n"
"ds_read_b64_tr_b16 a[0:1], v238 offset:0\n"
"ds_read_b64_tr_b16 a[2:3], v238 offset:2048\n"
"ds_read_b64_tr_b16 a[4:5], v238 offset:8\n"
"ds_read_b64_tr_b16 a[6:7], v238 offset:2056\n"
"ds_read_b64_tr_b16 a[8:9], v239 offset:0\n"
"ds_read_b64_tr_b16 a[10:11], v239 offset:2048\n"
"ds_read_b64_tr_b16 a[12:13], v239 offset:8\n"
"ds_read_b64_tr_b16 a[14:15], v239 offset:2056\n"
"ds_read_b64_tr_b16 a[16:17], v240 offset:0\n"
"ds_read_b64_tr_b16 a[18:19], v240 offset:2048\n"
"ds_read_b64_tr_b16 a[20:21], v240 offset:8\n"
"ds_read_b64_tr_b16 a[22:23], v240 offset:2056\n"
"ds_read_b64_tr_b16 a[24:25], v241 offset:0\n"
"ds_read_b64_tr_b16 a[26:27], v241 offset:2048\n"
"ds_read_b64_tr_b16 a[28:29], v241 offset:8\n"
"ds_read_b64_tr_b16 a[30:31], v241 offset:2056\n"
"ds_read_b64_tr_b16 a[32:33], v238 offset:16384\n"
"ds_read_b64_tr_b16 a[34:35], v238 offset:18432\n"
"ds_read_b64_tr_b16 a[36:37], v238 offset:16392\n"
"ds_read_b64_tr_b16 a[38:39], v238 offset:18440\n"
"ds_read_b64_tr_b16 a[40:41], v239 offset:16384\n"
"ds_read_b64_tr_b16 a[42:43], v239 offset:18432\n"
"ds_read_b64_tr_b16 a[44:45], v239 offset:16392\n"
"ds_read_b64_tr_b16 a[46:47], v239 offset:18440\n"
"ds_read_b64_tr_b16 a[48:49], v240 offset:16384\n"
"ds_read_b64_tr_b16 a[50:51], v240 offset:18432\n"
"ds_read_b64_tr_b16 a[52:53], v240 offset:16392\n"
"ds_read_b64_tr_b16 a[54:55], v240 offset:18440\n"
"ds_read_b64_tr_b16 a[56:57], v241 offset:16384\n"
"ds_read_b64_tr_b16 a[58:59], v241 offset:18432\n"
"ds_read_b64_tr_b16 a[60:61], v241 offset:16392\n"
"ds_read_b64_tr_b16 a[62:63], v241 offset:18440\n"
"v_xor_b32 v238, 0x28000, v238\n"
"v_xor_b32 v239, 0x28000, v239\n"
"v_xor_b32 v240, 0x28000, v240\n"
"v_xor_b32 v241, 0x28000, v241\n"
"ds_read_b128 a[128:131], v242 offset:24576\n"
"v_xor_b32 v242, 64, v242\n"
"ds_read_b128 a[132:135], v242 offset:24576\n"
"v_xor_b32 v242, 64, v242\n"
"ds_read_b128 a[136:139], v242 offset:26624\n"
"v_xor_b32 v242, 64, v242\n"
"ds_read_b128 a[140:143], v242 offset:26624\n"
"v_xor_b32 v242, 64, v242\n"
"ds_read_b128 a[144:147], v242 offset:28672\n"
"v_xor_b32 v242, 64, v242\n"
"ds_read_b128 a[148:151], v242 offset:28672\n"
"v_xor_b32 v242, 64, v242\n"
"ds_read_b128 a[152:155], v242 offset:30720\n"
"v_xor_b32 v242, 64, v242\n"
"ds_read_b128 a[156:159], v242 offset:30720\n"
"v_xor_b32 v242, 64, v242\n"
"ds_read_b128 a[160:163], v242 offset:32768\n"
"v_xor_b32 v242, 64, v242\n"
"ds_read_b128 a[164:167], v242 offset:32768\n"
"v_xor_b32 v242, 64, v242\n"
"ds_read_b128 a[168:171], v242 offset:34816\n"
"v_xor_b32 v242, 64, v242\n"
"ds_read_b128 a[172:175], v242 offset:34816\n"
"v_xor_b32 v242, 64, v242\n"
"s_cmp_lg_u32 s66, 0\n"
"s_cbranch_scc1 .Lf_nc0\n"
"s_waitcnt vmcnt(6) lgkmcnt(0)\n"
".Lf_nc0:\n"
"s_cmp_eq_u32 s66, 0\n"
"s_cbranch_scc1 .Lf_nw0\n"
"s_waitcnt vmcnt(12) lgkmcnt(0)\n"
".Lf_nw0:\n"
"s_barrier\n"
"v_mfma_f32_16x16x32_bf16 v[0:3], a[0:3], a[128:131], 0\n"
"v_mfma_f32_16x16x32_bf16 v[0:3], a[32:35], a[132:135], v[0:3]\n"
"ds_read_b64_tr_b16 a[64:65], v238 offset:0\n"
"s_add_u32 m0, s64, 131072\n"
"v_mfma_f32_16x16x32_bf16 v[4:7], a[4:7], a[128:131], 0\n"
"buffer_load_dwordx4 v244, s[48:51], s61 offen offset:0 lds\n"
"v_mfma_f32_16x16x32_bf16 v[4:7], a[36:39], a[132:135], v[4:7]\n"
"ds_read_b64_tr_b16 a[66:67], v238 offset:2048\n"
"v_mfma_f32_16x16x32_bf16 v[8:11], a[8:11], a[128:131], 0\n"
"v_mfma_f32_16x16x32_bf16 v[8:11], a[40:43], a[132:135], v[8:11]\n"
"ds_read_b64_tr_b16 a[68:69], v238 offset:8\n"
"v_mfma_f32_16x16x32_bf16 v[12:15], a[12:15], a[128:131], 0\n"
"buffer_load_dwordx4 v245, s[48:51], s61 offen offset:1024 lds\n"
"v_mfma_f32_16x16x32_bf16 v[12:15], a[44:47], a[132:135], v[12:15]\n"
"ds_read_b64_tr_b16 a[70:71], v238 offset:2056\n"
"v_mfma_f32_16x16x32_bf16 v[16:19], a[16:19], a[128:131], 0\n"
"v_mfma_f32_16x16x32_bf16 v[16:19], a[48:51], a[132:135], v[16:19]\n"
"ds_read_b64_tr_b16 a[72:73], v239 offset:0\n"
"v_mfma_f32_16x16x32_bf16 v[20:23], a[20:23], a[128:131], 0\n"
"buffer_load_dwordx4 v246, s[48:51], s61 offen offset:2048 lds\n"
"v_mfma_f32_16x16x32_bf16 v[20:23], a[52:55], a[132:135], v[20:23]\n"
"ds_read_b64_tr_b16 a[74:75], v239 offset:2048\n"
"v_mfma_f32_16x16x32_bf16 v[24:27], a[24:27], a[128:131], 0\n"
"v_mfma_f32_16x16x32_bf16 v[24:27], a[56:59], a[132:135], v[24:27]\n"
"ds_read_b64_tr_b16 a[76:77], v239 offset:8\n"
"v_mfma_f32_16x16x32_bf16 v[28:31], a[28:31], a[128:131], 0\n"
"buffer_load_dwordx4 v247, s[48:51], s61 offen offset:3072 lds\n"
"v_mfma_f32_16x16x32_bf16 v[28:31], a[60:63], a[132:135], v[28:31]\n"
"ds_read_b64_tr_b16 a[78:79], v239 offset:2056\n"
"v_mfma_f32_16x16x32_bf16 v[32:35], a[0:3], a[136:139], 0\n"
"v_mfma_f32_16x16x32_bf16 v[32:35], a[32:35], a[140:143], v[32:35]\n"
"ds_read_b64_tr_b16 a[80:81], v240 offset:0\n"
"s_add_u32 m0, s64, 135168\n"
"v_mfma_f32_16x16x32_bf16 v[36:39], a[4:7], a[136:139], 0\n"
"buffer_load_dwordx4 v244, s[48:51], s62 offen offset:0 lds\n"
"v_mfma_f32_16x16x32_bf16 v[36:39], a[36:39], a[140:143], v[36:39]\n"
"ds_read_b64_tr_b16 a[82:83], v240 offset:2048\n"
"v_mfma_f32_16x16x32_bf16 v[40:43], a[8:11], a[136:139], 0\n"
"v_mfma_f32_16x16x32_bf16 v[40:43], a[40:43], a[140:143], v[40:43]\n"
"ds_read_b64_tr_b16 a[84:85], v240 offset:8\n"
"v_mfma_f32_16x16x32_bf16 v[44:47], a[12:15], a[136:139], 0\n"
"buffer_load_dwordx4 v245, s[48:51], s62 offen offset:1024 lds\n"
"v_mfma_f32_16x16x32_bf16 v[44:47], a[44:47], a[140:143], v[44:47]\n"
"ds_read_b64_tr_b16 a[86:87], v240 offset:2056\n"
"v_mfma_f32_16x16x32_bf16 v[48:51], a[16:19], a[136:139], 0\n"
"v_mfma_f32_16x16x32_bf16 v[48:51], a[48:51], a[140:143], v[48:51]\n"
"ds_read_b64_tr_b16 a[88:89], v241 offset:0\n"
"v_mfma_f32_16x16x32_bf16 v[52:55], a[20:23], a[136:139], 0\n"
"buffer_load_dwordx4 v246, s[48:51], s62 offen offset:2048 lds\n"
"v_mfma_f32_16x16x32_bf16 v[52:55], a[52:55], a[140:143], v[52:55]\n"
"ds_read_b64_tr_b16 a[90:91], v241 offset:2048\n"
"v_mfma_f32_16x16x32_bf16 v[56:59], a[24:27], a[136:139], 0\n"
"v_mfma_f32_16x16x32_bf16 v[56:59], a[56:59], a[140:143], v[56:59]\n"
"ds_read_b64_tr_b16 a[92:93], v241 offset:8\n"
"v_mfma_f32_16x16x32_bf16 v[60:63], a[28:31], a[136:139], 0\n"
"buffer_load_dwordx4 v247, s[48:51], s62 offen offset:3072 lds\n"
"v_mfma_f32_16x16x32_bf16 v[60:63], a[60:63], a[140:143], v[60:63]\n"
"ds_read_b64_tr_b16 a[94:95], v241 offset:2056\n"
"v_mfma_f32_16x16x32_bf16 v[64:67], a[0:3], a[144:147], 0\n"
"s_add_u32 s61, s61, 524288\n"
"s_add_u32 s62, s62, 524288\n"
"v_mfma_f32_16x16x32_bf16 v[64:67], a[32:35], a[148:151], v[64:67]\n"
"ds_read_b64_tr_b16 a[96:97], v238 offset:16384\n"
"s_add_u32 m0, s63, 106496\n"
"v_mfma_f32_16x16x32_bf16 v[68:71], a[4:7], a[144:147], 0\n"
"buffer_load_dwordx4 v248, s[44:47], s59 offen offset:0 lds\n"
"v_mfma_f32_16x16x32_bf16 v[68:71], a[36:39], a[148:151], v[68:71]\n"
"ds_read_b64_tr_b16 a[98:99], v238 offset:18432\n"
"v_mfma_f32_16x16x32_bf16 v[72:75], a[8:11], a[144:147], 0\n"
"v_mfma_f32_16x16x32_bf16 v[72:75], a[40:43], a[148:151], v[72:75]\n"
"ds_read_b64_tr_b16 a[100:101], v238 offset:16392\n"
"v_mfma_f32_16x16x32_bf16 v[76:79], a[12:15], a[144:147], 0\n"
"buffer_load_dwordx4 v249, s[44:47], s59 offen offset:1024 lds\n"
"v_mfma_f32_16x16x32_bf16 v[76:79], a[44:47], a[148:151], v[76:79]\n"
"ds_read_b64_tr_b16 a[102:103], v238 offset:18440\n"
"v_mfma_f32_16x16x32_bf16 v[80:83], a[16:19], a[144:147], 0\n"
"v_mfma_f32_16x16x32_bf16 v[80:83], a[48:51], a[148:151], v[80:83]\n"
"ds_read_b64_tr_b16 a[104:105], v239 offset:16384\n"
"v_mfma_f32_16x16x32_bf16 v[84:87], a[20:23], a[144:147], 0\n"
"buffer_load_dwordx4 v250, s[44:47], s59 offen offset:2048 lds\n"
"v_mfma_f32_16x16x32_bf16 v[84:87], a[52:55], a[148:151], v[84:87]\n"
"ds_read_b64_tr_b16 a[106:107], v239 offset:18432\n"
"v_mfma_f32_16x16x32_bf16 v[88:91], a[24:27], a[144:147], 0\n"
"v_mfma_f32_16x16x32_bf16 v[88:91], a[56:59], a[148:151], v[88:91]\n"
"ds_read_b64_tr_b16 a[108:109], v239 offset:16392\n"
"v_mfma_f32_16x16x32_bf16 v[92:95], a[28:31], a[144:147], 0\n"
"buffer_load_dwordx4 v251, s[44:47], s59 offen offset:3072 lds\n"
"v_mfma_f32_16x16x32_bf16 v[92:95], a[60:63], a[148:151], v[92:95]\n"
"ds_read_b64_tr_b16 a[110:111], v239 offset:18440\n"
"v_mfma_f32_16x16x32_bf16 v[96:99], a[0:3], a[152:155], 0\n"
"v_mfma_f32_16x16x32_bf16 v[96:99], a[32:35], a[156:159], v[96:99]\n"
"ds_read_b64_tr_b16 a[112:113], v240 offset:16384\n"
"s_add_u32 m0, s63, 110592\n"
"v_mfma_f32_16x16x32_bf16 v[100:103], a[4:7], a[152:155], 0\n"
"buffer_load_dwordx4 v248, s[44:47], s60 offen offset:0 lds\n"
"v_mfma_f32_16x16x32_bf16 v[100:103], a[36:39], a[156:159], v[100:103]\n"
"ds_read_b64_tr_b16 a[114:115], v240 offset:18432\n"
"v_mfma_f32_16x16x32_bf16 v[104:107], a[8:11], a[152:155], 0\n"
"v_mfma_f32_16x16x32_bf16 v[104:107], a[40:43], a[156:159], v[104:107]\n"
"ds_read_b64_tr_b16 a[116:117], v240 offset:16392\n"
"v_mfma_f32_16x16x32_bf16 v[108:111], a[12:15], a[152:155], 0\n"
"buffer_load_dwordx4 v249, s[44:47], s60 offen offset:1024 lds\n"
"v_mfma_f32_16x16x32_bf16 v[108:111], a[44:47], a[156:159], v[108:111]\n"
"ds_read_b64_tr_b16 a[118:119], v240 offset:18440\n"
"s_add_u32 s59, s59, 128\n"
"s_add_u32 s60, s60, 128\n"
"v_mfma_f32_16x16x32_bf16 v[112:115], a[16:19], a[152:155], 0\n"
"v_mfma_f32_16x16x32_bf16 v[112:115], a[48:51], a[156:159], v[112:115]\n"
"ds_read_b64_tr_b16 a[120:121], v241 offset:16384\n"
"v_mfma_f32_16x16x32_bf16 v[116:119], a[20:23], a[152:155], 0\n"
"s_cmp_lg_u32 s66, 0\n"
"s_cbranch_scc1 .Lf_c0\n"
"s_add_u32 s73, s76, 2097152\n"
"s_bfm_b64 exec, 16, 0\n"
"buffer_load_dword v255, v252, s[48:51], s73 offen\n"
"s_mov_b64 exec, -1\n"
"s_add_u32 s73, s77, 512\n"
"s_bfm_b64 exec, 24, 0\n"
"buffer_load_dword v255, v253, s[44:47], s73 offen\n"
"s_mov_b64 exec, -1\n"
".Lf_c0:\n"
"v_mfma_f32_16x16x32_bf16 v[116:119], a[52:55], a[156:159], v[116:119]\n"
"ds_read_b64_tr_b16 a[122:123], v241 offset:18432\n"
"v_mfma_f32_16x16x32_bf16 v[120:123], a[24:27], a[152:155], 0\n"
"v_mfma_f32_16x16x32_bf16 v[120:123], a[56:59], a[156:159], v[120:123]\n"
"ds_read_b64_tr_b16 a[124:125], v241 offset:16392\n"
"v_mfma_f32_16x16x32_bf16 v[124:127], a[28:31], a[152:155], 0\n"
"v_mfma_f32_16x16x32_bf16 v[124:127], a[60:63], a[156:159], v[124:127]\n"
"ds_read_b64_tr_b16 a[126:127], v241 offset:18440\n"
"s_cmp_eq_u32 s66, 0\n"
"s_cbranch_scc1 .Lf_ps12\n"
"s_add_u32 s75, s67, 12288\n"
"buffer_store_dwordx4 a[192:195], v252, s[52:55], s75 offen offset:0 sc1\n"
".Lf_ps12:\n"
"v_xor_b32 v238, 0x28000, v238\n"
"v_xor_b32 v239, 0x28000, v239\n"
"v_xor_b32 v240, 0x28000, v240\n"
"v_xor_b32 v241, 0x28000, v241\n"
"v_mfma_f32_16x16x32_bf16 v[128:131], a[0:3], a[160:163], 0\n"
"v_mfma_f32_16x16x32_bf16 v[128:131], a[32:35], a[164:167], v[128:131]\n"
"ds_read_b128 a[176:179], v242 offset:0\n"
"v_xor_b32 v242, 64, v242\n"
"v_mfma_f32_16x16x32_bf16 v[132:135], a[4:7], a[160:163], 0\n"
"v_mfma_f32_16x16x32_bf16 v[132:135], a[36:39], a[164:167], v[132:135]\n"
"ds_read_b128 a[180:183], v242 offset:0\n"
"v_xor_b32 v242, 64, v242\n"
"v_mfma_f32_16x16x32_bf16 v[136:139], a[8:11], a[160:163], 0\n"
"v_mfma_f32_16x16x32_bf16 v[136:139], a[40:43], a[164:167], v[136:139]\n"
"ds_read_b128 a[128:131], v242 offset:2048\n"
"s_cmp_eq_u32 s66, 0\n"
"s_cbranch_scc1 .Lf_ps13\n"
"s_add_u32 s75, s67, 12288\n"
"buffer_store_dwordx4 a[196:199], v252, s[52:55], s75 offen offset:1024 sc1\n"
".Lf_ps13:\n"
"v_xor_b32 v242, 64, v242\n"
"v_mfma_f32_16x16x32_bf16 v[140:143], a[12:15], a[160:163], 0\n"
"v_mfma_f32_16x16x32_bf16 v[140:143], a[44:47], a[164:167], v[140:143]\n"
"ds_read_b128 a[132:135], v242 offset:2048\n"
"v_xor_b32 v242, 64, v242\n"
"v_mfma_f32_16x16x32_bf16 v[144:147], a[16:19], a[160:163], 0\n"
"v_mfma_f32_16x16x32_bf16 v[144:147], a[48:51], a[164:167], v[144:147]\n"
"ds_read_b128 a[136:139], v242 offset:4096\n"
"v_xor_b32 v242, 64, v242\n"
"v_mfma_f32_16x16x32_bf16 v[148:151], a[20:23], a[160:163], 0\n"
"v_mfma_f32_16x16x32_bf16 v[148:151], a[52:55], a[164:167], v[148:151]\n"
"ds_read_b128 a[140:143], v242 offset:4096\n"
"s_cmp_eq_u32 s66, 0\n"
"s_cbranch_scc1 .Lf_ps14\n"
"s_add_u32 s75, s67, 12288\n"
"buffer_store_dwordx4 a[200:203], v252, s[52:55], s75 offen offset:2048 sc1\n"
".Lf_ps14:\n"
"v_xor_b32 v242, 64, v242\n"
"v_mfma_f32_16x16x32_bf16 v[152:155], a[24:27], a[160:163], 0\n"
"v_mfma_f32_16x16x32_bf16 v[152:155], a[56:59], a[164:167], v[152:155]\n"
"ds_read_b128 a[144:147], v242 offset:6144\n"
"v_xor_b32 v242, 64, v242\n"
"v_mfma_f32_16x16x32_bf16 v[156:159], a[28:31], a[160:163], 0\n"
"v_mfma_f32_16x16x32_bf16 v[156:159], a[60:63], a[164:167], v[156:159]\n"
"ds_read_b128 a[148:151], v242 offset:6144\n"
"v_xor_b32 v242, 64, v242\n"
"v_mfma_f32_16x16x32_bf16 v[160:163], a[0:3], a[168:171], 0\n"
"v_mfma_f32_16x16x32_bf16 v[160:163], a[32:35], a[172:175], v[160:163]\n"
"ds_read_b128 a[152:155], v242 offset:8192\n"
"v_xor_b32 v242, 64, v242\n"
"v_mfma_f32_16x16x32_bf16 v[164:167], a[4:7], a[168:171], 0\n"
"v_mfma_f32_16x16x32_bf16 v[164:167], a[36:39], a[172:175], v[164:167]\n"
"ds_read_b128 a[156:159], v242 offset:8192\n"
"v_xor_b32 v242, 64, v242\n"
"v_mfma_f32_16x16x32_bf16 v[168:171], a[8:11], a[168:171], 0\n"
"v_mfma_f32_16x16x32_bf16 v[168:171], a[40:43], a[172:175], v[168:171]\n"
"ds_read_b128 a[160:163], v242 offset:10240\n"
"v_xor_b32 v242, 64, v242\n"
"v_mfma_f32_16x16x32_bf16 v[172:175], a[12:15], a[168:171], 0\n"
"v_mfma_f32_16x16x32_bf16 v[172:175], a[44:47], a[172:175], v[172:175]\n"
"ds_read_b128 a[164:167], v242 offset:10240\n"
"v_xor_b32 v242, 64, v242\n"
"v_mfma_f32_16x16x32_bf16 v[176:179], a[16:19], a[168:171], 0\n"
"v_mfma_f32_16x16x32_bf16 v[176:179], a[48:51], a[172:175], v[176:179]\n"
"v_mfma_f32_16x16x32_bf16 v[180:183], a[20:23], a[168:171], 0\n"
"v_mfma_f32_16x16x32_bf16 v[180:183], a[52:55], a[172:175], v[180:183]\n"
"v_mfma_f32_16x16x32_bf16 v[184:187], a[24:27], a[168:171], 0\n"
"v_mfma_f32_16x16x32_bf16 v[184:187], a[56:59], a[172:175], v[184:187]\n"
"v_mfma_f32_16x16x32_bf16 v[188:191], a[28:31], a[168:171], 0\n"
"v_mfma_f32_16x16x32_bf16 v[188:191], a[60:63], a[172:175], v[188:191]\n"
"s_cmp_lg_u32 s66, 0\n"
"s_cbranch_scc1 .Lf_nc1\n"
"s_waitcnt vmcnt(2) lgkmcnt(0)\n"
".Lf_nc1:\n"
"s_cmp_eq_u32 s66, 0\n"
"s_cbranch_scc1 .Lf_nw1\n"
"s_waitcnt vmcnt(3) lgkmcnt(0)\n"
".Lf_nw1:\n"
"s_barrier\n"
"v_mfma_f32_16x16x32_bf16 v[0:3], a[64:67], a[176:179], v[0:3]\n"
"v_mfma_f32_16x16x32_bf16 v[0:3], a[96:99], a[180:183], v[0:3]\n"
"ds_read_b64_tr_b16 a[0:1], v238 offset:0\n"
"s_add_u32 m0, s64, 32768\n"
"v_mfma_f32_16x16x32_bf16 v[4:7], a[68:71], a[176:179], v[4:7]\n"
"buffer_load_dwordx4 v244, s[48:51], s61 offen offset:0 lds\n"
"v_mfma_f32_16x16x32_bf16 v[4:7], a[100:103], a[180:183], v[4:7]\n"
"ds_read_b64_tr_b16 a[2:3], v238 offset:2048\n"
"v_mfma_f32_16x16x32_bf16 v[8:11], a[72:75], a[176:179], v[8:11]\n"
"v_mfma_f32_16x16x32_bf16 v[8:11], a[104:107], a[180:183], v[8:11]\n"
"ds_read_b64_tr_b16 a[4:5], v238 offset:8\n"
"v_mfma_f32_16x16x32_bf16 v[12:15], a[76:79], a[176:179], v[12:15]\n"
"buffer_load_dwordx4 v245, s[48:51], s61 offen offset:1024 lds\n"
"v_mfma_f32_16x16x32_bf16 v[12:15], a[108:111], a[180:183], v[12:15]\n"
"ds_read_b64_tr_b16 a[6:7], v238 offset:2056\n"
"v_mfma_f32_16x16x32_bf16 v[16:19], a[80:83], a[176:179], v[16:19]\n"
"v_mfma_f32_16x16x32_bf16 v[16:19], a[112:115], a[180:183], v[16:19]\n"
"ds_read_b64_tr_b16 a[8:9], v239 offset:0\n"
"v_mfma_f32_16x16x32_bf16 v[20:23], a[84:87], a[176:179], v[20:23]\n"
"buffer_load_dwordx4 v246, s[48:51], s61 offen offset:2048 lds\n"
"v_mfma_f32_16x16x32_bf16 v[20:23], a[116:119], a[180:183], v[20:23]\n"
"ds_read_b64_tr_b16 a[10:11], v239 offset:2048\n"
"v_mfma_f32_16x16x32_bf16 v[24:27], a[88:91], a[176:179], v[24:27]\n"
"v_mfma_f32_16x16x32_bf16 v[24:27], a[120:123], a[180:183], v[24:27]\n"
"ds_read_b64_tr_b16 a[12:13], v239 offset:8\n"
"v_mfma_f32_16x16x32_bf16 v[28:31], a[92:95], a[176:179], v[28:31]\n"
"buffer_load_dwordx4 v247, s[48:51], s61 offen offset:3072 lds\n"
"v_mfma_f32_16x16x32_bf16 v[28:31], a[124:127], a[180:183], v[28:31]\n"
"ds_read_b64_tr_b16 a[14:15], v239 offset:2056\n"
"v_mfma_f32_16x16x32_bf16 v[32:35], a[64:67], a[128:131], v[32:35]\n"
"v_mfma_f32_16x16x32_bf16 v[32:35], a[96:99], a[132:135], v[32:35]\n"
"ds_read_b64_tr_b16 a[16:17], v240 offset:0\n"
"s_add_u32 m0, s64, 36864\n"
"v_mfma_f32_16x16x32_bf16 v[36:39], a[68:71], a[128:131], v[36:39]\n"
"buffer_load_dwordx4 v244, s[48:51], s62 offen offset:0 lds\n"
"v_mfma_f32_16x16x32_bf16 v[36:39], a[100:103], a[132:135], v[36:39]\n"
"ds_read_b64_tr_b16 a[18:19], v240 offset:2048\n"
"v_mfma_f32_16x16x32_bf16 v[40:43], a[72:75], a[128:131], v[40:43]\n"
"v_mfma_f32_16x16x32_bf16 v[40:43], a[104:107], a[132:135], v[40:43]\n"
"ds_read_b64_tr_b16 a[20:21], v240 offset:8\n"
"v_mfma_f32_16x16x32_bf16 v[44:47], a[76:79], a[128:131], v[44:47]\n"
"buffer_load_dwordx4 v245, s[48:51], s62 offen offset:1024 lds\n"
"v_mfma_f32_16x16x32_bf16 v[44:47], a[108:111], a[132:135], v[44:47]\n"
"ds_read_b64_tr_b16 a[22:23], v240 offset:2056\n"
"v_mfma_f32_16x16x32_bf16 v[48:51], a[80:83], a[128:131], v[48:51]\n"
"v_mfma_f32_16x16x32_bf16 v[48:51], a[112:115], a[132:135], v[48:51]\n"
"ds_read_b64_tr_b16 a[24:25], v241 offset:0\n"
"v_mfma_f32_16x16x32_bf16 v[52:55], a[84:87], a[128:131], v[52:55]\n"
"buffer_load_dwordx4 v246, s[48:51], s62 offen offset:2048 lds\n"
"v_mfma_f32_16x16x32_bf16 v[52:55], a[116:119], a[132:135], v[52:55]\n"
"ds_read_b64_tr_b16 a[26:27], v241 offset:2048\n"
"v_mfma_f32_16x16x32_bf16 v[56:59], a[88:91], a[128:131], v[56:59]\n"
"v_mfma_f32_16x16x32_bf16 v[56:59], a[120:123], a[132:135], v[56:59]\n"
"ds_read_b64_tr_b16 a[28:29], v241 offset:8\n"
"v_mfma_f32_16x16x32_bf16 v[60:63], a[92:95], a[128:131], v[60:63]\n"
"buffer_load_dwordx4 v247, s[48:51], s62 offen offset:3072 lds\n"
"v_mfma_f32_16x16x32_bf16 v[60:63], a[124:127], a[132:135], v[60:63]\n"
"ds_read_b64_tr_b16 a[30:31], v241 offset:2056\n"
"v_mfma_f32_16x16x32_bf16 v[64:67], a[64:67], a[136:139], v[64:67]\n"
"s_add_u32 s61, s61, 524288\n"
"s_add_u32 s62, s62, 524288\n"
"v_mfma_f32_16x16x32_bf16 v[64:67], a[96:99], a[140:143], v[64:67]\n"
"ds_read_b64_tr_b16 a[32:33], v238 offset:16384\n"
"s_add_u32 m0, s63, 81920\n"
"v_mfma_f32_16x16x32_bf16 v[68:71], a[68:71], a[136:139], v[68:71]\n"
"buffer_load_dwordx4 v248, s[44:47], s59 offen offset:0 lds\n"
"v_mfma_f32_16x16x32_bf16 v[68:71], a[100:103], a[140:143], v[68:71]\n"
"ds_read_b64_tr_b16 a[34:35], v238 offset:18432\n"
"v_mfma_f32_16x16x32_bf16 v[72:75], a[72:75], a[136:139], v[72:75]\n"
"v_mfma_f32_16x16x32_bf16 v[72:75], a[104:107], a[140:143], v[72:75]\n"
"ds_read_b64_tr_b16 a[36:37], v238 offset:16392\n"
"v_mfma_f32_16x16x32_bf16 v[76:79], a[76:79], a[136:139], v[76:79]\n"
"buffer_load_dwordx4 v249, s[44:47], s59 offen offset:1024 lds\n"
"v_mfma_f32_16x16x32_bf16 v[76:79], a[108:111], a[140:143], v[76:79]\n"
"ds_read_b64_tr_b16 a[38:39], v238 offset:18440\n"
"v_mfma_f32_16x16x32_bf16 v[80:83], a[80:83], a[136:139], v[80:83]\n"
"v_mfma_f32_16x16x32_bf16 v[80:83], a[112:115], a[140:143], v[80:83]\n"
"ds_read_b64_tr_b16 a[40:41], v239 offset:16384\n"
"v_mfma_f32_16x16x32_bf16 v[84:87], a[84:87], a[136:139], v[84:87]\n"
"buffer_load_dwordx4 v250, s[44:47], s59 offen offset:2048 lds\n"
"v_mfma_f32_16x16x32_bf16 v[84:87], a[116:119], a[140:143], v[84:87]\n"
"ds_read_b64_tr_b16 a[42:43], v239 offset:18432\n"
"v_mfma_f32_16x16x32_bf16 v[88:91], a[88:91], a[136:139], v[88:91]\n"
"v_mfma_f32_16x16x32_bf16 v[88:91], a[120:123], a[140:143], v[88:91]\n"
"ds_read_b64_tr_b16 a[44:45], v239 offset:16392\n"
"v_mfma_f32_16x16x32_bf16 v[92:95], a[92:95], a[136:139], v[92:95]\n"
"buffer_load_dwordx4 v251, s[44:47], s59 offen offset:3072 lds\n"
"v_mfma_f32_16x16x32_bf16 v[92:95], a[124:127], a[140:143], v[92:95]\n"
"ds_read_b64_tr_b16 a[46:47], v239 offset:18440\n"
"v_mfma_f32_16x16x32_bf16 v[96:99], a[64:67], a[144:147], v[96:99]\n"
"v_mfma_f32_16x16x32_bf16 v[96:99], a[96:99], a[148:151], v[96:99]\n"
"ds_read_b64_tr_b16 a[48:49], v240 offset:16384\n"
"s_add_u32 m0, s63, 86016\n"
"v_mfma_f32_16x16x32_bf16 v[100:103], a[68:71], a[144:147], v[100:103]\n"
"buffer_load_dwordx4 v248, s[44:47], s60 offen offset:0 lds\n"
"v_mfma_f32_16x16x32_bf16 v[100:103], a[100:103], a[148:151], v[100:103]\n"
"ds_read_b64_tr_b16 a[50:51], v240 offset:18432\n"
"v_mfma_f32_16x16x32_bf16 v[104:107], a[72:75], a[144:147], v[104:107]\n"
"v_mfma_f32_16x16x32_bf16 v[104:107], a[104:107], a[148:151], v[104:107]\n"
"ds_read_b64_tr_b16 a[52:53], v240 offset:16392\n"
"v_mfma_f32_16x16x32_bf16 v[108:111], a[76:79], a[144:147], v[108:111]\n"
"buffer_load_dwordx4 v249, s[44:47], s60 offen offset:1024 lds\n"
"v_mfma_f32_16x16x32_bf16 v[108:111], a[108:111], a[148:151], v[108:111]\n"
"ds_read_b64_tr_b16 a[54:55], v240 offset:18440\n"
"s_add_u32 s59, s59, 128\n"
"s_add_u32 s60, s60, 128\n"
"v_mfma_f32_16x16x32_bf16 v[112:115], a[80:83], a[144:147], v[112:115]\n"
"v_mfma_f32_16x16x32_bf16 v[112:115], a[112:115], a[148:151], v[112:115]\n"
"ds_read_b64_tr_b16 a[56:57], v241 offset:16384\n"
"v_mfma_f32_16x16x32_bf16 v[116:119], a[84:87], a[144:147], v[116:119]\n"
"s_cmp_lg_u32 s66, 0\n"
"s_cbranch_scc1 .Lf_c1\n"
"s_add_u32 s73, s76, 3145728\n"
"s_bfm_b64 exec, 24, 0\n"
"buffer_load_dword v255, v252, s[48:51], s73 offen\n"
"s_mov_b64 exec, -1\n"
"s_add_u32 s73, s77, 768\n"
"s_bfm_b64 exec, 36, 0\n"
"buffer_load_dword v255, v253, s[44:47], s73 offen\n"
"s_mov_b64 exec, -1\n"
".Lf_c1:\n"
"v_mfma_f32_16x16x32_bf16 v[116:119], a[116:119], a[148:151], v[116:119]\n"
"ds_read_b64_tr_b16 a[58:59], v241 offset:18432\n"
"v_mfma_f32_16x16x32_bf16 v[120:123], a[88:91], a[144:147], v[120:123]\n"
"v_mfma_f32_16x16x32_bf16 v[120:123], a[120:123], a[148:151], v[120:123]\n"
"ds_read_b64_tr_b16 a[60:61], v241 offset:16392\n"
"v_mfma_f32_16x16x32_bf16 v[124:127], a[92:95], a[144:147], v[124:127]\n"
"v_mfma_f32_16x16x32_bf16 v[124:127], a[124:127], a[148:151], v[124:127]\n"
"ds_read_b64_tr_b16 a[62:63], v241 offset:18440\n"
"s_cmp_eq_u32 s66, 0\n"
"s_cbranch_scc1 .Lf_ps15\n"
"s_add_u32 s75, s67, 12288\n"
"buffer_store_dwordx4 a[204:207], v252, s[52:55], s75 offen offset:3072 sc1\n"
".Lf_ps15:\n"
"v_xor_b32 v238, 0x28000, v238\n"
"v_xor_b32 v239, 0x28000, v239\n"
"v_xor_b32 v240, 0x28000, v240\n"
"v_xor_b32 v241, 0x28000, v241\n"
"v_mfma_f32_16x16x32_bf16 v[128:131], a[64:67], a[152:155], v[128:131]\n"
"v_mfma_f32_16x16x32_bf16 v[128:131], a[96:99], a[156:159], v[128:131]\n"
"ds_read_b128 a[168:171], v242 offset:24576\n"
"v_xor_b32 v242, 64, v242\n"
"v_mfma_f32_16x16x32_bf16 v[132:135], a[68:71], a[152:155], v[132:135]\n"
"v_mfma_f32_16x16x32_bf16 v[132:135], a[100:103], a[156:159], v[132:135]\n"
"ds_read_b128 a[172:175], v242 offset:24576\n"
"v_xor_b32 v242, 64, v242\n"
"v_mfma_f32_16x16x32_bf16 v[136:139], a[72:75], a[152:155], v[136:139]\n"
"v_mfma_f32_16x16x32_bf16 v[136:139], a[104:107], a[156:159], v[136:139]\n"
"ds_read_b128 a[176:179], v242 offset:26624\n"
"s_cmp_eq_u32 s66, 0\n"
"s_cbranch_scc1 .Lf_ps16\n"
"s_add_u32 s75, s67, 16384\n"
"buffer_store_dwordx4 a[208:211], v252, s[52:55], s75 offen offset:0 sc1\n"
".Lf_ps16:\n"
"v_xor_b32 v242, 64, v242\n"
"v_mfma_f32_16x16x32_bf16 v[140:143], a[76:79], a[152:155], v[140:143]\n"
"v_mfma_f32_16x16x32_bf16 v[140:143], a[108:111], a[156:159], v[140:143]\n"
"ds_read_b128 a[180:183], v242 offset:26624\n"
"v_xor_b32 v242, 64, v242\n"
"v_mfma_f32_16x16x32_bf16 v[144:147], a[80:83], a[152:155], v[144:147]\n"
"v_mfma_f32_16x16x32_bf16 v[144:147], a[112:115], a[156:159], v[144:147]\n"
"ds_read_b128 a[128:131], v242 offset:28672\n"
"v_xor_b32 v242, 64, v242\n"
"v_mfma_f32_16x16x32_bf16 v[148:151], a[84:87], a[152:155], v[148:151]\n"
"v_mfma_f32_16x16x32_bf16 v[148:151], a[116:119], a[156:159], v[148:151]\n"
"ds_read_b128 a[132:135], v242 offset:28672\n"
"s_cmp_eq_u32 s66, 0\n"
"s_cbranch_scc1 .Lf_ps17\n"
"s_add_u32 s75, s67, 16384\n"
"buffer_store_dwordx4 a[212:215], v252, s[52:55], s75 offen offset:1024 sc1\n"
".Lf_ps17:\n"
"v_xor_b32 v242, 64, v242\n"
"v_mfma_f32_16x16x32_bf16 v[152:155], a[88:91], a[152:155], v[152:155]\n"
"v_mfma_f32_16x16x32_bf16 v[152:155], a[120:123], a[156:159], v[152:155]\n"
"ds_read_b128 a[136:139], v242 offset:30720\n"
"v_xor_b32 v242, 64, v242\n"
"v_mfma_f32_16x16x32_bf16 v[156:159], a[92:95], a[152:155], v[156:159]\n"
"v_mfma_f32_16x16x32_bf16 v[156:159], a[124:127], a[156:159], v[156:159]\n"
"ds_read_b128 a[140:143], v242 offset:30720\n"
"v_xor_b32 v242, 64, v242\n"
"v_mfma_f32_16x16x32_bf16 v[160:163], a[64:67], a[160:163], v[160:163]\n"
"v_mfma_f32_16x16x32_bf16 v[160:163], a[96:99], a[164:167], v[160:163]\n"
"ds_read_b128 a[144:147], v242 offset:32768\n"
"v_xor_b32 v242, 64, v242\n"
"v_mfma_f32_16x16x32_bf16 v[164:167], a[68:71], a[160:163], v[164:167]\n"
"v_mfma_f32_16x16x32_bf16 v[164:167], a[100:103], a[164:167], v[164:167]\n"
"ds_read_b128 a[148:151], v242 offset:32768\n"
"v_xor_b32 v242, 64, v242\n"
"v_mfma_f32_16x16x32_bf16 v[168:171], a[72:75], a[160:163], v[168:171]\n"
"v_mfma_f32_16x16x32_bf16 v[168:171], a[104:107], a[164:167], v[168:171]\n"
"ds_read_b128 a[152:155], v242 offset:34816\n"
"v_xor_b32 v242, 64, v242\n"
"v_mfma_f32_16x16x32_bf16 v[172:175], a[76:79], a[160:163], v[172:175]\n"
"v_mfma_f32_16x16x32_bf16 v[172:175], a[108:111], a[164:167], v[172:175]\n"
"ds_read_b128 a[156:159], v242 offset:34816\n"
"v_xor_b32 v242, 64, v242\n"
"v_mfma_f32_16x16x32_bf16 v[176:179], a[80:83], a[160:163], v[176:179]\n"
"v_mfma_f32_16x16x32_bf16 v[176:179], a[112:115], a[164:167], v[176:179]\n"
"v_mfma_f32_16x16x32_bf16 v[180:183], a[84:87], a[160:163], v[180:183]\n"
"v_mfma_f32_16x16x32_bf16 v[180:183], a[116:119], a[164:167], v[180:183]\n"
"v_mfma_f32_16x16x32_bf16 v[184:187], a[88:91], a[160:163], v[184:187]\n"
"v_mfma_f32_16x16x32_bf16 v[184:187], a[120:123], a[164:167], v[184:187]\n"
"v_mfma_f32_16x16x32_bf16 v[188:191], a[92:95], a[160:163], v[188:191]\n"
"v_mfma_f32_16x16x32_bf16 v[188:191], a[124:127], a[164:167], v[188:191]\n"
"s_cmp_lg_u32 s66, 0\n"
"s_cbranch_scc1 .Lf_nc2\n"
"s_waitcnt vmcnt(2) lgkmcnt(0)\n"
".Lf_nc2:\n"
"s_cmp_eq_u32 s66, 0\n"
"s_cbranch_scc1 .Lf_nw2\n"
"s_waitcnt vmcnt(3) lgkmcnt(0)\n"
".Lf_nw2:\n"
"s_barrier\n"
"v_mfma_f32_16x16x32_bf16 v[0:3], a[0:3], a[168:171], v[0:3]\n"
"v_mfma_f32_16x16x32_bf16 v[0:3], a[32:35], a[172:175], v[0:3]\n"
"ds_read_b64_tr_b16 a[64:65], v238 offset:0\n"
"s_add_u32 m0, s64, 131072\n"
"v_mfma_f32_16x16x32_bf16 v[4:7], a[4:7], a[168:171], v[4:7]\n"
"buffer_load_dwordx4 v244, s[48:51], s61 offen offset:0 lds\n"
"v_mfma_f32_16x16x32_bf16 v[4:7], a[36:39], a[172:175], v[4:7]\n"
"ds_read_b64_tr_b16 a[66:67], v238 offset:2048\n"
"v_mfma_f32_16x16x32_bf16 v[8:11], a[8:11], a[168:171], v[8:11]\n"
"v_mfma_f32_16x16x32_bf16 v[8:11], a[40:43], a[172:175], v[8:11]\n"
"ds_read_b64_tr_b16 a[68:69], v238 offset:8\n"
"v_mfma_f32_16x16x32_bf16 v[12:15], a[12:15], a[168:171], v[12:15]\n"
"buffer_load_dwordx4 v245, s[48:51], s61 offen offset:1024 lds\n"
"v_mfma_f32_16x16x32_bf16 v[12:15], a[44:47], a[172:175], v[12:15]\n"
"ds_read_b64_tr_b16 a[70:71], v238 offset:2056\n"
"v_mfma_f32_16x16x32_bf16 v[16:19], a[16:19], a[168:171], v[16:19]\n"
"v_mfma_f32_16x16x32_bf16 v[16:19], a[48:51], a[172:175], v[16:19]\n"
"ds_read_b64_tr_b16 a[72:73], v239 offset:0\n"
"v_mfma_f32_16x16x32_bf16 v[20:23], a[20:23], a[168:171], v[20:23]\n"
"buffer_load_dwordx4 v246, s[48:51], s61 offen offset:2048 lds\n"
"v_mfma_f32_16x16x32_bf16 v[20:23], a[52:55], a[172:175], v[20:23]\n"
"ds_read_b64_tr_b16 a[74:75], v239 offset:2048\n"
"v_mfma_f32_16x16x32_bf16 v[24:27], a[24:27], a[168:171], v[24:27]\n"
"v_mfma_f32_16x16x32_bf16 v[24:27], a[56:59], a[172:175], v[24:27]\n"
"ds_read_b64_tr_b16 a[76:77], v239 offset:8\n"
"v_mfma_f32_16x16x32_bf16 v[28:31], a[28:31], a[168:171], v[28:31]\n"
"buffer_load_dwordx4 v247, s[48:51], s61 offen offset:3072 lds\n"
"v_mfma_f32_16x16x32_bf16 v[28:31], a[60:63], a[172:175], v[28:31]\n"
"ds_read_b64_tr_b16 a[78:79], v239 offset:2056\n"
"v_mfma_f32_16x16x32_bf16 v[32:35], a[0:3], a[176:179], v[32:35]\n"
"v_mfma_f32_16x16x32_bf16 v[32:35], a[32:35], a[180:183], v[32:35]\n"
"ds_read_b64_tr_b16 a[80:81], v240 offset:0\n"
"s_add_u32 m0, s64, 135168\n"
"v_mfma_f32_16x16x32_bf16 v[36:39], a[4:7], a[176:179], v[36:39]\n"
"buffer_load_dwordx4 v244, s[48:51], s62 offen offset:0 lds\n"
"v_mfma_f32_16x16x32_bf16 v[36:39], a[36:39], a[180:183], v[36:39]\n"
"ds_read_b64_tr_b16 a[82:83], v240 offset:2048\n"
"v_mfma_f32_16x16x32_bf16 v[40:43], a[8:11], a[176:179], v[40:43]\n"
"v_mfma_f32_16x16x32_bf16 v[40:43], a[40:43], a[180:183], v[40:43]\n"
"ds_read_b64_tr_b16 a[84:85], v240 offset:8\n"
"v_mfma_f32_16x16x32_bf16 v[44:47], a[12:15], a[176:179], v[44:47]\n"
"buffer_load_dwordx4 v245, s[48:51], s62 offen offset:1024 lds\n"
"v_mfma_f32_16x16x32_bf16 v[44:47], a[44:47], a[180:183], v[44:47]\n"
"ds_read_b64_tr_b16 a[86:87], v240 offset:2056\n"
"v_mfma_f32_16x16x32_bf16 v[48:51], a[16:19], a[176:179], v[48:51]\n"
"v_mfma_f32_16x16x32_bf16 v[48:51], a[48:51], a[180:183], v[48:51]\n"
"ds_read_b64_tr_b16 a[88:89], v241 offset:0\n"
"v_mfma_f32_16x16x32_bf16 v[52:55], a[20:23], a[176:179], v[52:55]\n"
"buffer_load_dwordx4 v246, s[48:51], s62 offen offset:2048 lds\n"
"v_mfma_f32_16x16x32_bf16 v[52:55], a[52:55], a[180:183], v[52:55]\n"
"ds_read_b64_tr_b16 a[90:91], v241 offset:2048\n"
"v_mfma_f32_16x16x32_bf16 v[56:59], a[24:27], a[176:179], v[56:59]\n"
"v_mfma_f32_16x16x32_bf16 v[56:59], a[56:59], a[180:183], v[56:59]\n"
"ds_read_b64_tr_b16 a[92:93], v241 offset:8\n"
"v_mfma_f32_16x16x32_bf16 v[60:63], a[28:31], a[176:179], v[60:63]\n"
"buffer_load_dwordx4 v247, s[48:51], s62 offen offset:3072 lds\n"
"v_mfma_f32_16x16x32_bf16 v[60:63], a[60:63], a[180:183], v[60:63]\n"
"ds_read_b64_tr_b16 a[94:95], v241 offset:2056\n"
"v_mfma_f32_16x16x32_bf16 v[64:67], a[0:3], a[128:131], v[64:67]\n"
"s_add_u32 s61, s61, 524288\n"
"s_add_u32 s62, s62, 524288\n"
"v_mfma_f32_16x16x32_bf16 v[64:67], a[32:35], a[132:135], v[64:67]\n"
"ds_read_b64_tr_b16 a[96:97], v238 offset:16384\n"
"s_add_u32 m0, s63, 106496\n"
"v_mfma_f32_16x16x32_bf16 v[68:71], a[4:7], a[128:131], v[68:71]\n"
"buffer_load_dwordx4 v248, s[44:47], s59 offen offset:0 lds\n"
"v_mfma_f32_16x16x32_bf16 v[68:71], a[36:39], a[132:135], v[68:71]\n"
"ds_read_b64_tr_b16 a[98:99], v238 offset:18432\n"
"v_mfma_f32_16x16x32_bf16 v[72:75], a[8:11], a[128:131], v[72:75]\n"
"v_mfma_f32_16x16x32_bf16 v[72:75], a[40:43], a[132:135], v[72:75]\n"
"ds_read_b64_tr_b16 a[100:101], v238 offset:16392\n"
"v_mfma_f32_16x16x32_bf16 v[76:79], a[12:15], a[128:131], v[76:79]\n"
"buffer_load_dwordx4 v249, s[44:47], s59 offen offset:1024 lds\n"
"v_mfma_f32_16x16x32_bf16 v[76:79], a[44:47], a[132:135], v[76:79]\n"
"ds_read_b64_tr_b16 a[102:103], v238 offset:18440\n"
"v_mfma_f32_16x16x32_bf16 v[80:83], a[16:19], a[128:131], v[80:83]\n"
"v_mfma_f32_16x16x32_bf16 v[80:83], a[48:51], a[132:135], v[80:83]\n"
"ds_read_b64_tr_b16 a[104:105], v239 offset:16384\n"
"v_mfma_f32_16x16x32_bf16 v[84:87], a[20:23], a[128:131], v[84:87]\n"
"buffer_load_dwordx4 v250, s[44:47], s59 offen offset:2048 lds\n"
"v_mfma_f32_16x16x32_bf16 v[84:87], a[52:55], a[132:135], v[84:87]\n"
"ds_read_b64_tr_b16 a[106:107], v239 offset:18432\n"
"v_mfma_f32_16x16x32_bf16 v[88:91], a[24:27], a[128:131], v[88:91]\n"
"v_mfma_f32_16x16x32_bf16 v[88:91], a[56:59], a[132:135], v[88:91]\n"
"ds_read_b64_tr_b16 a[108:109], v239 offset:16392\n"
"v_mfma_f32_16x16x32_bf16 v[92:95], a[28:31], a[128:131], v[92:95]\n"
"buffer_load_dwordx4 v251, s[44:47], s59 offen offset:3072 lds\n"
"v_mfma_f32_16x16x32_bf16 v[92:95], a[60:63], a[132:135], v[92:95]\n"
"ds_read_b64_tr_b16 a[110:111], v239 offset:18440\n"
"v_mfma_f32_16x16x32_bf16 v[96:99], a[0:3], a[136:139], v[96:99]\n"
"v_mfma_f32_16x16x32_bf16 v[96:99], a[32:35], a[140:143], v[96:99]\n"
"ds_read_b64_tr_b16 a[112:113], v240 offset:16384\n"
"s_add_u32 m0, s63, 110592\n"
"v_mfma_f32_16x16x32_bf16 v[100:103], a[4:7], a[136:139], v[100:103]\n"
"buffer_load_dwordx4 v248, s[44:47], s60 offen offset:0 lds\n"
"v_mfma_f32_16x16x32_bf16 v[100:103], a[36:39], a[140:143], v[100:103]\n"
"ds_read_b64_tr_b16 a[114:115], v240 offset:18432\n"
"v_mfma_f32_16x16x32_bf16 v[104:107], a[8:11], a[136:139], v[104:107]\n"
"v_mfma_f32_16x16x32_bf16 v[104:107], a[40:43], a[140:143], v[104:107]\n"
"ds_read_b64_tr_b16 a[116:117], v240 offset:16392\n"
"v_mfma_f32_16x16x32_bf16 v[108:111], a[12:15], a[136:139], v[108:111]\n"
"buffer_load_dwordx4 v249, s[44:47], s60 offen offset:1024 lds\n"
"v_mfma_f32_16x16x32_bf16 v[108:111], a[44:47], a[140:143], v[108:111]\n"
"ds_read_b64_tr_b16 a[118:119], v240 offset:18440\n"
"s_add_u32 s59, s59, 128\n"
"s_add_u32 s60, s60, 128\n"
"v_mfma_f32_16x16x32_bf16 v[112:115], a[16:19], a[136:139], v[112:115]\n"
"v_mfma_f32_16x16x32_bf16 v[112:115], a[48:51], a[140:143], v[112:115]\n"
"ds_read_b64_tr_b16 a[120:121], v241 offset:16384\n"
"v_mfma_f32_16x16x32_bf16 v[116:119], a[20:23], a[136:139], v[116:119]\n"
"s_cmp_lg_u32 s66, 0\n"
"s_cbranch_scc1 .Lf_c2\n"
".Lf_c2:\n"
"v_mfma_f32_16x16x32_bf16 v[116:119], a[52:55], a[140:143], v[116:119]\n"
"ds_read_b64_tr_b16 a[122:123], v241 offset:18432\n"
"v_mfma_f32_16x16x32_bf16 v[120:123], a[24:27], a[136:139], v[120:123]\n"
"v_mfma_f32_16x16x32_bf16 v[120:123], a[56:59], a[140:143], v[120:123]\n"
"ds_read_b64_tr_b16 a[124:125], v241 offset:16392\n"
"v_mfma_f32_16x16x32_bf16 v[124:127], a[28:31], a[136:139], v[124:127]\n"
"v_mfma_f32_16x16x32_bf16 v[124:127], a[60:63], a[140:143], v[124:127]\n"
"ds_read_b64_tr_b16 a[126:127], v241 offset:18440\n"
"s_cmp_eq_u32 s66, 0\n"
"s_cbranch_scc1 .Lf_ps18\n"
"s_add_u32 s75, s67, 16384\n"
"buffer_store_dwordx4 a[216:219], v252, s[52:55], s75 offen offset:2048 sc1\n"
".Lf_ps18:\n"
"v_xor_b32 v238, 0x28000, v238\n"
"v_xor_b32 v239, 0x28000, v239\n"
"v_xor_b32 v240, 0x28000, v240\n"
"v_xor_b32 v241, 0x28000, v241\n"
"v_mfma_f32_16x16x32_bf16 v[128:131], a[0:3], a[144:147], v[128:131]\n"
"v_mfma_f32_16x16x32_bf16 v[128:131], a[32:35], a[148:151], v[128:131]\n"
"ds_read_b128 a[160:163], v242 offset:0\n"
"v_xor_b32 v242, 64, v242\n"
"v_mfma_f32_16x16x32_bf16 v[132:135], a[4:7], a[144:147], v[132:135]\n"
"v_mfma_f32_16x16x32_bf16 v[132:135], a[36:39], a[148:151], v[132:135]\n"
"ds_read_b128 a[164:167], v242 offset:0\n"
"v_xor_b32 v242, 64, v242\n"
"v_mfma_f32_16x16x32_bf16 v[136:139], a[8:11], a[144:147], v[136:139]\n"
"v_mfma_f32_16x16x32_bf16 v[136:139], a[40:43], a[148:151], v[136:139]\n"
"ds_read_b128 a[168:171], v242 offset:2048\n"
"s_cmp_eq_u32 s66, 0\n"
"s_cbranch_scc1 .Lf_ps19\n"
"s_add_u32 s75, s67, 16384\n"
"buffer_store_dwordx4 a[220:223], v252, s[52:55], s75 offen offset:3072 sc1\n"
".Lf_ps19:\n"
"v_xor_b32 v242, 64, v242\n"
"v_mfma_f32_16x16x32_bf16 v[140:143], a[12:15], a[144:147], v[140:143]\n"
"v_mfma_f32_16x16x32_bf16 v[140:143], a[44:47], a[148:151], v[140:143]\n"
"ds_read_b128 a[172:175], v242 offset:2048\n"
"v_xor_b32 v242, 64, v242\n"
"v_mfma_f32_16x16x32_bf16 v[144:147], a[16:19], a[144:147], v[144:147]\n"
"v_mfma_f32_16x16x32_bf16 v[144:147], a[48:51], a[148:151], v[144:147]\n"
"ds_read_b128 a[176:179], v242 offset:4096\n"
"v_xor_b32 v242, 64, v242\n"
"v_mfma_f32_16x16x32_bf16 v[148:151], a[20:23], a[144:147], v[148:151]\n"
"v_mfma_f32_16x16x32_bf16 v[148:151], a[52:55], a[148:151], v[148:151]\n"
"ds_read_b128 a[180:183], v242 offset:4096\n"
"s_cmp_eq_u32 s66, 0\n"
"s_cbranch_scc1 .Lf_ps20\n"
"s_add_u32 s75, s67, 20480\n"
"buffer_store_dwordx4 a[224:227], v252, s[52:55], s75 offen offset:0 sc1\n"
".Lf_ps20:\n"
"v_xor_b32 v242, 64, v242\n"
"v_mfma_f32_16x16x32_bf16 v[152:155], a[24:27], a[144:147], v[152:155]\n"
"v_mfma_f32_16x16x32_bf16 v[152:155], a[56:59], a[148:151], v[152:155]\n"
"ds_read_b128 a[128:131], v242 offset:6144\n"
"v_xor_b32 v242, 64, v242\n"
"v_mfma_f32_16x16x32_bf16 v[156:159], a[28:31], a[144:147], v[156:159]\n"
"v_mfma_f32_16x16x32_bf16 v[156:159], a[60:63], a[148:151], v[156:159]\n"
"ds_read_b128 a[132:135], v242 offset:6144\n"
"v_xor_b32 v242, 64, v242\n"
"v_mfma_f32_16x16x32_bf16 v[160:163], a[0:3], a[152:155], v[160:163]\n"
"v_mfma_f32_16x16x32_bf16 v[160:163], a[32:35], a[156:159], v[160:163]\n"
"ds_read_b128 a[136:139], v242 offset:8192\n"
"v_xor_b32 v242, 64, v242\n"
"v_mfma_f32_16x16x32_bf16 v[164:167], a[4:7], a[152:155], v[164:167]\n"
"v_mfma_f32_16x16x32_bf16 v[164:167], a[36:39], a[156:159], v[164:167]\n"
"ds_read_b128 a[140:143], v242 offset:8192\n"
"v_xor_b32 v242, 64, v242\n"
"v_mfma_f32_16x16x32_bf16 v[168:171], a[8:11], a[152:155], v[168:171]\n"
"v_mfma_f32_16x16x32_bf16 v[168:171], a[40:43], a[156:159], v[168:171]\n"
"ds_read_b128 a[144:147], v242 offset:10240\n"
"v_xor_b32 v242, 64, v242\n"
"v_mfma_f32_16x16x32_bf16 v[172:175], a[12:15], a[152:155], v[172:175]\n"
"v_mfma_f32_16x16x32_bf16 v[172:175], a[44:47], a[156:159], v[172:175]\n"
"ds_read_b128 a[148:151], v242 offset:10240\n"
"v_xor_b32 v242, 64, v242\n"
"v_mfma_f32_16x16x32_bf16 v[176:179], a[16:19], a[152:155], v[176:179]\n"
"v_mfma_f32_16x16x32_bf16 v[176:179], a[48:51], a[156:159], v[176:179]\n"
"v_mfma_f32_16x16x32_bf16 v[180:183], a[20:23], a[152:155], v[180:183]\n"
"v_mfma_f32_16x16x32_bf16 v[180:183], a[52:55], a[156:159], v[180:183]\n"
"v_mfma_f32_16x16x32_bf16 v[184:187], a[24:27], a[152:155], v[184:187]\n"
"v_mfma_f32_16x16x32_bf16 v[184:187], a[56:59], a[156:159], v[184:187]\n"
"v_mfma_f32_16x16x32_bf16 v[188:191], a[28:31], a[152:155], v[188:191]\n"
"v_mfma_f32_16x16x32_bf16 v[188:191], a[60:63], a[156:159], v[188:191]\n"
"s_cmp_lg_u32 s66, 0\n"
"s_cbranch_scc1 .Lf_nc3\n"
"s_waitcnt vmcnt(0) lgkmcnt(0)\n"
".Lf_nc3:\n"
"s_cmp_eq_u32 s66, 0\n"
"s_cbranch_scc1 .Lf_nw3\n"
"s_waitcnt vmcnt(3) lgkmcnt(0)\n"
".Lf_nw3:\n"
"s_barrier\n"
"v_mfma_f32_16x16x32_bf16 v[0:3], a[64:67], a[160:163], v[0:3]\n"
"v_mfma_f32_16x16x32_bf16 v[0:3], a[96:99], a[164:167], v[0:3]\n"
"ds_read_b64_tr_b16 a[0:1], v238 offset:0\n"
"s_add_u32 m0, s64, 32768\n"
"v_mfma_f32_16x16x32_bf16 v[4:7], a[68:71], a[160:163], v[4:7]\n"
"buffer_load_dwordx4 v244, s[48:51], s61 offen offset:0 lds\n"
"v_mfma_f32_16x16x32_bf16 v[4:7], a[100:103], a[164:167], v[4:7]\n"
"ds_read_b64_tr_b16 a[2:3], v238 offset:2048\n"
"v_mfma_f32_16x16x32_bf16 v[8:11], a[72:75], a[160:163], v[8:11]\n"
"v_mfma_f32_16x16x32_bf16 v[8:11], a[104:107], a[164:167], v[8:11]\n"
"ds_read_b64_tr_b16 a[4:5], v238 offset:8\n"
"v_mfma_f32_16x16x32_bf16 v[12:15], a[76:79], a[160:163], v[12:15]\n"
"buffer_load_dwordx4 v245, s[48:51], s61 offen offset:1024 lds\n"
"v_mfma_f32_16x16x32_bf16 v[12:15], a[108:111], a[164:167], v[12:15]\n"
"ds_read_b64_tr_b16 a[6:7], v238 offset:2056\n"
"v_mfma_f32_16x16x32_bf16 v[16:19], a[80:83], a[160:163], v[16:19]\n"
"v_mfma_f32_16x16x32_bf16 v[16:19], a[112:115], a[164:167], v[16:19]\n"
"ds_read_b64_tr_b16 a[8:9], v239 offset:0\n"
"v_mfma_f32_16x16x32_bf16 v[20:23], a[84:87], a[160:163], v[20:23]\n"
"buffer_load_dwordx4 v246, s[48:51], s61 offen offset:2048 lds\n"
"v_mfma_f32_16x16x32_bf16 v[20:23], a[116:119], a[164:167], v[20:23]\n"
"ds_read_b64_tr_b16 a[10:11], v239 offset:2048\n"
"v_mfma_f32_16x16x32_bf16 v[24:27], a[88:91], a[160:163], v[24:27]\n"
"v_mfma_f32_16x16x32_bf16 v[24:27], a[120:123], a[164:167], v[24:27]\n"
"ds_read_b64_tr_b16 a[12:13], v239 offset:8\n"
"v_mfma_f32_16x16x32_bf16 v[28:31], a[92:95], a[160:163], v[28:31]\n"
"buffer_load_dwordx4 v247, s[48:51], s61 offen offset:3072 lds\n"
"v_mfma_f32_16x16x32_bf16 v[28:31], a[124:127], a[164:167], v[28:31]\n"
"ds_read_b64_tr_b16 a[14:15], v239 offset:2056\n"
"v_mfma_f32_16x16x32_bf16 v[32:35], a[64:67], a[168:171], v[32:35]\n"
"v_mfma_f32_16x16x32_bf16 v[32:35], a[96:99], a[172:175], v[32:35]\n"
"ds_read_b64_tr_b16 a[16:17], v240 offset:0\n"
"s_add_u32 m0, s64, 36864\n"
"v_mfma_f32_16x16x32_bf16 v[36:39], a[68:71], a[168:171], v[36:39]\n"
"buffer_load_dwordx4 v244, s[48:51], s62 offen offset:0 lds\n"
"v_mfma_f32_16x16x32_bf16 v[36:39], a[100:103], a[172:175], v[36:39]\n"
"ds_read_b64_tr_b16 a[18:19], v240 offset:2048\n"
"v_mfma_f32_16x16x32_bf16 v[40:43], a[72:75], a[168:171], v[40:43]\n"
"v_mfma_f32_16x16x32_bf16 v[40:43], a[104:107], a[172:175], v[40:43]\n"
"ds_read_b64_tr_b16 a[20:21], v240 offset:8\n"
"v_mfma_f32_16x16x32_bf16 v[44:47], a[76:79], a[168:171], v[44:47]\n"
"buffer_load_dwordx4 v245, s[48:51], s62 offen offset:1024 lds\n"
"v_mfma_f32_16x16x32_bf16 v[44:47], a[108:111], a[172:175], v[44:47]\n"
"ds_read_b64_tr_b16 a[22:23], v240 offset:2056\n"
"v_mfma_f32_16x16x32_bf16 v[48:51], a[80:83], a[168:171], v[48:51]\n"
"v_mfma_f32_16x16x32_bf16 v[48:51], a[112:115], a[172:175], v[48:51]\n"
"ds_read_b64_tr_b16 a[24:25], v241 offset:0\n"
"v_mfma_f32_16x16x32_bf16 v[52:55], a[84:87], a[168:171], v[52:55]\n"
"buffer_load_dwordx4 v246, s[48:51], s62 offen offset:2048 lds\n"
"v_mfma_f32_16x16x32_bf16 v[52:55], a[116:119], a[172:175], v[52:55]\n"
"ds_read_b64_tr_b16 a[26:27], v241 offset:2048\n"
"v_mfma_f32_16x16x32_bf16 v[56:59], a[88:91], a[168:171], v[56:59]\n"
"v_mfma_f32_16x16x32_bf16 v[56:59], a[120:123], a[172:175], v[56:59]\n"
"ds_read_b64_tr_b16 a[28:29], v241 offset:8\n"
"v_mfma_f32_16x16x32_bf16 v[60:63], a[92:95], a[168:171], v[60:63]\n"
"buffer_load_dwordx4 v247, s[48:51], s62 offen offset:3072 lds\n"
"v_mfma_f32_16x16x32_bf16 v[60:63], a[124:127], a[172:175], v[60:63]\n"
"ds_read_b64_tr_b16 a[30:31], v241 offset:2056\n"
"v_mfma_f32_16x16x32_bf16 v[64:67], a[64:67], a[176:179], v[64:67]\n"
"s_add_u32 s61, s61, 524288\n"
"s_add_u32 s62, s62, 524288\n"
"v_mfma_f32_16x16x32_bf16 v[64:67], a[96:99], a[180:183], v[64:67]\n"
"ds_read_b64_tr_b16 a[32:33], v238 offset:16384\n"
"s_add_u32 m0, s63, 81920\n"
"v_mfma_f32_16x16x32_bf16 v[68:71], a[68:71], a[176:179], v[68:71]\n"
"buffer_load_dwordx4 v248, s[44:47], s59 offen offset:0 lds\n"
"v_mfma_f32_16x16x32_bf16 v[68:71], a[100:103], a[180:183], v[68:71]\n"
"ds_read_b64_tr_b16 a[34:35], v238 offset:18432\n"
"v_mfma_f32_16x16x32_bf16 v[72:75], a[72:75], a[176:179], v[72:75]\n"
"v_mfma_f32_16x16x32_bf16 v[72:75], a[104:107], a[180:183], v[72:75]\n"
"ds_read_b64_tr_b16 a[36:37], v238 offset:16392\n"
"v_mfma_f32_16x16x32_bf16 v[76:79], a[76:79], a[176:179], v[76:79]\n"
"buffer_load_dwordx4 v249, s[44:47], s59 offen offset:1024 lds\n"
"v_mfma_f32_16x16x32_bf16 v[76:79], a[108:111], a[180:183], v[76:79]\n"
"ds_read_b64_tr_b16 a[38:39], v238 offset:18440\n"
"v_mfma_f32_16x16x32_bf16 v[80:83], a[80:83], a[176:179], v[80:83]\n"
"v_mfma_f32_16x16x32_bf16 v[80:83], a[112:115], a[180:183], v[80:83]\n"
"ds_read_b64_tr_b16 a[40:41], v239 offset:16384\n"
"v_mfma_f32_16x16x32_bf16 v[84:87], a[84:87], a[176:179], v[84:87]\n"
"buffer_load_dwordx4 v250, s[44:47], s59 offen offset:2048 lds\n"
"v_mfma_f32_16x16x32_bf16 v[84:87], a[116:119], a[180:183], v[84:87]\n"
"ds_read_b64_tr_b16 a[42:43], v239 offset:18432\n"
"v_mfma_f32_16x16x32_bf16 v[88:91], a[88:91], a[176:179], v[88:91]\n"
"v_mfma_f32_16x16x32_bf16 v[88:91], a[120:123], a[180:183], v[88:91]\n"
"ds_read_b64_tr_b16 a[44:45], v239 offset:16392\n"
"v_mfma_f32_16x16x32_bf16 v[92:95], a[92:95], a[176:179], v[92:95]\n"
"buffer_load_dwordx4 v251, s[44:47], s59 offen offset:3072 lds\n"
"v_mfma_f32_16x16x32_bf16 v[92:95], a[124:127], a[180:183], v[92:95]\n"
"ds_read_b64_tr_b16 a[46:47], v239 offset:18440\n"
"v_mfma_f32_16x16x32_bf16 v[96:99], a[64:67], a[128:131], v[96:99]\n"
"v_mfma_f32_16x16x32_bf16 v[96:99], a[96:99], a[132:135], v[96:99]\n"
"ds_read_b64_tr_b16 a[48:49], v240 offset:16384\n"
"s_add_u32 m0, s63, 86016\n"
"v_mfma_f32_16x16x32_bf16 v[100:103], a[68:71], a[128:131], v[100:103]\n"
"buffer_load_dwordx4 v248, s[44:47], s60 offen offset:0 lds\n"
"v_mfma_f32_16x16x32_bf16 v[100:103], a[100:103], a[132:135], v[100:103]\n"
"ds_read_b64_tr_b16 a[50:51], v240 offset:18432\n"
"v_mfma_f32_16x16x32_bf16 v[104:107], a[72:75], a[128:131], v[104:107]\n"
"v_mfma_f32_16x16x32_bf16 v[104:107], a[104:107], a[132:135], v[104:107]\n"
"ds_read_b64_tr_b16 a[52:53], v240 offset:16392\n"
"v_mfma_f32_16x16x32_bf16 v[108:111], a[76:79], a[128:131], v[108:111]\n"
"buffer_load_dwordx4 v249, s[44:47], s60 offen offset:1024 lds\n"
"v_mfma_f32_16x16x32_bf16 v[108:111], a[108:111], a[132:135], v[108:111]\n"
"ds_read_b64_tr_b16 a[54:55], v240 offset:18440\n"
"s_add_u32 s59, s59, 128\n"
"s_add_u32 s60, s60, 128\n"
"v_mfma_f32_16x16x32_bf16 v[112:115], a[80:83], a[128:131], v[112:115]\n"
"v_mfma_f32_16x16x32_bf16 v[112:115], a[112:115], a[132:135], v[112:115]\n"
"ds_read_b64_tr_b16 a[56:57], v241 offset:16384\n"
"v_mfma_f32_16x16x32_bf16 v[116:119], a[84:87], a[128:131], v[116:119]\n"
"v_mfma_f32_16x16x32_bf16 v[116:119], a[116:119], a[132:135], v[116:119]\n"
"ds_read_b64_tr_b16 a[58:59], v241 offset:18432\n"
"v_mfma_f32_16x16x32_bf16 v[120:123], a[88:91], a[128:131], v[120:123]\n"
"s_cmp_lg_u32 s66, 0\n"
"s_cbranch_scc1 .Lf_k00\n"
"s_add_u32 m0, s57, 0\n"
"s_nop 0\n"
"buffer_load_dwordx4 v234, s[36:39], s56 offen offset:0 nt lds\n"
".Lf_k00:\n"
"v_mfma_f32_16x16x32_bf16 v[120:123], a[120:123], a[132:135], v[120:123]\n"
"ds_read_b64_tr_b16 a[60:61], v241 offset:16392\n"
"v_mfma_f32_16x16x32_bf16 v[124:127], a[92:95], a[128:131], v[124:127]\n"
"v_mfma_f32_16x16x32_bf16 v[124:127], a[124:127], a[132:135], v[124:127]\n"
"ds_read_b64_tr_b16 a[62:63], v241 offset:18440\n"
"s_cmp_eq_u32 s66, 0\n"
"s_cbranch_scc1 .Lf_ps21\n"
"s_add_u32 s75, s67, 20480\n"
"buffer_store_dwordx4 a[228:231], v252, s[52:55], s75 offen offset:1024 sc1\n"
".Lf_ps21:\n"
"v_xor_b32 v238, 0x28000, v238\n"
"v_xor_b32 v239, 0x28000, v239\n"
"v_xor_b32 v240, 0x28000, v240\n"
"v_xor_b32 v241, 0x28000, v241\n"
"v_mfma_f32_16x16x32_bf16 v[128:131], a[64:67], a[136:139], v[128:131]\n"
"v_mfma_f32_16x16x32_bf16 v[128:131], a[96:99], a[140:143], v[128:131]\n"
"ds_read_b128 a[152:155], v242 offset:24576\n"
"v_xor_b32 v242, 64, v242\n"
"v_mfma_f32_16x16x32_bf16 v[132:135], a[68:71], a[136:139], v[132:135]\n"
"s_cmp_lg_u32 s66, 0\n"
"s_cbranch_scc1 .Lf_k01\n"
"s_add_u32 m0, s57, 0\n"
"s_nop 0\n"
"buffer_load_dwordx4 v234, s[36:39], s56 offen offset:1024 nt lds\n"
".Lf_k01:\n"
"v_mfma_f32_16x16x32_bf16 v[132:135], a[100:103], a[140:143], v[132:135]\n"
"ds_read_b128 a[156:159], v242 offset:24576\n"
"v_xor_b32 v242, 64, v242\n"
"v_mfma_f32_16x16x32_bf16 v[136:139], a[72:75], a[136:139], v[136:139]\n"
"v_mfma_f32_16x16x32_bf16 v[136:139], a[104:107], a[140:143], v[136:139]\n"
"ds_read_b128 a[160:163], v242 offset:26624\n"
"s_cmp_eq_u32 s66, 0\n"
"s_cbranch_scc1 .Lf_ps22\n"
"s_add_u32 s75, s67, 20480\n"
"buffer_store_dwordx4 a[232:235], v252, s[52:55], s75 offen offset:2048 sc1\n"
".Lf_ps22:\n"
"v_xor_b32 v242, 64, v242\n"
"v_mfma_f32_16x16x32_bf16 v[140:143], a[76:79], a[136:139], v[140:143]\n"
"v_mfma_f32_16x16x32_bf16 v[140:143], a[108:111], a[140:143], v[140:143]\n"
"ds_read_b128 a[164:167], v242 offset:26624\n"
"v_xor_b32 v242, 64, v242\n"
"v_mfma_f32_16x16x32_bf16 v[144:147], a[80:83], a[136:139], v[144:147]\n"
"s_cmp_lg_u32 s66, 0\n"
"s_cbranch_scc1 .Lf_k02\n"
"s_add_u32 m0, s57, 0\n"
"s_nop 0\n"
"buffer_load_dwordx4 v235, s[36:39], s56 offen offset:2048 nt lds\n"
".Lf_k02:\n"
"v_mfma_f32_16x16x32_bf16 v[144:147], a[112:115], a[140:143], v[144:147]\n"
"ds_read_b128 a[168:171], v242 offset:28672\n"
"v_xor_b32 v242, 64, v242\n"
"v_mfma_f32_16x16x32_bf16 v[148:151], a[84:87], a[136:139], v[148:151]\n"
"v_mfma_f32_16x16x32_bf16 v[148:151], a[116:119], a[140:143], v[148:151]\n"
"ds_read_b128 a[172:175], v242 offset:28672\n"
"s_cmp_eq_u32 s66, 0\n"
"s_cbranch_scc1 .Lf_ps23\n"
"s_add_u32 s75, s67, 20480\n"
"buffer_store_dwordx4 a[236:239], v252, s[52:55], s75 offen offset:3072 sc1\n"
".Lf_ps23:\n"
"v_xor_b32 v242, 64, v242\n"
"v_mfma_f32_16x16x32_bf16 v[152:155], a[88:91], a[136:139], v[152:155]\n"
"v_mfma_f32_16x16x32_bf16 v[152:155], a[120:123], a[140:143], v[152:155]\n"
"ds_read_b128 a[176:179], v242 offset:30720\n"
"v_xor_b32 v242, 64, v242\n"
"v_mfma_f32_16x16x32_bf16 v[156:159], a[92:95], a[136:139], v[156:159]\n"
"s_cmp_lg_u32 s66, 0\n"
"s_cbranch_scc1 .Lf_k03\n"
"s_add_u32 m0, s57, 0\n"
"s_nop 0\n"
"buffer_load_dwordx4 v235, s[36:39], s56 offen offset:3072 nt lds\n"
".Lf_k03:\n"
"v_mfma_f32_16x16x32_bf16 v[156:159], a[124:127], a[140:143], v[156:159]\n"
"ds_read_b128 a[180:183], v242 offset:30720\n"
"v_xor_b32 v242, 64, v242\n"
"v_mfma_f32_16x16x32_bf16 v[160:163], a[64:67], a[144:147], v[160:163]\n"
"v_mfma_f32_16x16x32_bf16 v[160:163], a[96:99], a[148:151], v[160:163]\n"
"ds_read_b128 a[128:131], v242 offset:32768\n"
"v_xor_b32 v242, 64, v242\n"
"v_mfma_f32_16x16x32_bf16 v[164:167], a[68:71], a[144:147], v[164:167]\n"
"v_mfma_f32_16x16x32_bf16 v[164:167], a[100:103], a[148:151], v[164:167]\n"
"ds_read_b128 a[132:135], v242 offset:32768\n"
"v_xor_b32 v242, 64, v242\n"
"v_mfma_f32_16x16x32_bf16 v[168:171], a[72:75], a[144:147], v[168:171]\n"
"v_mfma_f32_16x16x32_bf16 v[168:171], a[104:107], a[148:151], v[168:171]\n"
"ds_read_b128 a[136:139], v242 offset:34816\n"
"v_xor_b32 v242, 64, v242\n"
"v_mfma_f32_16x16x32_bf16 v[172:175], a[76:79], a[144:147], v[172:175]\n"
"v_mfma_f32_16x16x32_bf16 v[172:175], a[108:111], a[148:151], v[172:175]\n"
"ds_read_b128 a[140:143], v242 offset:34816\n"
"v_xor_b32 v242, 64, v242\n"
"v_mfma_f32_16x16x32_bf16 v[176:179], a[80:83], a[144:147], v[176:179]\n"
"v_mfma_f32_16x16x32_bf16 v[176:179], a[112:115], a[148:151], v[176:179]\n"
"v_mfma_f32_16x16x32_bf16 v[180:183], a[84:87], a[144:147], v[180:183]\n"
"v_mfma_f32_16x16x32_bf16 v[180:183], a[116:119], a[148:151], v[180:183]\n"
"v_mfma_f32_16x16x32_bf16 v[184:187], a[88:91], a[144:147], v[184:187]\n"
"v_mfma_f32_16x16x32_bf16 v[184:187], a[120:123], a[148:151], v[184:187]\n"
"v_mfma_f32_16x16x32_bf16 v[188:191], a[92:95], a[144:147], v[188:191]\n"
"v_mfma_f32_16x16x32_bf16 v[188:191], a[124:127], a[148:151], v[188:191]\n"
"s_cmp_lg_u32 s66, 0\n"
"s_cbranch_scc1 .Lf_nc4\n"
"s_waitcnt vmcnt(4) lgkmcnt(0)\n"
".Lf_nc4:\n"
"s_cmp_eq_u32 s66, 0\n"
"s_cbranch_scc1 .Lf_nw4\n"
"s_waitcnt vmcnt(3) lgkmcnt(0)\n"
".Lf_nw4:\n"
"s_barrier\n"
"v_mfma_f32_16x16x32_bf16 v[0:3], a[0:3], a[152:155], v[0:3]\n"
"v_mfma_f32_16x16x32_bf16 v[0:3], a[32:35], a[156:159], v[0:3]\n"
"ds_read_b64_tr_b16 a[64:65], v238 offset:0\n"
"s_add_u32 m0, s64, 131072\n"
"v_mfma_f32_16x16x32_bf16 v[4:7], a[4:7], a[152:155], v[4:7]\n"
"buffer_load_dwordx4 v244, s[48:51], s61 offen offset:0 lds\n"
"v_mfma_f32_16x16x32_bf16 v[4:7], a[36:39], a[156:159], v[4:7]\n"
"ds_read_b64_tr_b16 a[66:67], v238 offset:2048\n"
"v_mfma_f32_16x16x32_bf16 v[8:11], a[8:11], a[152:155], v[8:11]\n"
"v_mfma_f32_16x16x32_bf16 v[8:11], a[40:43], a[156:159], v[8:11]\n"
"ds_read_b64_tr_b16 a[68:69], v238 offset:8\n"
"v_mfma_f32_16x16x32_bf16 v[12:15], a[12:15], a[152:155], v[12:15]\n"
"buffer_load_dwordx4 v245, s[48:51], s61 offen offset:1024 lds\n"
"v_mfma_f32_16x16x32_bf16 v[12:15], a[44:47], a[156:159], v[12:15]\n"
"ds_read_b64_tr_b16 a[70:71], v238 offset:2056\n"
"v_mfma_f32_16x16x32_bf16 v[16:19], a[16:19], a[152:155], v[16:19]\n"
"v_mfma_f32_16x16x32_bf16 v[16:19], a[48:51], a[156:159], v[16:19]\n"
"ds_read_b64_tr_b16 a[72:73], v239 offset:0\n"
"v_mfma_f32_16x16x32_bf16 v[20:23], a[20:23], a[152:155], v[20:23]\n"
"buffer_load_dwordx4 v246, s[48:51], s61 offen offset:2048 lds\n"
"v_mfma_f32_16x16x32_bf16 v[20:23], a[52:55], a[156:159], v[20:23]\n"
"ds_read_b64_tr_b16 a[74:75], v239 offset:2048\n"
"v_mfma_f32_16x16x32_bf16 v[24:27], a[24:27], a[152:155], v[24:27]\n"
"v_mfma_f32_16x16x32_bf16 v[24:27], a[56:59], a[156:159], v[24:27]\n"
"ds_read_b64_tr_b16 a[76:77], v239 offset:8\n"
"v_mfma_f32_16x16x32_bf16 v[28:31], a[28:31], a[152:155], v[28:31]\n"
"buffer_load_dwordx4 v247, s[48:51], s61 offen offset:3072 lds\n"
"v_mfma_f32_16x16x32_bf16 v[28:31], a[60:63], a[156:159], v[28:31]\n"
"ds_read_b64_tr_b16 a[78:79], v239 offset:2056\n"
"v_mfma_f32_16x16x32_bf16 v[32:35], a[0:3], a[160:163], v[32:35]\n"
"v_mfma_f32_16x16x32_bf16 v[32:35], a[32:35], a[164:167], v[32:35]\n"
"ds_read_b64_tr_b16 a[80:81], v240 offset:0\n"
"s_add_u32 m0, s64, 135168\n"
"v_mfma_f32_16x16x32_bf16 v[36:39], a[4:7], a[160:163], v[36:39]\n"
"buffer_load_dwordx4 v244, s[48:51], s62 offen offset:0 lds\n"
"v_mfma_f32_16x16x32_bf16 v[36:39], a[36:39], a[164:167], v[36:39]\n"
"ds_read_b64_tr_b16 a[82:83], v240 offset:2048\n"
"v_mfma_f32_16x16x32_bf16 v[40:43], a[8:11], a[160:163], v[40:43]\n"
"v_mfma_f32_16x16x32_bf16 v[40:43], a[40:43], a[164:167], v[40:43]\n"
"ds_read_b64_tr_b16 a[84:85], v240 offset:8\n"
"v_mfma_f32_16x16x32_bf16 v[44:47], a[12:15], a[160:163], v[44:47]\n"
"buffer_load_dwordx4 v245, s[48:51], s62 offen offset:1024 lds\n"
"v_mfma_f32_16x16x32_bf16 v[44:47], a[44:47], a[164:167], v[44:47]\n"
"ds_read_b64_tr_b16 a[86:87], v240 offset:2056\n"
"v_mfma_f32_16x16x32_bf16 v[48:51], a[16:19], a[160:163], v[48:51]\n"
"v_mfma_f32_16x16x32_bf16 v[48:51], a[48:51], a[164:167], v[48:51]\n"
"ds_read_b64_tr_b16 a[88:89], v241 offset:0\n"
"v_mfma_f32_16x16x32_bf16 v[52:55], a[20:23], a[160:163], v[52:55]\n"
"buffer_load_dwordx4 v246, s[48:51], s62 offen offset:2048 lds\n"
"v_mfma_f32_16x16x32_bf16 v[52:55], a[52:55], a[164:167], v[52:55]\n"
"ds_read_b64_tr_b16 a[90:91], v241 offset:2048\n"
"v_mfma_f32_16x16x32_bf16 v[56:59], a[24:27], a[160:163], v[56:59]\n"
"v_mfma_f32_16x16x32_bf16 v[56:59], a[56:59], a[164:167], v[56:59]\n"
"ds_read_b64_tr_b16 a[92:93], v241 offset:8\n"
"v_mfma_f32_16x16x32_bf16 v[60:63], a[28:31], a[160:163], v[60:63]\n"
"buffer_load_dwordx4 v247, s[48:51], s62 offen offset:3072 lds\n"
"v_mfma_f32_16x16x32_bf16 v[60:63], a[60:63], a[164:167], v[60:63]\n"
"ds_read_b64_tr_b16 a[94:95], v241 offset:2056\n"
"v_mfma_f32_16x16x32_bf16 v[64:67], a[0:3], a[168:171], v[64:67]\n"
"s_add_u32 s61, s61, 524288\n"
"s_add_u32 s62, s62, 524288\n"
"v_mfma_f32_16x16x32_bf16 v[64:67], a[32:35], a[172:175], v[64:67]\n"
"ds_read_b64_tr_b16 a[96:97], v238 offset:16384\n"
"s_add_u32 m0, s63, 106496\n"
"v_mfma_f32_16x16x32_bf16 v[68:71], a[4:7], a[168:171], v[68:71]\n"
"buffer_load_dwordx4 v248, s[44:47], s59 offen offset:0 lds\n"
"v_mfma_f32_16x16x32_bf16 v[68:71], a[36:39], a[172:175], v[68:71]\n"
"ds_read_b64_tr_b16 a[98:99], v238 offset:18432\n"
"v_mfma_f32_16x16x32_bf16 v[72:75], a[8:11], a[168:171], v[72:75]\n"
"v_mfma_f32_16x16x32_bf16 v[72:75], a[40:43], a[172:175], v[72:75]\n"
"ds_read_b64_tr_b16 a[100:101], v238 offset:16392\n"
"v_mfma_f32_16x16x32_bf16 v[76:79], a[12:15], a[168:171], v[76:79]\n"
"buffer_load_dwordx4 v249, s[44:47], s59 offen offset:1024 lds\n"
"v_mfma_f32_16x16x32_bf16 v[76:79], a[44:47], a[172:175], v[76:79]\n"
"ds_read_b64_tr_b16 a[102:103], v238 offset:18440\n"
"v_mfma_f32_16x16x32_bf16 v[80:83], a[16:19], a[168:171], v[80:83]\n"
"v_mfma_f32_16x16x32_bf16 v[80:83], a[48:51], a[172:175], v[80:83]\n"
"ds_read_b64_tr_b16 a[104:105], v239 offset:16384\n"
"v_mfma_f32_16x16x32_bf16 v[84:87], a[20:23], a[168:171], v[84:87]\n"
"buffer_load_dwordx4 v250, s[44:47], s59 offen offset:2048 lds\n"
"v_mfma_f32_16x16x32_bf16 v[84:87], a[52:55], a[172:175], v[84:87]\n"
"ds_read_b64_tr_b16 a[106:107], v239 offset:18432\n"
"v_mfma_f32_16x16x32_bf16 v[88:91], a[24:27], a[168:171], v[88:91]\n"
"v_mfma_f32_16x16x32_bf16 v[88:91], a[56:59], a[172:175], v[88:91]\n"
"ds_read_b64_tr_b16 a[108:109], v239 offset:16392\n"
"v_mfma_f32_16x16x32_bf16 v[92:95], a[28:31], a[168:171], v[92:95]\n"
"buffer_load_dwordx4 v251, s[44:47], s59 offen offset:3072 lds\n"
"v_mfma_f32_16x16x32_bf16 v[92:95], a[60:63], a[172:175], v[92:95]\n"
"ds_read_b64_tr_b16 a[110:111], v239 offset:18440\n"
"v_mfma_f32_16x16x32_bf16 v[96:99], a[0:3], a[176:179], v[96:99]\n"
"v_mfma_f32_16x16x32_bf16 v[96:99], a[32:35], a[180:183], v[96:99]\n"
"ds_read_b64_tr_b16 a[112:113], v240 offset:16384\n"
"s_add_u32 m0, s63, 110592\n"
"v_mfma_f32_16x16x32_bf16 v[100:103], a[4:7], a[176:179], v[100:103]\n"
"buffer_load_dwordx4 v248, s[44:47], s60 offen offset:0 lds\n"
"v_mfma_f32_16x16x32_bf16 v[100:103], a[36:39], a[180:183], v[100:103]\n"
"ds_read_b64_tr_b16 a[114:115], v240 offset:18432\n"
"v_mfma_f32_16x16x32_bf16 v[104:107], a[8:11], a[176:179], v[104:107]\n"
"v_mfma_f32_16x16x32_bf16 v[104:107], a[40:43], a[180:183], v[104:107]\n"
"ds_read_b64_tr_b16 a[116:117], v240 offset:16392\n"
"v_mfma_f32_16x16x32_bf16 v[108:111], a[12:15], a[176:179], v[108:111]\n"
"buffer_load_dwordx4 v249, s[44:47], s60 offen offset:1024 lds\n"
"v_mfma_f32_16x16x32_bf16 v[108:111], a[44:47], a[180:183], v[108:111]\n"
"ds_read_b64_tr_b16 a[118:119], v240 offset:18440\n"
"s_add_u32 s59, s59, 128\n"
"s_add_u32 s60, s60, 128\n"
"v_mfma_f32_16x16x32_bf16 v[112:115], a[16:19], a[176:179], v[112:115]\n"
"v_mfma_f32_16x16x32_bf16 v[112:115], a[48:51], a[180:183], v[112:115]\n"
"ds_read_b64_tr_b16 a[120:121], v241 offset:16384\n"
"v_mfma_f32_16x16x32_bf16 v[116:119], a[20:23], a[176:179], v[116:119]\n"
"v_mfma_f32_16x16x32_bf16 v[116:119], a[52:55], a[180:183], v[116:119]\n"
"ds_read_b64_tr_b16 a[122:123], v241 offset:18432\n"
"v_mfma_f32_16x16x32_bf16 v[120:123], a[24:27], a[176:179], v[120:123]\n"
"s_cmp_lg_u32 s66, 0\n"
"s_cbranch_scc1 .Lf_v00\n"
"s_add_u32 m0, s58, 0\n"
"s_nop 0\n"
"buffer_load_dwordx4 v236, s[40:43], s56 offen offset:0 nt lds\n"
".Lf_v00:\n"
"v_mfma_f32_16x16x32_bf16 v[120:123], a[56:59], a[180:183], v[120:123]\n"
"ds_read_b64_tr_b16 a[124:125], v241 offset:16392\n"
"v_mfma_f32_16x16x32_bf16 v[124:127], a[28:31], a[176:179], v[124:127]\n"
"v_mfma_f32_16x16x32_bf16 v[124:127], a[60:63], a[180:183], v[124:127]\n"
"ds_read_b64_tr_b16 a[126:127], v241 offset:18440\n"
"v_xor_b32 v238, 0x28000, v238\n"
"v_xor_b32 v239, 0x28000, v239\n"
"v_xor_b32 v240, 0x28000, v240\n"
"v_xor_b32 v241, 0x28000, v241\n"
"v_mfma_f32_16x16x32_bf16 v[128:131], a[0:3], a[128:131], v[128:131]\n"
"v_mfma_f32_16x16x32_bf16 v[128:131], a[32:35], a[132:135], v[128:131]\n"
"ds_read_b128 a[144:147], v242 offset:0\n"
"v_xor_b32 v242, 64, v242\n"
"v_mfma_f32_16x16x32_bf16 v[132:135], a[4:7], a[128:131], v[132:135]\n"
"s_cmp_lg_u32 s66, 0\n"
"s_cbranch_scc1 .Lf_v01\n"
"s_add_u32 m0, s58, 0\n"
"s_nop 0\n"
"buffer_load_dwordx4 v236, s[40:43], s56 offen offset:1024 nt lds\n"
".Lf_v01:\n"
"v_mfma_f32_16x16x32_bf16 v[132:135], a[36:39], a[132:135], v[132:135]\n"
"ds_read_b128 a[148:151], v242 offset:0\n"
"v_xor_b32 v242, 64, v242\n"
"v_mfma_f32_16x16x32_bf16 v[136:139], a[8:11], a[128:131], v[136:139]\n"
"v_mfma_f32_16x16x32_bf16 v[136:139], a[40:43], a[132:135], v[136:139]\n"
"ds_read_b128 a[152:155], v242 offset:2048\n"
"v_xor_b32 v242, 64, v242\n"
"v_mfma_f32_16x16x32_bf16 v[140:143], a[12:15], a[128:131], v[140:143]\n"
"v_mfma_f32_16x16x32_bf16 v[140:143], a[44:47], a[132:135], v[140:143]\n"
"ds_read_b128 a[156:159], v242 offset:2048\n"
"v_xor_b32 v242, 64, v242\n"
"v_mfma_f32_16x16x32_bf16 v[144:147], a[16:19], a[128:131], v[144:147]\n"
"s_cmp_lg_u32 s66, 0\n"
"s_cbranch_scc1 .Lf_v02\n"
"s_add_u32 m0, s58, 0\n"
"s_nop 0\n"
"buffer_load_dwordx4 v237, s[40:43], s56 offen offset:2048 nt lds\n"
".Lf_v02:\n"
"v_mfma_f32_16x16x32_bf16 v[144:147], a[48:51], a[132:135], v[144:147]\n"
"ds_read_b128 a[160:163], v242 offset:4096\n"
"v_xor_b32 v242, 64, v242\n"
"v_mfma_f32_16x16x32_bf16 v[148:151], a[20:23], a[128:131], v[148:151]\n"
"v_mfma_f32_16x16x32_bf16 v[148:151], a[52:55], a[132:135], v[148:151]\n"
"ds_read_b128 a[164:167], v242 offset:4096\n"
"v_xor_b32 v242, 64, v242\n"
"v_mfma_f32_16x16x32_bf16 v[152:155], a[24:27], a[128:131], v[152:155]\n"
"v_mfma_f32_16x16x32_bf16 v[152:155], a[56:59], a[132:135], v[152:155]\n"
"ds_read_b128 a[168:171], v242 offset:6144\n"
"v_xor_b32 v242, 64, v242\n"
"v_mfma_f32_16x16x32_bf16 v[156:159], a[28:31], a[128:131], v[156:159]\n"
"s_cmp_lg_u32 s66, 0\n"
"s_cbranch_scc1 .Lf_v03\n"
"s_add_u32 m0, s58, 0\n"
"s_nop 0\n"
"buffer_load_dwordx4 v237, s[40:43], s56 offen offset:3072 nt lds\n"
"s_add_u32 s56, s56, 8192\n"
"s_and_b32 s56, s56, 0x1ffff\n"
".Lf_v03:\n"
"v_mfma_f32_16x16x32_bf16 v[156:159], a[60:63], a[132:135], v[156:159]\n"
"ds_read_b128 a[172:175], v242 offset:6144\n"
"v_xor_b32 v242, 64, v242\n"
"v_mfma_f32_16x16x32_bf16 v[160:163], a[0:3], a[136:139], v[160:163]\n"
"v_mfma_f32_16x16x32_bf16 v[160:163], a[32:35], a[140:143], v[160:163]\n"
"ds_read_b128 a[176:179], v242 offset:8192\n"
"v_xor_b32 v242, 64, v242\n"
"v_mfma_f32_16x16x32_bf16 v[164:167], a[4:7], a[136:139], v[164:167]\n"
"v_mfma_f32_16x16x32_bf16 v[164:167], a[36:39], a[140:143], v[164:167]\n"
"ds_read_b128 a[180:183], v242 offset:8192\n"
"v_xor_b32 v242, 64, v242\n"
"v_mfma_f32_16x16x32_bf16 v[168:171], a[8:11], a[136:139], v[168:171]\n"
"v_mfma_f32_16x16x32_bf16 v[168:171], a[40:43], a[140:143], v[168:171]\n"
"ds_read_b128 a[128:131], v242 offset:10240\n"
"v_xor_b32 v242, 64, v242\n"
"v_mfma_f32_16x16x32_bf16 v[172:175], a[12:15], a[136:139], v[172:175]\n"
"v_mfma_f32_16x16x32_bf16 v[172:175], a[44:47], a[140:143], v[172:175]\n"
"ds_read_b128 a[132:135], v242 offset:10240\n"
"v_xor_b32 v242, 64, v242\n"
"v_mfma_f32_16x16x32_bf16 v[176:179], a[16:19], a[136:139], v[176:179]\n"
"v_mfma_f32_16x16x32_bf16 v[176:179], a[48:51], a[140:143], v[176:179]\n"
"v_mfma_f32_16x16x32_bf16 v[180:183], a[20:23], a[136:139], v[180:183]\n"
"v_mfma_f32_16x16x32_bf16 v[180:183], a[52:55], a[140:143], v[180:183]\n"
"v_mfma_f32_16x16x32_bf16 v[184:187], a[24:27], a[136:139], v[184:187]\n"
"v_mfma_f32_16x16x32_bf16 v[184:187], a[56:59], a[140:143], v[184:187]\n"
"v_mfma_f32_16x16x32_bf16 v[188:191], a[28:31], a[136:139], v[188:191]\n"
"v_mfma_f32_16x16x32_bf16 v[188:191], a[60:63], a[140:143], v[188:191]\n"
"s_cmp_lg_u32 s66, 0\n"
"s_cbranch_scc1 .Lf_nc5\n"
"s_waitcnt vmcnt(4) lgkmcnt(0)\n"
".Lf_nc5:\n"
"s_cmp_eq_u32 s66, 0\n"
"s_cbranch_scc1 .Lf_nw5\n"
"s_waitcnt vmcnt(0) lgkmcnt(0)\n"
".Lf_nw5:\n"
"s_barrier\n"
"v_mfma_f32_16x16x32_bf16 v[0:3], a[64:67], a[144:147], v[0:3]\n"
"v_mfma_f32_16x16x32_bf16 v[0:3], a[96:99], a[148:151], v[0:3]\n"
"ds_read_b64_tr_b16 a[0:1], v238 offset:0\n"
"s_add_u32 m0, s64, 32768\n"
"v_mfma_f32_16x16x32_bf16 v[4:7], a[68:71], a[144:147], v[4:7]\n"
"buffer_load_dwordx4 v244, s[48:51], s61 offen offset:0 lds\n"
"v_mfma_f32_16x16x32_bf16 v[4:7], a[100:103], a[148:151], v[4:7]\n"
"ds_read_b64_tr_b16 a[2:3], v238 offset:2048\n"
"v_mfma_f32_16x16x32_bf16 v[8:11], a[72:75], a[144:147], v[8:11]\n"
"v_mfma_f32_16x16x32_bf16 v[8:11], a[104:107], a[148:151], v[8:11]\n"
"ds_read_b64_tr_b16 a[4:5], v238 offset:8\n"
"v_mfma_f32_16x16x32_bf16 v[12:15], a[76:79], a[144:147], v[12:15]\n"
"buffer_load_dwordx4 v245, s[48:51], s61 offen offset:1024 lds\n"
"v_mfma_f32_16x16x32_bf16 v[12:15], a[108:111], a[148:151], v[12:15]\n"
"ds_read_b64_tr_b16 a[6:7], v238 offset:2056\n"
"v_mfma_f32_16x16x32_bf16 v[16:19], a[80:83], a[144:147], v[16:19]\n"
"v_mfma_f32_16x16x32_bf16 v[16:19], a[112:115], a[148:151], v[16:19]\n"
"ds_read_b64_tr_b16 a[8:9], v239 offset:0\n"
"v_mfma_f32_16x16x32_bf16 v[20:23], a[84:87], a[144:147], v[20:23]\n"
"buffer_load_dwordx4 v246, s[48:51], s61 offen offset:2048 lds\n"
"v_mfma_f32_16x16x32_bf16 v[20:23], a[116:119], a[148:151], v[20:23]\n"
"ds_read_b64_tr_b16 a[10:11], v239 offset:2048\n"
"v_mfma_f32_16x16x32_bf16 v[24:27], a[88:91], a[144:147], v[24:27]\n"
"v_mfma_f32_16x16x32_bf16 v[24:27], a[120:123], a[148:151], v[24:27]\n"
"ds_read_b64_tr_b16 a[12:13], v239 offset:8\n"
"v_mfma_f32_16x16x32_bf16 v[28:31], a[92:95], a[144:147], v[28:31]\n"
"buffer_load_dwordx4 v247, s[48:51], s61 offen offset:3072 lds\n"
"v_mfma_f32_16x16x32_bf16 v[28:31], a[124:127], a[148:151], v[28:31]\n"
"ds_read_b64_tr_b16 a[14:15], v239 offset:2056\n"
"v_mfma_f32_16x16x32_bf16 v[32:35], a[64:67], a[152:155], v[32:35]\n"
"v_mfma_f32_16x16x32_bf16 v[32:35], a[96:99], a[156:159], v[32:35]\n"
"ds_read_b64_tr_b16 a[16:17], v240 offset:0\n"
"s_add_u32 m0, s64, 36864\n"
"v_mfma_f32_16x16x32_bf16 v[36:39], a[68:71], a[152:155], v[36:39]\n"
"buffer_load_dwordx4 v244, s[48:51], s62 offen offset:0 lds\n"
"v_mfma_f32_16x16x32_bf16 v[36:39], a[100:103], a[156:159], v[36:39]\n"
"ds_read_b64_tr_b16 a[18:19], v240 offset:2048\n"
"v_mfma_f32_16x16x32_bf16 v[40:43], a[72:75], a[152:155], v[40:43]\n"
"v_mfma_f32_16x16x32_bf16 v[40:43], a[104:107], a[156:159], v[40:43]\n"
"ds_read_b64_tr_b16 a[20:21], v240 offset:8\n"
"v_mfma_f32_16x16x32_bf16 v[44:47], a[76:79], a[152:155], v[44:47]\n"
"buffer_load_dwordx4 v245, s[48:51], s62 offen offset:1024 lds\n"
"v_mfma_f32_16x16x32_bf16 v[44:47], a[108:111], a[156:159], v[44:47]\n"
"ds_read_b64_tr_b16 a[22:23], v240 offset:2056\n"
"v_mfma_f32_16x16x32_bf16 v[48:51], a[80:83], a[152:155], v[48:51]\n"
"v_mfma_f32_16x16x32_bf16 v[48:51], a[112:115], a[156:159], v[48:51]\n"
"ds_read_b64_tr_b16 a[24:25], v241 offset:0\n"
"v_mfma_f32_16x16x32_bf16 v[52:55], a[84:87], a[152:155], v[52:55]\n"
"buffer_load_dwordx4 v246, s[48:51], s62 offen offset:2048 lds\n"
"v_mfma_f32_16x16x32_bf16 v[52:55], a[116:119], a[156:159], v[52:55]\n"
"ds_read_b64_tr_b16 a[26:27], v241 offset:2048\n"
"v_mfma_f32_16x16x32_bf16 v[56:59], a[88:91], a[152:155], v[56:59]\n"
"v_mfma_f32_16x16x32_bf16 v[56:59], a[120:123], a[156:159], v[56:59]\n"
"ds_read_b64_tr_b16 a[28:29], v241 offset:8\n"
"v_mfma_f32_16x16x32_bf16 v[60:63], a[92:95], a[152:155], v[60:63]\n"
"buffer_load_dwordx4 v247, s[48:51], s62 offen offset:3072 lds\n"
"v_mfma_f32_16x16x32_bf16 v[60:63], a[124:127], a[156:159], v[60:63]\n"
"ds_read_b64_tr_b16 a[30:31], v241 offset:2056\n"
"v_mfma_f32_16x16x32_bf16 v[64:67], a[64:67], a[160:163], v[64:67]\n"
"s_add_u32 s61, s61, 524288\n"
"s_add_u32 s62, s62, 524288\n"
"v_mfma_f32_16x16x32_bf16 v[64:67], a[96:99], a[164:167], v[64:67]\n"
"ds_read_b64_tr_b16 a[32:33], v238 offset:16384\n"
"s_add_u32 m0, s63, 81920\n"
"v_mfma_f32_16x16x32_bf16 v[68:71], a[68:71], a[160:163], v[68:71]\n"
"buffer_load_dwordx4 v248, s[44:47], s59 offen offset:0 lds\n"
"v_mfma_f32_16x16x32_bf16 v[68:71], a[100:103], a[164:167], v[68:71]\n"
"ds_read_b64_tr_b16 a[34:35], v238 offset:18432\n"
"v_mfma_f32_16x16x32_bf16 v[72:75], a[72:75], a[160:163], v[72:75]\n"
"v_mfma_f32_16x16x32_bf16 v[72:75], a[104:107], a[164:167], v[72:75]\n"
"ds_read_b64_tr_b16 a[36:37], v238 offset:16392\n"
"v_mfma_f32_16x16x32_bf16 v[76:79], a[76:79], a[160:163], v[76:79]\n"
"buffer_load_dwordx4 v249, s[44:47], s59 offen offset:1024 lds\n"
"v_mfma_f32_16x16x32_bf16 v[76:79], a[108:111], a[164:167], v[76:79]\n"
"ds_read_b64_tr_b16 a[38:39], v238 offset:18440\n"
"v_mfma_f32_16x16x32_bf16 v[80:83], a[80:83], a[160:163], v[80:83]\n"
"v_mfma_f32_16x16x32_bf16 v[80:83], a[112:115], a[164:167], v[80:83]\n"
"ds_read_b64_tr_b16 a[40:41], v239 offset:16384\n"
"v_mfma_f32_16x16x32_bf16 v[84:87], a[84:87], a[160:163], v[84:87]\n"
"buffer_load_dwordx4 v250, s[44:47], s59 offen offset:2048 lds\n"
"v_mfma_f32_16x16x32_bf16 v[84:87], a[116:119], a[164:167], v[84:87]\n"
"ds_read_b64_tr_b16 a[42:43], v239 offset:18432\n"
"v_mfma_f32_16x16x32_bf16 v[88:91], a[88:91], a[160:163], v[88:91]\n"
"v_mfma_f32_16x16x32_bf16 v[88:91], a[120:123], a[164:167], v[88:91]\n"
"ds_read_b64_tr_b16 a[44:45], v239 offset:16392\n"
"v_mfma_f32_16x16x32_bf16 v[92:95], a[92:95], a[160:163], v[92:95]\n"
"buffer_load_dwordx4 v251, s[44:47], s59 offen offset:3072 lds\n"
"v_mfma_f32_16x16x32_bf16 v[92:95], a[124:127], a[164:167], v[92:95]\n"
"ds_read_b64_tr_b16 a[46:47], v239 offset:18440\n"
"v_mfma_f32_16x16x32_bf16 v[96:99], a[64:67], a[168:171], v[96:99]\n"
"v_mfma_f32_16x16x32_bf16 v[96:99], a[96:99], a[172:175], v[96:99]\n"
"ds_read_b64_tr_b16 a[48:49], v240 offset:16384\n"
"s_add_u32 m0, s63, 86016\n"
"v_mfma_f32_16x16x32_bf16 v[100:103], a[68:71], a[168:171], v[100:103]\n"
"buffer_load_dwordx4 v248, s[44:47], s60 offen offset:0 lds\n"
"v_mfma_f32_16x16x32_bf16 v[100:103], a[100:103], a[172:175], v[100:103]\n"
"ds_read_b64_tr_b16 a[50:51], v240 offset:18432\n"
"v_mfma_f32_16x16x32_bf16 v[104:107], a[72:75], a[168:171], v[104:107]\n"
"v_mfma_f32_16x16x32_bf16 v[104:107], a[104:107], a[172:175], v[104:107]\n"
"ds_read_b64_tr_b16 a[52:53], v240 offset:16392\n"
"v_mfma_f32_16x16x32_bf16 v[108:111], a[76:79], a[168:171], v[108:111]\n"
"buffer_load_dwordx4 v249, s[44:47], s60 offen offset:1024 lds\n"
"v_mfma_f32_16x16x32_bf16 v[108:111], a[108:111], a[172:175], v[108:111]\n"
"ds_read_b64_tr_b16 a[54:55], v240 offset:18440\n"
"s_add_u32 s59, s59, 128\n"
"s_add_u32 s60, s60, 128\n"
"v_mfma_f32_16x16x32_bf16 v[112:115], a[80:83], a[168:171], v[112:115]\n"
"v_mfma_f32_16x16x32_bf16 v[112:115], a[112:115], a[172:175], v[112:115]\n"
"ds_read_b64_tr_b16 a[56:57], v241 offset:16384\n"
"v_mfma_f32_16x16x32_bf16 v[116:119], a[84:87], a[168:171], v[116:119]\n"
"s_cmp_eq_u32 s66, 0\n"
"s_cbranch_scc1 .Lf_w5\n"
"v_mov_b32 v252, 0\n"
"s_add_u32 s73, s84, 1\n"
"v_mov_b32 v253, s73\n"
"global_store_dword v252, v253, %15 sc1\n"
".Lf_w5:\n"
"v_mfma_f32_16x16x32_bf16 v[116:119], a[116:119], a[172:175], v[116:119]\n"
"ds_read_b64_tr_b16 a[58:59], v241 offset:18432\n"
"v_mfma_f32_16x16x32_bf16 v[120:123], a[88:91], a[168:171], v[120:123]\n"
"s_cmp_lg_u32 s66, 0\n"
"s_cbranch_scc1 .Lf_k10\n"
"s_add_u32 m0, s57, 16384\n"
"s_nop 0\n"
"buffer_load_dwordx4 v234, s[36:39], s56 offen offset:0 nt lds\n"
".Lf_k10:\n"
"v_mfma_f32_16x16x32_bf16 v[120:123], a[120:123], a[172:175], v[120:123]\n"
"ds_read_b64_tr_b16 a[60:61], v241 offset:16392\n"
"v_mfma_f32_16x16x32_bf16 v[124:127], a[92:95], a[168:171], v[124:127]\n"
"v_mfma_f32_16x16x32_bf16 v[124:127], a[124:127], a[172:175], v[124:127]\n"
"ds_read_b64_tr_b16 a[62:63], v241 offset:18440\n"
"v_xor_b32 v238, 0x28000, v238\n"
"v_xor_b32 v239, 0x28000, v239\n"
"v_xor_b32 v240, 0x28000, v240\n"
"v_xor_b32 v241, 0x28000, v241\n"
"v_mfma_f32_16x16x32_bf16 v[128:131], a[64:67], a[176:179], v[128:131]\n"
"v_mfma_f32_16x16x32_bf16 v[128:131], a[96:99], a[180:183], v[128:131]\n"
"ds_read_b128 a[136:139], v242 offset:24576\n"
"v_xor_b32 v242, 64, v242\n"
"v_mfma_f32_16x16x32_bf16 v[132:135], a[68:71], a[176:179], v[132:135]\n"
"s_cmp_lg_u32 s66, 0\n"
"s_cbranch_scc1 .Lf_k11\n"
"s_add_u32 m0, s57, 16384\n"
"s_nop 0\n"
"buffer_load_dwordx4 v234, s[36:39], s56 offen offset:1024 nt lds\n"
".Lf_k11:\n"
"v_mfma_f32_16x16x32_bf16 v[132:135], a[100:103], a[180:183], v[132:135]\n"
"ds_read_b128 a[140:143], v242 offset:24576\n"
"v_xor_b32 v242, 64, v242\n"
"v_mfma_f32_16x16x32_bf16 v[136:139], a[72:75], a[176:179], v[136:139]\n"
"v_mfma_f32_16x16x32_bf16 v[136:139], a[104:107], a[180:183], v[136:139]\n"
"ds_read_b128 a[144:147], v242 offset:26624\n"
"v_xor_b32 v242, 64, v242\n"
"v_mfma_f32_16x16x32_bf16 v[140:143], a[76:79], a[176:179], v[140:143]\n"
"v_mfma_f32_16x16x32_bf16 v[140:143], a[108:111], a[180:183], v[140:143]\n"
"ds_read_b128 a[148:151], v242 offset:26624\n"
"v_xor_b32 v242, 64, v242\n"
"v_mfma_f32_16x16x32_bf16 v[144:147], a[80:83], a[176:179], v[144:147]\n"
"s_cmp_lg_u32 s66, 0\n"
"s_cbranch_scc1 .Lf_k12\n"
"s_add_u32 m0, s57, 16384\n"
"s_nop 0\n"
"buffer_load_dwordx4 v235, s[36:39], s56 offen offset:2048 nt lds\n"
".Lf_k12:\n"
"v_mfma_f32_16x16x32_bf16 v[144:147], a[112:115], a[180:183], v[144:147]\n"
"ds_read_b128 a[152:155], v242 offset:28672\n"
"v_xor_b32 v242, 64, v242\n"
"v_mfma_f32_16x16x32_bf16 v[148:151], a[84:87], a[176:179], v[148:151]\n"
"v_mfma_f32_16x16x32_bf16 v[148:151], a[116:119], a[180:183], v[148:151]\n"
"ds_read_b128 a[156:159], v242 offset:28672\n"
"v_xor_b32 v242, 64, v242\n"
"v_mfma_f32_16x16x32_bf16 v[152:155], a[88:91], a[176:179], v[152:155]\n"
"v_mfma_f32_16x16x32_bf16 v[152:155], a[120:123], a[180:183], v[152:155]\n"
"ds_read_b128 a[160:163], v242 offset:30720\n"
"v_xor_b32 v242, 64, v242\n"
"v_mfma_f32_16x16x32_bf16 v[156:159], a[92:95], a[176:179], v[156:159]\n"
"s_cmp_lg_u32 s66, 0\n"
"s_cbranch_scc1 .Lf_k13\n"
"s_add_u32 m0, s57, 16384\n"
"s_nop 0\n"
"buffer_load_dwordx4 v235, s[36:39], s56 offen offset:3072 nt lds\n"
".Lf_k13:\n"
"v_mfma_f32_16x16x32_bf16 v[156:159], a[124:127], a[180:183], v[156:159]\n"
"ds_read_b128 a[164:167], v242 offset:30720\n"
"v_xor_b32 v242, 64, v242\n"
"v_mfma_f32_16x16x32_bf16 v[160:163], a[64:67], a[128:131], v[160:163]\n"
"v_mfma_f32_16x16x32_bf16 v[160:163], a[96:99], a[132:135], v[160:163]\n"
"ds_read_b128 a[168:171], v242 offset:32768\n"
"v_xor_b32 v242, 64, v242\n"
"v_mfma_f32_16x16x32_bf16 v[164:167], a[68:71], a[128:131], v[164:167]\n"
"v_mfma_f32_16x16x32_bf16 v[164:167], a[100:103], a[132:135], v[164:167]\n"
"ds_read_b128 a[172:175], v242 offset:32768\n"
"v_xor_b32 v242, 64, v242\n"
"v_mfma_f32_16x16x32_bf16 v[168:171], a[72:75], a[128:131], v[168:171]\n"
"v_mfma_f32_16x16x32_bf16 v[168:171], a[104:107], a[132:135], v[168:171]\n"
"ds_read_b128 a[176:179], v242 offset:34816\n"
"v_xor_b32 v242, 64, v242\n"
"v_mfma_f32_16x16x32_bf16 v[172:175], a[76:79], a[128:131], v[172:175]\n"
"v_mfma_f32_16x16x32_bf16 v[172:175], a[108:111], a[132:135], v[172:175]\n"
"ds_read_b128 a[180:183], v242 offset:34816\n"
"v_xor_b32 v242, 64, v242\n"
"v_mfma_f32_16x16x32_bf16 v[176:179], a[80:83], a[128:131], v[176:179]\n"
"v_mfma_f32_16x16x32_bf16 v[176:179], a[112:115], a[132:135], v[176:179]\n"
"v_mfma_f32_16x16x32_bf16 v[180:183], a[84:87], a[128:131], v[180:183]\n"
"v_mfma_f32_16x16x32_bf16 v[180:183], a[116:119], a[132:135], v[180:183]\n"
"v_mfma_f32_16x16x32_bf16 v[184:187], a[88:91], a[128:131], v[184:187]\n"
"v_mfma_f32_16x16x32_bf16 v[184:187], a[120:123], a[132:135], v[184:187]\n"
"v_mfma_f32_16x16x32_bf16 v[188:191], a[92:95], a[128:131], v[188:191]\n"
"v_mfma_f32_16x16x32_bf16 v[188:191], a[124:127], a[132:135], v[188:191]\n"
"s_cmp_lg_u32 s66, 0\n"
"s_cbranch_scc1 .Lf_nc6\n"
"s_waitcnt vmcnt(4) lgkmcnt(0)\n"
".Lf_nc6:\n"
"s_cmp_eq_u32 s66, 0\n"
"s_cbranch_scc1 .Lf_nw6\n"
"s_waitcnt vmcnt(1) lgkmcnt(0)\n"
".Lf_nw6:\n"
"s_barrier\n"
"v_mfma_f32_16x16x32_bf16 v[0:3], a[0:3], a[136:139], v[0:3]\n"
"v_mfma_f32_16x16x32_bf16 v[0:3], a[32:35], a[140:143], v[0:3]\n"
"ds_read_b64_tr_b16 a[64:65], v238 offset:0\n"
"s_add_u32 m0, s64, 131072\n"
"v_mfma_f32_16x16x32_bf16 v[4:7], a[4:7], a[136:139], v[4:7]\n"
"buffer_load_dwordx4 v244, s[48:51], s61 offen offset:0 lds\n"
"v_mfma_f32_16x16x32_bf16 v[4:7], a[36:39], a[140:143], v[4:7]\n"
"ds_read_b64_tr_b16 a[66:67], v238 offset:2048\n"
"v_mfma_f32_16x16x32_bf16 v[8:11], a[8:11], a[136:139], v[8:11]\n"
"v_mfma_f32_16x16x32_bf16 v[8:11], a[40:43], a[140:143], v[8:11]\n"
"ds_read_b64_tr_b16 a[68:69], v238 offset:8\n"
"v_mfma_f32_16x16x32_bf16 v[12:15], a[12:15], a[136:139], v[12:15]\n"
"buffer_load_dwordx4 v245, s[48:51], s61 offen offset:1024 lds\n"
"v_mfma_f32_16x16x32_bf16 v[12:15], a[44:47], a[140:143], v[12:15]\n"
"ds_read_b64_tr_b16 a[70:71], v238 offset:2056\n"
"v_mfma_f32_16x16x32_bf16 v[16:19], a[16:19], a[136:139], v[16:19]\n"
"v_mfma_f32_16x16x32_bf16 v[16:19], a[48:51], a[140:143], v[16:19]\n"
"ds_read_b64_tr_b16 a[72:73], v239 offset:0\n"
"v_mfma_f32_16x16x32_bf16 v[20:23], a[20:23], a[136:139], v[20:23]\n"
"buffer_load_dwordx4 v246, s[48:51], s61 offen offset:2048 lds\n"
"v_mfma_f32_16x16x32_bf16 v[20:23], a[52:55], a[140:143], v[20:23]\n"
"ds_read_b64_tr_b16 a[74:75], v239 offset:2048\n"
"v_mfma_f32_16x16x32_bf16 v[24:27], a[24:27], a[136:139], v[24:27]\n"
"v_mfma_f32_16x16x32_bf16 v[24:27], a[56:59], a[140:143], v[24:27]\n"
"ds_read_b64_tr_b16 a[76:77], v239 offset:8\n"
"v_mfma_f32_16x16x32_bf16 v[28:31], a[28:31], a[136:139], v[28:31]\n"
"buffer_load_dwordx4 v247, s[48:51], s61 offen offset:3072 lds\n"
"v_mfma_f32_16x16x32_bf16 v[28:31], a[60:63], a[140:143], v[28:31]\n"
"ds_read_b64_tr_b16 a[78:79], v239 offset:2056\n"
"v_mfma_f32_16x16x32_bf16 v[32:35], a[0:3], a[144:147], v[32:35]\n"
"v_mfma_f32_16x16x32_bf16 v[32:35], a[32:35], a[148:151], v[32:35]\n"
"ds_read_b64_tr_b16 a[80:81], v240 offset:0\n"
"s_add_u32 m0, s64, 135168\n"
"v_mfma_f32_16x16x32_bf16 v[36:39], a[4:7], a[144:147], v[36:39]\n"
"buffer_load_dwordx4 v244, s[48:51], s62 offen offset:0 lds\n"
"v_mfma_f32_16x16x32_bf16 v[36:39], a[36:39], a[148:151], v[36:39]\n"
"ds_read_b64_tr_b16 a[82:83], v240 offset:2048\n"
"v_mfma_f32_16x16x32_bf16 v[40:43], a[8:11], a[144:147], v[40:43]\n"
"v_mfma_f32_16x16x32_bf16 v[40:43], a[40:43], a[148:151], v[40:43]\n"
"ds_read_b64_tr_b16 a[84:85], v240 offset:8\n"
"v_mfma_f32_16x16x32_bf16 v[44:47], a[12:15], a[144:147], v[44:47]\n"
"buffer_load_dwordx4 v245, s[48:51], s62 offen offset:1024 lds\n"
"v_mfma_f32_16x16x32_bf16 v[44:47], a[44:47], a[148:151], v[44:47]\n"
"ds_read_b64_tr_b16 a[86:87], v240 offset:2056\n"
"v_mfma_f32_16x16x32_bf16 v[48:51], a[16:19], a[144:147], v[48:51]\n"
"v_mfma_f32_16x16x32_bf16 v[48:51], a[48:51], a[148:151], v[48:51]\n"
"ds_read_b64_tr_b16 a[88:89], v241 offset:0\n"
"v_mfma_f32_16x16x32_bf16 v[52:55], a[20:23], a[144:147], v[52:55]\n"
"buffer_load_dwordx4 v246, s[48:51], s62 offen offset:2048 lds\n"
"v_mfma_f32_16x16x32_bf16 v[52:55], a[52:55], a[148:151], v[52:55]\n"
"ds_read_b64_tr_b16 a[90:91], v241 offset:2048\n"
"v_mfma_f32_16x16x32_bf16 v[56:59], a[24:27], a[144:147], v[56:59]\n"
"v_mfma_f32_16x16x32_bf16 v[56:59], a[56:59], a[148:151], v[56:59]\n"
"ds_read_b64_tr_b16 a[92:93], v241 offset:8\n"
"v_mfma_f32_16x16x32_bf16 v[60:63], a[28:31], a[144:147], v[60:63]\n"
"buffer_load_dwordx4 v247, s[48:51], s62 offen offset:3072 lds\n"
"v_mfma_f32_16x16x32_bf16 v[60:63], a[60:63], a[148:151], v[60:63]\n"
"ds_read_b64_tr_b16 a[94:95], v241 offset:2056\n"
"v_mfma_f32_16x16x32_bf16 v[64:67], a[0:3], a[152:155], v[64:67]\n"
"s_add_u32 s61, s61, 524288\n"
"s_add_u32 s62, s62, 524288\n"
"v_mfma_f32_16x16x32_bf16 v[64:67], a[32:35], a[156:159], v[64:67]\n"
"ds_read_b64_tr_b16 a[96:97], v238 offset:16384\n"
"s_add_u32 m0, s63, 106496\n"
"v_mfma_f32_16x16x32_bf16 v[68:71], a[4:7], a[152:155], v[68:71]\n"
"buffer_load_dwordx4 v248, s[44:47], s59 offen offset:0 lds\n"
"v_mfma_f32_16x16x32_bf16 v[68:71], a[36:39], a[156:159], v[68:71]\n"
"ds_read_b64_tr_b16 a[98:99], v238 offset:18432\n"
"v_mfma_f32_16x16x32_bf16 v[72:75], a[8:11], a[152:155], v[72:75]\n"
"v_mfma_f32_16x16x32_bf16 v[72:75], a[40:43], a[156:159], v[72:75]\n"
"ds_read_b64_tr_b16 a[100:101], v238 offset:16392\n"
"v_mfma_f32_16x16x32_bf16 v[76:79], a[12:15], a[152:155], v[76:79]\n"
"buffer_load_dwordx4 v249, s[44:47], s59 offen offset:1024 lds\n"
"v_mfma_f32_16x16x32_bf16 v[76:79], a[44:47], a[156:159], v[76:79]\n"
"ds_read_b64_tr_b16 a[102:103], v238 offset:18440\n"
"v_mfma_f32_16x16x32_bf16 v[80:83], a[16:19], a[152:155], v[80:83]\n"
"v_mfma_f32_16x16x32_bf16 v[80:83], a[48:51], a[156:159], v[80:83]\n"
"ds_read_b64_tr_b16 a[104:105], v239 offset:16384\n"
"v_mfma_f32_16x16x32_bf16 v[84:87], a[20:23], a[152:155], v[84:87]\n"
"buffer_load_dwordx4 v250, s[44:47], s59 offen offset:2048 lds\n"
"v_mfma_f32_16x16x32_bf16 v[84:87], a[52:55], a[156:159], v[84:87]\n"
"ds_read_b64_tr_b16 a[106:107], v239 offset:18432\n"
"v_mfma_f32_16x16x32_bf16 v[88:91], a[24:27], a[152:155], v[88:91]\n"
"v_mfma_f32_16x16x32_bf16 v[88:91], a[56:59], a[156:159], v[88:91]\n"
"ds_read_b64_tr_b16 a[108:109], v239 offset:16392\n"
"v_mfma_f32_16x16x32_bf16 v[92:95], a[28:31], a[152:155], v[92:95]\n"
"buffer_load_dwordx4 v251, s[44:47], s59 offen offset:3072 lds\n"
"v_mfma_f32_16x16x32_bf16 v[92:95], a[60:63], a[156:159], v[92:95]\n"
"ds_read_b64_tr_b16 a[110:111], v239 offset:18440\n"
"v_mfma_f32_16x16x32_bf16 v[96:99], a[0:3], a[160:163], v[96:99]\n"
"v_mfma_f32_16x16x32_bf16 v[96:99], a[32:35], a[164:167], v[96:99]\n"
"ds_read_b64_tr_b16 a[112:113], v240 offset:16384\n"
"s_add_u32 m0, s63, 110592\n"
"v_mfma_f32_16x16x32_bf16 v[100:103], a[4:7], a[160:163], v[100:103]\n"
"buffer_load_dwordx4 v248, s[44:47], s60 offen offset:0 lds\n"
"v_mfma_f32_16x16x32_bf16 v[100:103], a[36:39], a[164:167], v[100:103]\n"
"ds_read_b64_tr_b16 a[114:115], v240 offset:18432\n"
"v_mfma_f32_16x16x32_bf16 v[104:107], a[8:11], a[160:163], v[104:107]\n"
"v_mfma_f32_16x16x32_bf16 v[104:107], a[40:43], a[164:167], v[104:107]\n"
"ds_read_b64_tr_b16 a[116:117], v240 offset:16392\n"
"v_mfma_f32_16x16x32_bf16 v[108:111], a[12:15], a[160:163], v[108:111]\n"
"buffer_load_dwordx4 v249, s[44:47], s60 offen offset:1024 lds\n"
"v_mfma_f32_16x16x32_bf16 v[108:111], a[44:47], a[164:167], v[108:111]\n"
"ds_read_b64_tr_b16 a[118:119], v240 offset:18440\n"
"s_add_u32 s59, s59, 128\n"
"s_add_u32 s60, s60, 128\n"
"v_mfma_f32_16x16x32_bf16 v[112:115], a[16:19], a[160:163], v[112:115]\n"
"v_mfma_f32_16x16x32_bf16 v[112:115], a[48:51], a[164:167], v[112:115]\n"
"ds_read_b64_tr_b16 a[120:121], v241 offset:16384\n"
"v_mfma_f32_16x16x32_bf16 v[116:119], a[20:23], a[160:163], v[116:119]\n"
"v_mfma_f32_16x16x32_bf16 v[116:119], a[52:55], a[164:167], v[116:119]\n"
"ds_read_b64_tr_b16 a[122:123], v241 offset:18432\n"
"v_mfma_f32_16x16x32_bf16 v[120:123], a[24:27], a[160:163], v[120:123]\n"
"v_mfma_f32_16x16x32_bf16 v[120:123], a[56:59], a[164:167], v[120:123]\n"
"ds_read_b64_tr_b16 a[124:125], v241 offset:16392\n"
"v_mfma_f32_16x16x32_bf16 v[124:127], a[28:31], a[160:163], v[124:127]\n"
"v_mfma_f32_16x16x32_bf16 v[124:127], a[60:63], a[164:167], v[124:127]\n"
"ds_read_b64_tr_b16 a[126:127], v241 offset:18440\n"
"v_xor_b32 v238, 0x28000, v238\n"
"v_xor_b32 v239, 0x28000, v239\n"
"v_xor_b32 v240, 0x28000, v240\n"
"v_xor_b32 v241, 0x28000, v241\n"
"v_mfma_f32_16x16x32_bf16 v[128:131], a[0:3], a[168:171], v[128:131]\n"
"v_mfma_f32_16x16x32_bf16 v[128:131], a[32:35], a[172:175], v[128:131]\n"
"ds_read_b128 a[128:131], v242 offset:0\n"
"v_xor_b32 v242, 64, v242\n"
"v_mfma_f32_16x16x32_bf16 v[132:135], a[4:7], a[168:171], v[132:135]\n"
"v_mfma_f32_16x16x32_bf16 v[132:135], a[36:39], a[172:175], v[132:135]\n"
"ds_read_b128 a[132:135], v242 offset:0\n"
"v_xor_b32 v242, 64, v242\n"
"v_mfma_f32_16x16x32_bf16 v[136:139], a[8:11], a[168:171], v[136:139]\n"
"v_mfma_f32_16x16x32_bf16 v[136:139], a[40:43], a[172:175], v[136:139]\n"
"ds_read_b128 a[136:139], v242 offset:2048\n"
"v_xor_b32 v242, 64, v242\n"
"v_mfma_f32_16x16x32_bf16 v[140:143], a[12:15], a[168:171], v[140:143]\n"
"v_mfma_f32_16x16x32_bf16 v[140:143], a[44:47], a[172:175], v[140:143]\n"
"ds_read_b128 a[140:143], v242 offset:2048\n"
"v_xor_b32 v242, 64, v242\n"
"v_mfma_f32_16x16x32_bf16 v[144:147], a[16:19], a[168:171], v[144:147]\n"
"v_mfma_f32_16x16x32_bf16 v[144:147], a[48:51], a[172:175], v[144:147]\n"
"ds_read_b128 a[144:147], v242 offset:4096\n"
"v_xor_b32 v242, 64, v242\n"
"v_mfma_f32_16x16x32_bf16 v[148:151], a[20:23], a[168:171], v[148:151]\n"
"v_mfma_f32_16x16x32_bf16 v[148:151], a[52:55], a[172:175], v[148:151]\n"
"ds_read_b128 a[148:151], v242 offset:4096\n"
"v_xor_b32 v242, 64, v242\n"
"v_mfma_f32_16x16x32_bf16 v[152:155], a[24:27], a[168:171], v[152:155]\n"
"v_mfma_f32_16x16x32_bf16 v[152:155], a[56:59], a[172:175], v[152:155]\n"
"ds_read_b128 a[152:155], v242 offset:6144\n"
"v_xor_b32 v242, 64, v242\n"
"v_mfma_f32_16x16x32_bf16 v[156:159], a[28:31], a[168:171], v[156:159]\n"
"v_mfma_f32_16x16x32_bf16 v[156:159], a[60:63], a[172:175], v[156:159]\n"
"ds_read_b128 a[156:159], v242 offset:6144\n"
"v_xor_b32 v242, 64, v242\n"
"v_mfma_f32_16x16x32_bf16 v[160:163], a[0:3], a[176:179], v[160:163]\n"
"v_mfma_f32_16x16x32_bf16 v[160:163], a[32:35], a[180:183], v[160:163]\n"
"ds_read_b128 a[160:163], v242 offset:8192\n"
"v_xor_b32 v242, 64, v242\n"
"v_mfma_f32_16x16x32_bf16 v[164:167], a[4:7], a[176:179], v[164:167]\n"
"v_mfma_f32_16x16x32_bf16 v[164:167], a[36:39], a[180:183], v[164:167]\n"
"ds_read_b128 a[164:167], v242 offset:8192\n"
"v_xor_b32 v242, 64, v242\n"
"v_mfma_f32_16x16x32_bf16 v[168:171], a[8:11], a[176:179], v[168:171]\n"
"v_mfma_f32_16x16x32_bf16 v[168:171], a[40:43], a[180:183], v[168:171]\n"
"ds_read_b128 a[168:171], v242 offset:10240\n"
"v_xor_b32 v242, 64, v242\n"
"v_mfma_f32_16x16x32_bf16 v[172:175], a[12:15], a[176:179], v[172:175]\n"
"v_mfma_f32_16x16x32_bf16 v[172:175], a[44:47], a[180:183], v[172:175]\n"
"ds_read_b128 a[172:175], v242 offset:10240\n"
"v_xor_b32 v242, 64, v242\n"
"v_mfma_f32_16x16x32_bf16 v[176:179], a[16:19], a[176:179], v[176:179]\n"
"v_mfma_f32_16x16x32_bf16 v[176:179], a[48:51], a[180:183], v[176:179]\n"
"v_mfma_f32_16x16x32_bf16 v[180:183], a[20:23], a[176:179], v[180:183]\n"
"v_mfma_f32_16x16x32_bf16 v[180:183], a[52:55], a[180:183], v[180:183]\n"
"v_mfma_f32_16x16x32_bf16 v[184:187], a[24:27], a[176:179], v[184:187]\n"
"v_mfma_f32_16x16x32_bf16 v[184:187], a[56:59], a[180:183], v[184:187]\n"
"v_mfma_f32_16x16x32_bf16 v[188:191], a[28:31], a[176:179], v[188:191]\n"
"v_mfma_f32_16x16x32_bf16 v[188:191], a[60:63], a[180:183], v[188:191]\n"
"s_cmp_lg_u32 s66, 0\n"
"s_cbranch_scc1 .Lf_nc7\n"
"s_waitcnt vmcnt(0) lgkmcnt(0)\n"
".Lf_nc7:\n"
"s_cmp_eq_u32 s66, 0\n"
"s_cbranch_scc1 .Lf_nw7\n"
"s_waitcnt vmcnt(0) lgkmcnt(0)\n"
".Lf_nw7:\n"
"s_barrier\n"
"v_mfma_f32_16x16x32_bf16 v[0:3], a[64:67], a[128:131], v[0:3]\n"
"v_mfma_f32_16x16x32_bf16 v[0:3], a[96:99], a[132:135], v[0:3]\n"
"ds_read_b64_tr_b16 a[0:1], v238 offset:0\n"
"s_add_u32 m0, s58, 16384\n"
"v_mfma_f32_16x16x32_bf16 v[4:7], a[68:71], a[128:131], v[4:7]\n"
"buffer_load_dwordx4 v236, s[40:43], s56 offen offset:0 nt lds\n"
"v_mfma_f32_16x16x32_bf16 v[4:7], a[100:103], a[132:135], v[4:7]\n"
"ds_read_b64_tr_b16 a[2:3], v238 offset:2048\n"
"v_mfma_f32_16x16x32_bf16 v[8:11], a[72:75], a[128:131], v[8:11]\n"
"v_mfma_f32_16x16x32_bf16 v[8:11], a[104:107], a[132:135], v[8:11]\n"
"ds_read_b64_tr_b16 a[4:5], v238 offset:8\n"
"v_mfma_f32_16x16x32_bf16 v[12:15], a[76:79], a[128:131], v[12:15]\n"
"buffer_load_dwordx4 v236, s[40:43], s56 offen offset:1024 nt lds\n"
"v_mfma_f32_16x16x32_bf16 v[12:15], a[108:111], a[132:135], v[12:15]\n"
"ds_read_b64_tr_b16 a[6:7], v238 offset:2056\n"
"v_mfma_f32_16x16x32_bf16 v[16:19], a[80:83], a[128:131], v[16:19]\n"
"v_mfma_f32_16x16x32_bf16 v[16:19], a[112:115], a[132:135], v[16:19]\n"
"ds_read_b64_tr_b16 a[8:9], v239 offset:0\n"
"v_mfma_f32_16x16x32_bf16 v[20:23], a[84:87], a[128:131], v[20:23]\n"
"buffer_load_dwordx4 v237, s[40:43], s56 offen offset:2048 nt lds\n"
"v_mfma_f32_16x16x32_bf16 v[20:23], a[116:119], a[132:135], v[20:23]\n"
"ds_read_b64_tr_b16 a[10:11], v239 offset:2048\n"
"v_mfma_f32_16x16x32_bf16 v[24:27], a[88:91], a[128:131], v[24:27]\n"
"v_mfma_f32_16x16x32_bf16 v[24:27], a[120:123], a[132:135], v[24:27]\n"
"ds_read_b64_tr_b16 a[12:13], v239 offset:8\n"
"v_mfma_f32_16x16x32_bf16 v[28:31], a[92:95], a[128:131], v[28:31]\n"
"buffer_load_dwordx4 v237, s[40:43], s56 offen offset:3072 nt lds\n"
"v_mfma_f32_16x16x32_bf16 v[28:31], a[124:127], a[132:135], v[28:31]\n"
"ds_read_b64_tr_b16 a[14:15], v239 offset:2056\n"
"v_mfma_f32_16x16x32_bf16 v[32:35], a[64:67], a[136:139], v[32:35]\n"
"s_add_u32 s56, s56, 8192\n"
"s_and_b32 s56, s56, 0x1ffff\n"
"v_mfma_f32_16x16x32_bf16 v[32:35], a[96:99], a[140:143], v[32:35]\n"
"ds_read_b64_tr_b16 a[16:17], v240 offset:0\n"
"s_add_u32 m0, s57, 32768\n"
"v_mfma_f32_16x16x32_bf16 v[36:39], a[68:71], a[136:139], v[36:39]\n"
"buffer_load_dwordx4 v234, s[36:39], s56 offen offset:0 nt lds\n"
"v_mfma_f32_16x16x32_bf16 v[36:39], a[100:103], a[140:143], v[36:39]\n"
"ds_read_b64_tr_b16 a[18:19], v240 offset:2048\n"
"v_mfma_f32_16x16x32_bf16 v[40:43], a[72:75], a[136:139], v[40:43]\n"
"v_mfma_f32_16x16x32_bf16 v[40:43], a[104:107], a[140:143], v[40:43]\n"
"ds_read_b64_tr_b16 a[20:21], v240 offset:8\n"
"v_mfma_f32_16x16x32_bf16 v[44:47], a[76:79], a[136:139], v[44:47]\n"
"buffer_load_dwordx4 v234, s[36:39], s56 offen offset:1024 nt lds\n"
"v_mfma_f32_16x16x32_bf16 v[44:47], a[108:111], a[140:143], v[44:47]\n"
"ds_read_b64_tr_b16 a[22:23], v240 offset:2056\n"
"v_mfma_f32_16x16x32_bf16 v[48:51], a[80:83], a[136:139], v[48:51]\n"
"v_mfma_f32_16x16x32_bf16 v[48:51], a[112:115], a[140:143], v[48:51]\n"
"ds_read_b64_tr_b16 a[24:25], v241 offset:0\n"
"v_mfma_f32_16x16x32_bf16 v[52:55], a[84:87], a[136:139], v[52:55]\n"
"buffer_load_dwordx4 v235, s[36:39], s56 offen offset:2048 nt lds\n"
"v_mfma_f32_16x16x32_bf16 v[52:55], a[116:119], a[140:143], v[52:55]\n"
"ds_read_b64_tr_b16 a[26:27], v241 offset:2048\n"
"v_mfma_f32_16x16x32_bf16 v[56:59], a[88:91], a[136:139], v[56:59]\n"
"v_mfma_f32_16x16x32_bf16 v[56:59], a[120:123], a[140:143], v[56:59]\n"
"ds_read_b64_tr_b16 a[28:29], v241 offset:8\n"
"v_mfma_f32_16x16x32_bf16 v[60:63], a[92:95], a[136:139], v[60:63]\n"
"buffer_load_dwordx4 v235, s[36:39], s56 offen offset:3072 nt lds\n"
"v_mfma_f32_16x16x32_bf16 v[60:63], a[124:127], a[140:143], v[60:63]\n"
"ds_read_b64_tr_b16 a[30:31], v241 offset:2056\n"
"v_mfma_f32_16x16x32_bf16 v[64:67], a[64:67], a[144:147], v[64:67]\n"
"s_add_u32 s56, s56, 8192\n"
"s_and_b32 s56, s56, 0x1ffff\n"
"v_mfma_f32_16x16x32_bf16 v[64:67], a[96:99], a[148:151], v[64:67]\n"
"ds_read_b64_tr_b16 a[32:33], v238 offset:16384\n"
"s_add_u32 m0, s57, 49152\n"
"v_mfma_f32_16x16x32_bf16 v[68:71], a[68:71], a[144:147], v[68:71]\n"
"buffer_load_dwordx4 v234, s[36:39], s56 offen offset:0 nt lds\n"
"v_mfma_f32_16x16x32_bf16 v[68:71], a[100:103], a[148:151], v[68:71]\n"
"ds_read_b64_tr_b16 a[34:35], v238 offset:18432\n"
"v_mfma_f32_16x16x32_bf16 v[72:75], a[72:75], a[144:147], v[72:75]\n"
"v_mfma_f32_16x16x32_bf16 v[72:75], a[104:107], a[148:151], v[72:75]\n"
"ds_read_b64_tr_b16 a[36:37], v238 offset:16392\n"
"v_mfma_f32_16x16x32_bf16 v[76:79], a[76:79], a[144:147], v[76:79]\n"
"buffer_load_dwordx4 v234, s[36:39], s56 offen offset:1024 nt lds\n"
"v_mfma_f32_16x16x32_bf16 v[76:79], a[108:111], a[148:151], v[76:79]\n"
"ds_read_b64_tr_b16 a[38:39], v238 offset:18440\n"
"v_mfma_f32_16x16x32_bf16 v[80:83], a[80:83], a[144:147], v[80:83]\n"
"v_mfma_f32_16x16x32_bf16 v[80:83], a[112:115], a[148:151], v[80:83]\n"
"ds_read_b64_tr_b16 a[40:41], v239 offset:16384\n"
"v_mfma_f32_16x16x32_bf16 v[84:87], a[84:87], a[144:147], v[84:87]\n"
"buffer_load_dwordx4 v235, s[36:39], s56 offen offset:2048 nt lds\n"
"v_mfma_f32_16x16x32_bf16 v[84:87], a[116:119], a[148:151], v[84:87]\n"
"ds_read_b64_tr_b16 a[42:43], v239 offset:18432\n"
"v_mfma_f32_16x16x32_bf16 v[88:91], a[88:91], a[144:147], v[88:91]\n"
"v_mfma_f32_16x16x32_bf16 v[88:91], a[120:123], a[148:151], v[88:91]\n"
"ds_read_b64_tr_b16 a[44:45], v239 offset:16392\n"
"v_mfma_f32_16x16x32_bf16 v[92:95], a[92:95], a[144:147], v[92:95]\n"
"buffer_load_dwordx4 v235, s[36:39], s56 offen offset:3072 nt lds\n"
"v_mfma_f32_16x16x32_bf16 v[92:95], a[124:127], a[148:151], v[92:95]\n"
"ds_read_b64_tr_b16 a[46:47], v239 offset:18440\n"
"s_add_u32 s56, s56, 0x1e000\n"
"s_and_b32 s56, s56, 0x1ffff\n"
"v_mfma_f32_16x16x32_bf16 v[96:99], a[64:67], a[152:155], v[96:99]\n"
"v_mfma_f32_16x16x32_bf16 v[96:99], a[96:99], a[156:159], v[96:99]\n"
"ds_read_b64_tr_b16 a[48:49], v240 offset:16384\n"
"v_mfma_f32_16x16x32_bf16 v[100:103], a[68:71], a[152:155], v[100:103]\n"
"v_mfma_f32_16x16x32_bf16 v[100:103], a[100:103], a[156:159], v[100:103]\n"
"ds_read_b64_tr_b16 a[50:51], v240 offset:18432\n"
"v_mfma_f32_16x16x32_bf16 v[104:107], a[72:75], a[152:155], v[104:107]\n"
"v_mfma_f32_16x16x32_bf16 v[104:107], a[104:107], a[156:159], v[104:107]\n"
"ds_read_b64_tr_b16 a[52:53], v240 offset:16392\n"
"v_mfma_f32_16x16x32_bf16 v[108:111], a[76:79], a[152:155], v[108:111]\n"
"v_mfma_f32_16x16x32_bf16 v[108:111], a[108:111], a[156:159], v[108:111]\n"
"ds_read_b64_tr_b16 a[54:55], v240 offset:18440\n"
"v_mfma_f32_16x16x32_bf16 v[112:115], a[80:83], a[152:155], v[112:115]\n"
"v_mfma_f32_16x16x32_bf16 v[112:115], a[112:115], a[156:159], v[112:115]\n"
"ds_read_b64_tr_b16 a[56:57], v241 offset:16384\n"
"v_mfma_f32_16x16x32_bf16 v[116:119], a[84:87], a[152:155], v[116:119]\n"
"v_mfma_f32_16x16x32_bf16 v[116:119], a[116:119], a[156:159], v[116:119]\n"
"ds_read_b64_tr_b16 a[58:59], v241 offset:18432\n"
"v_mfma_f32_16x16x32_bf16 v[120:123], a[88:91], a[152:155], v[120:123]\n"
"v_mfma_f32_16x16x32_bf16 v[120:123], a[120:123], a[156:159], v[120:123]\n"
"ds_read_b64_tr_b16 a[60:61], v241 offset:16392\n"
"v_mfma_f32_16x16x32_bf16 v[124:127], a[92:95], a[152:155], v[124:127]\n"
"v_mfma_f32_16x16x32_bf16 v[124:127], a[124:127], a[156:159], v[124:127]\n"
"ds_read_b64_tr_b16 a[62:63], v241 offset:18440\n"
"v_xor_b32 v238, 0x28000, v238\n"
"v_xor_b32 v239, 0x28000, v239\n"
"v_xor_b32 v240, 0x28000, v240\n"
"v_xor_b32 v241, 0x28000, v241\n"
"v_mfma_f32_16x16x32_bf16 v[128:131], a[64:67], a[160:163], v[128:131]\n"
"v_mfma_f32_16x16x32_bf16 v[128:131], a[96:99], a[164:167], v[128:131]\n"
"ds_read_b128 a[176:179], v242 offset:24576\n"
"v_xor_b32 v242, 64, v242\n"
"v_mfma_f32_16x16x32_bf16 v[132:135], a[68:71], a[160:163], v[132:135]\n"
"v_mfma_f32_16x16x32_bf16 v[132:135], a[100:103], a[164:167], v[132:135]\n"
"ds_read_b128 a[180:183], v242 offset:24576\n"
"v_xor_b32 v242, 64, v242\n"
"v_mfma_f32_16x16x32_bf16 v[136:139], a[72:75], a[160:163], v[136:139]\n"
"v_mfma_f32_16x16x32_bf16 v[136:139], a[104:107], a[164:167], v[136:139]\n"
"ds_read_b128 a[128:131], v242 offset:26624\n"
"v_xor_b32 v242, 64, v242\n"
"v_mfma_f32_16x16x32_bf16 v[140:143], a[76:79], a[160:163], v[140:143]\n"
"v_mfma_f32_16x16x32_bf16 v[140:143], a[108:111], a[164:167], v[140:143]\n"
"ds_read_b128 a[132:135], v242 offset:26624\n"
"v_xor_b32 v242, 64, v242\n"
"v_mfma_f32_16x16x32_bf16 v[144:147], a[80:83], a[160:163], v[144:147]\n"
"v_mfma_f32_16x16x32_bf16 v[144:147], a[112:115], a[164:167], v[144:147]\n"
"ds_read_b128 a[136:139], v242 offset:28672\n"
"v_xor_b32 v242, 64, v242\n"
"v_mfma_f32_16x16x32_bf16 v[148:151], a[84:87], a[160:163], v[148:151]\n"
"v_mfma_f32_16x16x32_bf16 v[148:151], a[116:119], a[164:167], v[148:151]\n"
"ds_read_b128 a[140:143], v242 offset:28672\n"
"v_xor_b32 v242, 64, v242\n"
"v_mfma_f32_16x16x32_bf16 v[152:155], a[88:91], a[160:163], v[152:155]\n"
"v_mfma_f32_16x16x32_bf16 v[152:155], a[120:123], a[164:167], v[152:155]\n"
"ds_read_b128 a[144:147], v242 offset:30720\n"
"v_xor_b32 v242, 64, v242\n"
"v_mfma_f32_16x16x32_bf16 v[156:159], a[92:95], a[160:163], v[156:159]\n"
"v_mfma_f32_16x16x32_bf16 v[156:159], a[124:127], a[164:167], v[156:159]\n"
"ds_read_b128 a[148:151], v242 offset:30720\n"
"v_xor_b32 v242, 64, v242\n"
"v_mfma_f32_16x16x32_bf16 v[160:163], a[64:67], a[168:171], v[160:163]\n"
"v_mfma_f32_16x16x32_bf16 v[160:163], a[96:99], a[172:175], v[160:163]\n"
"ds_read_b128 a[152:155], v242 offset:32768\n"
"v_xor_b32 v242, 64, v242\n"
"v_mfma_f32_16x16x32_bf16 v[164:167], a[68:71], a[168:171], v[164:167]\n"
"v_mfma_f32_16x16x32_bf16 v[164:167], a[100:103], a[172:175], v[164:167]\n"
"ds_read_b128 a[156:159], v242 offset:32768\n"
"v_xor_b32 v242, 64, v242\n"
"v_mfma_f32_16x16x32_bf16 v[168:171], a[72:75], a[168:171], v[168:171]\n"
"v_mfma_f32_16x16x32_bf16 v[168:171], a[104:107], a[172:175], v[168:171]\n"
"ds_read_b128 a[160:163], v242 offset:34816\n"
"v_xor_b32 v242, 64, v242\n"
"v_mfma_f32_16x16x32_bf16 v[172:175], a[76:79], a[168:171], v[172:175]\n"
"v_mfma_f32_16x16x32_bf16 v[172:175], a[108:111], a[172:175], v[172:175]\n"
"ds_read_b128 a[164:167], v242 offset:34816\n"
"v_xor_b32 v242, 64, v242\n"
"v_mfma_f32_16x16x32_bf16 v[176:179], a[80:83], a[168:171], v[176:179]\n"
"v_mfma_f32_16x16x32_bf16 v[176:179], a[112:115], a[172:175], v[176:179]\n"
"v_mfma_f32_16x16x32_bf16 v[180:183], a[84:87], a[168:171], v[180:183]\n"
"v_mfma_f32_16x16x32_bf16 v[180:183], a[116:119], a[172:175], v[180:183]\n"
"v_mfma_f32_16x16x32_bf16 v[184:187], a[88:91], a[168:171], v[184:187]\n"
"v_mfma_f32_16x16x32_bf16 v[184:187], a[120:123], a[172:175], v[184:187]\n"
"v_mfma_f32_16x16x32_bf16 v[188:191], a[92:95], a[168:171], v[188:191]\n"
"v_mfma_f32_16x16x32_bf16 v[188:191], a[124:127], a[172:175], v[188:191]\n"
"s_waitcnt lgkmcnt(0)\n"
"s_barrier\n"
"v_mfma_f32_16x16x32_bf16 v[0:3], a[0:3], a[176:179], v[0:3]\n"
"v_mfma_f32_16x16x32_bf16 v[0:3], a[32:35], a[180:183], v[0:3]\n"
"ds_read_b128 a[192:195], v222 offset:0\n"
"s_add_u32 m0, s58, 32768\n"
"v_mfma_f32_16x16x32_bf16 v[4:7], a[4:7], a[176:179], v[4:7]\n"
"buffer_load_dwordx4 v236, s[40:43], s56 offen offset:0 nt lds\n"
"v_mfma_f32_16x16x32_bf16 v[4:7], a[36:39], a[180:183], v[4:7]\n"
"ds_read_b128 a[196:199], v223 offset:0\n"
"v_mfma_f32_16x16x32_bf16 v[8:11], a[8:11], a[176:179], v[8:11]\n"
"v_mfma_f32_16x16x32_bf16 v[8:11], a[40:43], a[180:183], v[8:11]\n"
"ds_read_b128 a[200:203], v224 offset:0\n"
"v_mfma_f32_16x16x32_bf16 v[12:15], a[12:15], a[176:179], v[12:15]\n"
"buffer_load_dwordx4 v236, s[40:43], s56 offen offset:1024 nt lds\n"
"v_mfma_f32_16x16x32_bf16 v[12:15], a[44:47], a[180:183], v[12:15]\n"
"ds_read_b128 a[204:207], v225 offset:0\n"
"v_mfma_f32_16x16x32_bf16 v[16:19], a[16:19], a[176:179], v[16:19]\n"
"v_mfma_f32_16x16x32_bf16 v[16:19], a[48:51], a[180:183], v[16:19]\n"
"ds_read_b128 a[208:211], v222 offset:1024\n"
"v_mfma_f32_16x16x32_bf16 v[20:23], a[20:23], a[176:179], v[20:23]\n"
"buffer_load_dwordx4 v237, s[40:43], s56 offen offset:2048 nt lds\n"
"v_mfma_f32_16x16x32_bf16 v[20:23], a[52:55], a[180:183], v[20:23]\n"
"ds_read_b128 a[212:215], v223 offset:1024\n"
"v_mfma_f32_16x16x32_bf16 v[24:27], a[24:27], a[176:179], v[24:27]\n"
"v_mfma_f32_16x16x32_bf16 v[24:27], a[56:59], a[180:183], v[24:27]\n"
"ds_read_b128 a[216:219], v224 offset:1024\n"
"v_mfma_f32_16x16x32_bf16 v[28:31], a[28:31], a[176:179], v[28:31]\n"
"buffer_load_dwordx4 v237, s[40:43], s56 offen offset:3072 nt lds\n"
"v_mfma_f32_16x16x32_bf16 v[28:31], a[60:63], a[180:183], v[28:31]\n"
"ds_read_b128 a[220:223], v225 offset:1024\n"
"v_mfma_f32_16x16x32_bf16 v[32:35], a[0:3], a[128:131], v[32:35]\n"
"s_add_u32 s56, s56, 8192\n"
"s_and_b32 s56, s56, 0x1ffff\n"
"v_mfma_f32_16x16x32_bf16 v[32:35], a[32:35], a[132:135], v[32:35]\n"
"ds_read_b64_tr_b16 a[224:225], v226 offset:0\n"
"s_add_u32 m0, s58, 49152\n"
"v_mfma_f32_16x16x32_bf16 v[36:39], a[4:7], a[128:131], v[36:39]\n"
"buffer_load_dwordx4 v236, s[40:43], s56 offen offset:0 nt lds\n"
"v_mfma_f32_16x16x32_bf16 v[36:39], a[36:39], a[132:135], v[36:39]\n"
"ds_read_b64_tr_b16 a[226:227], v226 offset:1024\n"
"v_mul_f32 v0, s68, v0\n"
"v_mul_f32 v1, s68, v1\n"
"v_mul_f32 v2, s68, v2\n"
"v_mul_f32 v3, s68, v3\n"
"v_mfma_f32_16x16x32_bf16 v[40:43], a[8:11], a[128:131], v[40:43]\n"
"v_mul_f32 v4, s68, v4\n"
"v_mul_f32 v5, s68, v5\n"
"v_mul_f32 v6, s68, v6\n"
"v_mul_f32 v7, s68, v7\n"
"v_mfma_f32_16x16x32_bf16 v[40:43], a[40:43], a[132:135], v[40:43]\n"
"ds_read_b64_tr_b16 a[228:229], v227 offset:0\n"
"v_cvt_pk_bf16_f32 v0, v0, v1\n"
"v_cvt_pk_bf16_f32 v1, v2, v3\n"
"v_cvt_pk_bf16_f32 v2, v4, v5\n"
"v_cvt_pk_bf16_f32 v3, v6, v7\n"
"v_mfma_f32_16x16x32_bf16 v[44:47], a[12:15], a[128:131], v[44:47]\n"
"buffer_load_dwordx4 v236, s[40:43], s56 offen offset:1024 nt lds\n"
"v_mul_f32 v8, s68, v8\n"
"v_mul_f32 v9, s68, v9\n"
"v_mul_f32 v10, s68, v10\n"
"v_mfma_f32_16x16x32_bf16 v[44:47], a[44:47], a[132:135], v[44:47]\n"
"ds_read_b64_tr_b16 a[230:231], v227 offset:1024\n"
"v_mul_f32 v11, s68, v11\n"
"v_mul_f32 v12, s68, v12\n"
"v_mul_f32 v13, s68, v13\n"
"v_mul_f32 v14, s68, v14\n"
"v_mfma_f32_16x16x32_bf16 v[48:51], a[16:19], a[128:131], v[48:51]\n"
"v_mul_f32 v15, s68, v15\n"
"v_cvt_pk_bf16_f32 v4, v8, v9\n"
"v_cvt_pk_bf16_f32 v5, v10, v11\n"
"v_cvt_pk_bf16_f32 v6, v12, v13\n"
"v_mfma_f32_16x16x32_bf16 v[48:51], a[48:51], a[132:135], v[48:51]\n"
"ds_read_b64_tr_b16 a[232:233], v228 offset:0\n"
"v_cvt_pk_bf16_f32 v7, v14, v15\n"
"v_mul_f32 v16, s68, v16\n"
"v_mul_f32 v17, s68, v17\n"
"v_mfma_f32_16x16x32_bf16 v[52:55], a[20:23], a[128:131], v[52:55]\n"
"buffer_load_dwordx4 v237, s[40:43], s56 offen offset:2048 nt lds\n"
"v_mul_f32 v18, s68, v18\n"
"v_mul_f32 v19, s68, v19\n"
"v_mul_f32 v20, s68, v20\n"
"v_mul_f32 v21, s68, v21\n"
"v_mfma_f32_16x16x32_bf16 v[52:55], a[52:55], a[132:135], v[52:55]\n"
"ds_read_b64_tr_b16 a[234:235], v228 offset:1024\n"
"v_mul_f32 v22, s68, v22\n"
"v_mul_f32 v23, s68, v23\n"
"v_cvt_pk_bf16_f32 v8, v16, v17\n"
"v_cvt_pk_bf16_f32 v9, v18, v19\n"
"v_mfma_f32_16x16x32_bf16 v[56:59], a[24:27], a[128:131], v[56:59]\n"
"v_cvt_pk_bf16_f32 v10, v20, v21\n"
"v_cvt_pk_bf16_f32 v11, v22, v23\n"
"v_mul_f32 v24, s68, v24\n"
"v_mfma_f32_16x16x32_bf16 v[56:59], a[56:59], a[132:135], v[56:59]\n"
"ds_read_b64_tr_b16 a[236:237], v229 offset:0\n"
"v_mul_f32 v25, s68, v25\n"
"v_mul_f32 v26, s68, v26\n"
"v_mul_f32 v27, s68, v27\n"
"v_mul_f32 v28, s68, v28\n"
"v_mfma_f32_16x16x32_bf16 v[60:63], a[28:31], a[128:131], v[60:63]\n"
"buffer_load_dwordx4 v237, s[40:43], s56 offen offset:3072 nt lds\n"
"v_mul_f32 v29, s68, v29\n"
"v_mul_f32 v30, s68, v30\n"
"v_mul_f32 v31, s68, v31\n"
"v_cvt_pk_bf16_f32 v12, v24, v25\n"
"v_mfma_f32_16x16x32_bf16 v[60:63], a[60:63], a[132:135], v[60:63]\n"
"ds_read_b64_tr_b16 a[238:239], v229 offset:1024\n"
"s_add_u32 s56, s56, 8192\n"
"s_and_b32 s56, s56, 0x1ffff\n"
"v_cvt_pk_bf16_f32 v13, v26, v27\n"
"v_cvt_pk_bf16_f32 v14, v28, v29\n"
"v_cvt_pk_bf16_f32 v15, v30, v31\n"
"v_mfma_f32_16x16x32_bf16 v[64:67], a[0:3], a[136:139], v[64:67]\n"
"v_mfma_f32_16x16x32_bf16 v[64:67], a[32:35], a[140:143], v[64:67]\n"
"ds_read_b64_tr_b16 a[240:241], v230 offset:0\n"
"v_mfma_f32_16x16x32_bf16 v[68:71], a[4:7], a[136:139], v[68:71]\n"
"v_mfma_f32_16x16x32_bf16 v[68:71], a[36:39], a[140:143], v[68:71]\n"
"ds_read_b64_tr_b16 a[242:243], v230 offset:1024\n"
"v_mul_f32 v32, s68, v32\n"
"v_mul_f32 v33, s68, v33\n"
"v_mul_f32 v34, s68, v34\n"
"v_mul_f32 v35, s68, v35\n"
"v_mfma_f32_16x16x32_bf16 v[72:75], a[8:11], a[136:139], v[72:75]\n"
"v_mul_f32 v36, s68, v36\n"
"v_mul_f32 v37, s68, v37\n"
"v_mul_f32 v38, s68, v38\n"
"v_mul_f32 v39, s68, v39\n"
"v_mfma_f32_16x16x32_bf16 v[72:75], a[40:43], a[140:143], v[72:75]\n"
"ds_read_b64_tr_b16 a[244:245], v231 offset:0\n"
"v_cvt_pk_bf16_f32 v16, v32, v33\n"
"v_cvt_pk_bf16_f32 v17, v34, v35\n"
"v_cvt_pk_bf16_f32 v18, v36, v37\n"
"v_cvt_pk_bf16_f32 v19, v38, v39\n"
"v_mfma_f32_16x16x32_bf16 v[76:79], a[12:15], a[136:139], v[76:79]\n"
"v_mul_f32 v40, s68, v40\n"
"v_mul_f32 v41, s68, v41\n"
"v_mul_f32 v42, s68, v42\n"
"v_mfma_f32_16x16x32_bf16 v[76:79], a[44:47], a[140:143], v[76:79]\n"
"ds_read_b64_tr_b16 a[246:247], v231 offset:1024\n"
"v_mul_f32 v43, s68, v43\n"
"v_mul_f32 v44, s68, v44\n"
"v_mul_f32 v45, s68, v45\n"
"v_mul_f32 v46, s68, v46\n"
"v_mfma_f32_16x16x32_bf16 v[80:83], a[16:19], a[136:139], v[80:83]\n"
"v_mul_f32 v47, s68, v47\n"
"v_cvt_pk_bf16_f32 v20, v40, v41\n"
"v_cvt_pk_bf16_f32 v21, v42, v43\n"
"v_cvt_pk_bf16_f32 v22, v44, v45\n"
"v_mfma_f32_16x16x32_bf16 v[80:83], a[48:51], a[140:143], v[80:83]\n"
"ds_read_b64_tr_b16 a[248:249], v232 offset:0\n"
"v_cvt_pk_bf16_f32 v23, v46, v47\n"
"v_mul_f32 v48, s68, v48\n"
"v_mul_f32 v49, s68, v49\n"
"v_mfma_f32_16x16x32_bf16 v[84:87], a[20:23], a[136:139], v[84:87]\n"
"v_mul_f32 v50, s68, v50\n"
"v_mul_f32 v51, s68, v51\n"
"v_mul_f32 v52, s68, v52\n"
"v_mul_f32 v53, s68, v53\n"
"v_mfma_f32_16x16x32_bf16 v[84:87], a[52:55], a[140:143], v[84:87]\n"
"ds_read_b64_tr_b16 a[250:251], v232 offset:1024\n"
"v_mul_f32 v54, s68, v54\n"
"v_mul_f32 v55, s68, v55\n"
"v_cvt_pk_bf16_f32 v24, v48, v49\n"
"v_cvt_pk_bf16_f32 v25, v50, v51\n"
"v_mfma_f32_16x16x32_bf16 v[88:91], a[24:27], a[136:139], v[88:91]\n"
"v_cvt_pk_bf16_f32 v26, v52, v53\n"
"v_cvt_pk_bf16_f32 v27, v54, v55\n"
"v_mul_f32 v56, s68, v56\n"
"v_mfma_f32_16x16x32_bf16 v[88:91], a[56:59], a[140:143], v[88:91]\n"
"ds_read_b64_tr_b16 a[252:253], v233 offset:0\n"
"v_mul_f32 v57, s68, v57\n"
"v_mul_f32 v58, s68, v58\n"
"v_mul_f32 v59, s68, v59\n"
"v_mul_f32 v60, s68, v60\n"
"v_mfma_f32_16x16x32_bf16 v[92:95], a[28:31], a[136:139], v[92:95]\n"
"v_mul_f32 v61, s68, v61\n"
"v_mul_f32 v62, s68, v62\n"
"v_mul_f32 v63, s68, v63\n"
"v_cvt_pk_bf16_f32 v28, v56, v57\n"
"v_mfma_f32_16x16x32_bf16 v[92:95], a[60:63], a[140:143], v[92:95]\n"
"ds_read_b64_tr_b16 a[254:255], v233 offset:1024\n"
"v_cvt_pk_bf16_f32 v29, v58, v59\n"
"v_cvt_pk_bf16_f32 v30, v60, v61\n"
"v_cvt_pk_bf16_f32 v31, v62, v63\n"
"v_mfma_f32_16x16x32_bf16 v[96:99], a[0:3], a[144:147], v[96:99]\n"
"v_mfma_f32_16x16x32_bf16 v[96:99], a[32:35], a[148:151], v[96:99]\n"
"v_mfma_f32_16x16x32_bf16 v[100:103], a[4:7], a[144:147], v[100:103]\n"
"v_mfma_f32_16x16x32_bf16 v[100:103], a[36:39], a[148:151], v[100:103]\n"
"v_mul_f32 v64, s68, v64\n"
"v_mul_f32 v65, s68, v65\n"
"v_mul_f32 v66, s68, v66\n"
"v_mul_f32 v67, s68, v67\n"
"v_mfma_f32_16x16x32_bf16 v[104:107], a[8:11], a[144:147], v[104:107]\n"
"v_mul_f32 v68, s68, v68\n"
"v_mul_f32 v69, s68, v69\n"
"v_mul_f32 v70, s68, v70\n"
"v_mul_f32 v71, s68, v71\n"
"v_mfma_f32_16x16x32_bf16 v[104:107], a[40:43], a[148:151], v[104:107]\n"
"v_cvt_pk_bf16_f32 v32, v64, v65\n"
"v_cvt_pk_bf16_f32 v33, v66, v67\n"
"v_cvt_pk_bf16_f32 v34, v68, v69\n"
"v_cvt_pk_bf16_f32 v35, v70, v71\n"
"v_mfma_f32_16x16x32_bf16 v[108:111], a[12:15], a[144:147], v[108:111]\n"
"v_mul_f32 v72, s68, v72\n"
"v_mul_f32 v73, s68, v73\n"
"v_mul_f32 v74, s68, v74\n"
"v_mfma_f32_16x16x32_bf16 v[108:111], a[44:47], a[148:151], v[108:111]\n"
"v_mul_f32 v75, s68, v75\n"
"v_mul_f32 v76, s68, v76\n"
"v_mul_f32 v77, s68, v77\n"
"v_mul_f32 v78, s68, v78\n"
"v_mfma_f32_16x16x32_bf16 v[112:115], a[16:19], a[144:147], v[112:115]\n"
"v_mul_f32 v79, s68, v79\n"
"v_cvt_pk_bf16_f32 v36, v72, v73\n"
"v_cvt_pk_bf16_f32 v37, v74, v75\n"
"v_cvt_pk_bf16_f32 v38, v76, v77\n"
"v_mfma_f32_16x16x32_bf16 v[112:115], a[48:51], a[148:151], v[112:115]\n"
"v_cvt_pk_bf16_f32 v39, v78, v79\n"
"v_mul_f32 v80, s68, v80\n"
"v_mul_f32 v81, s68, v81\n"
"v_mfma_f32_16x16x32_bf16 v[116:119], a[20:23], a[144:147], v[116:119]\n"
"v_mul_f32 v82, s68, v82\n"
"v_mul_f32 v83, s68, v83\n"
"v_mul_f32 v84, s68, v84\n"
"v_mul_f32 v85, s68, v85\n"
"v_mfma_f32_16x16x32_bf16 v[116:119], a[52:55], a[148:151], v[116:119]\n"
"v_mul_f32 v86, s68, v86\n"
"v_mul_f32 v87, s68, v87\n"
"v_cvt_pk_bf16_f32 v40, v80, v81\n"
"v_cvt_pk_bf16_f32 v41, v82, v83\n"
"v_mfma_f32_16x16x32_bf16 v[120:123], a[24:27], a[144:147], v[120:123]\n"
"v_cvt_pk_bf16_f32 v42, v84, v85\n"
"v_cvt_pk_bf16_f32 v43, v86, v87\n"
"v_mul_f32 v88, s68, v88\n"
"v_mfma_f32_16x16x32_bf16 v[120:123], a[56:59], a[148:151], v[120:123]\n"
"v_mul_f32 v89, s68, v89\n"
"v_mul_f32 v90, s68, v90\n"
"v_mul_f32 v91, s68, v91\n"
"v_mul_f32 v92, s68, v92\n"
"v_mfma_f32_16x16x32_bf16 v[124:127], a[28:31], a[144:147], v[124:127]\n"
"v_mul_f32 v93, s68, v93\n"
"v_mul_f32 v94, s68, v94\n"
"v_mul_f32 v95, s68, v95\n"
"v_cvt_pk_bf16_f32 v44, v88, v89\n"
"v_mfma_f32_16x16x32_bf16 v[124:127], a[60:63], a[148:151], v[124:127]\n"
"v_cvt_pk_bf16_f32 v45, v90, v91\n"
"v_cvt_pk_bf16_f32 v46, v92, v93\n"
"v_cvt_pk_bf16_f32 v47, v94, v95\n"
"v_mfma_f32_16x16x32_bf16 v[128:131], a[0:3], a[152:155], v[128:131]\n"
"v_mfma_f32_16x16x32_bf16 v[128:131], a[32:35], a[156:159], v[128:131]\n"
"v_mfma_f32_16x16x32_bf16 v[132:135], a[4:7], a[152:155], v[132:135]\n"
"v_mfma_f32_16x16x32_bf16 v[132:135], a[36:39], a[156:159], v[132:135]\n"
"v_mul_f32 v96, s68, v96\n"
"v_mul_f32 v97, s68, v97\n"
"v_mul_f32 v98, s68, v98\n"
"v_mul_f32 v99, s68, v99\n"
"v_mfma_f32_16x16x32_bf16 v[136:139], a[8:11], a[152:155], v[136:139]\n"
"v_mul_f32 v100, s68, v100\n"
"v_mul_f32 v101, s68, v101\n"
"v_mul_f32 v102, s68, v102\n"
"v_mul_f32 v103, s68, v103\n"
"v_mfma_f32_16x16x32_bf16 v[136:139], a[40:43], a[156:159], v[136:139]\n"
"v_cvt_pk_bf16_f32 v48, v96, v97\n"
"v_cvt_pk_bf16_f32 v49, v98, v99\n"
"v_cvt_pk_bf16_f32 v50, v100, v101\n"
"v_cvt_pk_bf16_f32 v51, v102, v103\n"
"v_mfma_f32_16x16x32_bf16 v[140:143], a[12:15], a[152:155], v[140:143]\n"
"v_mul_f32 v104, s68, v104\n"
"v_mul_f32 v105, s68, v105\n"
"v_mul_f32 v106, s68, v106\n"
"v_mfma_f32_16x16x32_bf16 v[140:143], a[44:47], a[156:159], v[140:143]\n"
"v_mul_f32 v107, s68, v107\n"
"v_mul_f32 v108, s68, v108\n"
"v_mul_f32 v109, s68, v109\n"
"v_mul_f32 v110, s68, v110\n"
"v_mfma_f32_16x16x32_bf16 v[144:147], a[16:19], a[152:155], v[144:147]\n"
"v_mul_f32 v111, s68, v111\n"
"v_cvt_pk_bf16_f32 v52, v104, v105\n"
"v_cvt_pk_bf16_f32 v53, v106, v107\n"
"v_cvt_pk_bf16_f32 v54, v108, v109\n"
"v_mfma_f32_16x16x32_bf16 v[144:147], a[48:51], a[156:159], v[144:147]\n"
"v_cvt_pk_bf16_f32 v55, v110, v111\n"
"v_mul_f32 v112, s68, v112\n"
"v_mul_f32 v113, s68, v113\n"
"v_mfma_f32_16x16x32_bf16 v[148:151], a[20:23], a[152:155], v[148:151]\n"
"v_mul_f32 v114, s68, v114\n"
"v_mul_f32 v115, s68, v115\n"
"v_mul_f32 v116, s68, v116\n"
"v_mul_f32 v117, s68, v117\n"
"v_mfma_f32_16x16x32_bf16 v[148:151], a[52:55], a[156:159], v[148:151]\n"
"v_mul_f32 v118, s68, v118\n"
"v_mul_f32 v119, s68, v119\n"
"v_cvt_pk_bf16_f32 v56, v112, v113\n"
"v_cvt_pk_bf16_f32 v57, v114, v115\n"
"v_mfma_f32_16x16x32_bf16 v[152:155], a[24:27], a[152:155], v[152:155]\n"
"v_cvt_pk_bf16_f32 v58, v116, v117\n"
"v_cvt_pk_bf16_f32 v59, v118, v119\n"
"v_mul_f32 v120, s68, v120\n"
"v_mfma_f32_16x16x32_bf16 v[152:155], a[56:59], a[156:159], v[152:155]\n"
"v_mul_f32 v121, s68, v121\n"
"v_mul_f32 v122, s68, v122\n"
"v_mul_f32 v123, s68, v123\n"
"v_mul_f32 v124, s68, v124\n"
"v_mfma_f32_16x16x32_bf16 v[156:159], a[28:31], a[152:155], v[156:159]\n"
"v_mul_f32 v125, s68, v125\n"
"v_mul_f32 v126, s68, v126\n"
"v_mul_f32 v127, s68, v127\n"
"v_cvt_pk_bf16_f32 v60, v120, v121\n"
"v_mfma_f32_16x16x32_bf16 v[156:159], a[60:63], a[156:159], v[156:159]\n"
"v_cvt_pk_bf16_f32 v61, v122, v123\n"
"v_cvt_pk_bf16_f32 v62, v124, v125\n"
"v_cvt_pk_bf16_f32 v63, v126, v127\n"
"v_mfma_f32_16x16x32_bf16 v[160:163], a[0:3], a[160:163], v[160:163]\n"
"v_mfma_f32_16x16x32_bf16 v[160:163], a[32:35], a[164:167], v[160:163]\n"
"v_mfma_f32_16x16x32_bf16 v[164:167], a[4:7], a[160:163], v[164:167]\n"
"v_mfma_f32_16x16x32_bf16 v[164:167], a[36:39], a[164:167], v[164:167]\n"
"v_mul_f32 v128, s68, v128\n"
"v_mul_f32 v129, s68, v129\n"
"v_mul_f32 v130, s68, v130\n"
"v_mul_f32 v131, s68, v131\n"
"v_mfma_f32_16x16x32_bf16 v[168:171], a[8:11], a[160:163], v[168:171]\n"
"v_mul_f32 v132, s68, v132\n"
"v_mul_f32 v133, s68, v133\n"
"v_mul_f32 v134, s68, v134\n"
"v_mul_f32 v135, s68, v135\n"
"v_mfma_f32_16x16x32_bf16 v[168:171], a[40:43], a[164:167], v[168:171]\n"
"v_cvt_pk_bf16_f32 v64, v128, v129\n"
"v_cvt_pk_bf16_f32 v65, v130, v131\n"
"v_cvt_pk_bf16_f32 v66, v132, v133\n"
"v_cvt_pk_bf16_f32 v67, v134, v135\n"
"v_mfma_f32_16x16x32_bf16 v[172:175], a[12:15], a[160:163], v[172:175]\n"
"v_mul_f32 v136, s68, v136\n"
"v_mul_f32 v137, s68, v137\n"
"v_mul_f32 v138, s68, v138\n"
"v_mfma_f32_16x16x32_bf16 v[172:175], a[44:47], a[164:167], v[172:175]\n"
"v_mul_f32 v139, s68, v139\n"
"v_mul_f32 v140, s68, v140\n"
"v_mul_f32 v141, s68, v141\n"
"v_mul_f32 v142, s68, v142\n"
"v_mfma_f32_16x16x32_bf16 v[176:179], a[16:19], a[160:163], v[176:179]\n"
"v_mul_f32 v143, s68, v143\n"
"v_cvt_pk_bf16_f32 v68, v136, v137\n"
"v_cvt_pk_bf16_f32 v69, v138, v139\n"
"v_cvt_pk_bf16_f32 v70, v140, v141\n"
"v_mfma_f32_16x16x32_bf16 v[176:179], a[48:51], a[164:167], v[176:179]\n"
"v_cvt_pk_bf16_f32 v71, v142, v143\n"
"v_mul_f32 v144, s68, v144\n"
"v_mul_f32 v145, s68, v145\n"
"v_mfma_f32_16x16x32_bf16 v[180:183], a[20:23], a[160:163], v[180:183]\n"
"v_mul_f32 v146, s68, v146\n"
"v_mul_f32 v147, s68, v147\n"
"v_mul_f32 v148, s68, v148\n"
"v_mul_f32 v149, s68, v149\n"
"v_mfma_f32_16x16x32_bf16 v[180:183], a[52:55], a[164:167], v[180:183]\n"
"v_mul_f32 v150, s68, v150\n"
"v_mul_f32 v151, s68, v151\n"
"v_cvt_pk_bf16_f32 v72, v144, v145\n"
"v_cvt_pk_bf16_f32 v73, v146, v147\n"
"v_mfma_f32_16x16x32_bf16 v[184:187], a[24:27], a[160:163], v[184:187]\n"
"v_cvt_pk_bf16_f32 v74, v148, v149\n"
"v_cvt_pk_bf16_f32 v75, v150, v151\n"
"v_mul_f32 v152, s68, v152\n"
"v_mfma_f32_16x16x32_bf16 v[184:187], a[56:59], a[164:167], v[184:187]\n"
"v_mul_f32 v153, s68, v153\n"
"v_mul_f32 v154, s68, v154\n"
"v_mul_f32 v155, s68, v155\n"
"v_mul_f32 v156, s68, v156\n"
"v_mfma_f32_16x16x32_bf16 v[188:191], a[28:31], a[160:163], v[188:191]\n"
"v_mul_f32 v157, s68, v157\n"
"v_mul_f32 v158, s68, v158\n"
"v_mul_f32 v159, s68, v159\n"
"v_cvt_pk_bf16_f32 v76, v152, v153\n"
"v_mfma_f32_16x16x32_bf16 v[188:191], a[60:63], a[164:167], v[188:191]\n"
"v_cvt_pk_bf16_f32 v77, v154, v155\n"
"v_cvt_pk_bf16_f32 v78, v156, v157\n"
"v_cvt_pk_bf16_f32 v79, v158, v159\n"
"s_cmp_lg_u32 s66, 0\n"
"s_cbranch_scc1 .Lf_ep\n"
"s_and_b32 s73, %24, 4\n"
"s_and_b32 s74, %5, 1\n"
"s_lshl_b32 s74, s74, 1\n"
"s_add_u32 s73, s73, s74\n"
"s_add_u32 s74, s73, 1\n"
"s_xor_b32 s75, %24, 4\n"
"v_readlane_b32 s84, v221, %24\n"
"v_readlane_b32 s85, v221, s73\n"
"v_readlane_b32 s86, v221, s74\n"
"v_readlane_b32 s87, v221, s75\n"
"s_nop 1\n"
"s_add_u32 s84, s84, 0x9e3779b1\n"
"s_add_u32 s85, s85, 0x9e3779b1\n"
"s_add_u32 s86, s86, 0x9e3779b1\n"
"s_add_u32 s87, s87, 0x9e3779b1\n"
".Lf_ep:\n"
"v_xor_b32 v238, 0x28000, v238\n"
"v_xor_b32 v239, 0x28000, v239\n"
"v_xor_b32 v240, 0x28000, v240\n"
"v_xor_b32 v241, 0x28000, v241\n"
"v_pk_mul_f32 v[160:161], v[160:161], s[68:69]\n"
"v_pk_mul_f32 v[162:163], v[162:163], s[68:69]\n"
"v_pk_mul_f32 v[164:165], v[164:165], s[68:69]\n"
"v_pk_mul_f32 v[166:167], v[166:167], s[68:69]\n"
"v_cvt_pk_bf16_f32 v80, v160, v161\n"
"v_cvt_pk_bf16_f32 v81, v162, v163\n"
"v_cvt_pk_bf16_f32 v82, v164, v165\n"
"v_cvt_pk_bf16_f32 v83, v166, v167\n"
"v_pk_mul_f32 v[168:169], v[168:169], s[68:69]\n"
"v_pk_mul_f32 v[170:171], v[170:171], s[68:69]\n"
"v_pk_mul_f32 v[172:173], v[172:173], s[68:69]\n"
"v_pk_mul_f32 v[174:175], v[174:175], s[68:69]\n"
"v_cvt_pk_bf16_f32 v84, v168, v169\n"
"v_cvt_pk_bf16_f32 v85, v170, v171\n"
"v_cvt_pk_bf16_f32 v86, v172, v173\n"
"v_cvt_pk_bf16_f32 v87, v174, v175\n"
"v_pk_mul_f32 v[176:177], v[176:177], s[68:69]\n"
"v_pk_mul_f32 v[178:179], v[178:179], s[68:69]\n"
"v_pk_mul_f32 v[180:181], v[180:181], s[68:69]\n"
"v_pk_mul_f32 v[182:183], v[182:183], s[68:69]\n"
"v_cvt_pk_bf16_f32 v88, v176, v177\n"
"v_cvt_pk_bf16_f32 v89, v178, v179\n"
"v_cvt_pk_bf16_f32 v90, v180, v181\n"
"v_cvt_pk_bf16_f32 v91, v182, v183\n"
"v_pk_mul_f32 v[184:185], v[184:185], s[68:69]\n"
"v_pk_mul_f32 v[186:187], v[186:187], s[68:69]\n"
"v_pk_mul_f32 v[188:189], v[188:189], s[68:69]\n"
"v_pk_mul_f32 v[190:191], v[190:191], s[68:69]\n"
"v_cvt_pk_bf16_f32 v92, v184, v185\n"
"v_cvt_pk_bf16_f32 v93, v186, v187\n"
"v_cvt_pk_bf16_f32 v94, v188, v189\n"
"v_cvt_pk_bf16_f32 v95, v190, v191\n"
"s_waitcnt vmcnt(16) lgkmcnt(0)\n"
"s_barrier\n"
"v_mfma_f32_16x16x32_bf16 v[160:163], a[192:195], v[0:3], 0\n"
"v_mfma_f32_16x16x32_bf16 v[160:163], a[196:199], v[4:7], v[160:163]\n"
"v_mfma_f32_16x16x32_bf16 v[160:163], a[200:203], v[8:11], v[160:163]\n"
"v_mfma_f32_16x16x32_bf16 v[160:163], a[204:207], v[12:15], v[160:163]\n"
"v_mfma_f32_16x16x32_bf16 v[164:167], a[208:211], v[0:3], 0\n"
"v_mfma_f32_16x16x32_bf16 v[164:167], a[212:215], v[4:7], v[164:167]\n"
"v_mfma_f32_16x16x32_bf16 v[164:167], a[216:219], v[8:11], v[164:167]\n"
"v_mfma_f32_16x16x32_bf16 v[164:167], a[220:223], v[12:15], v[164:167]\n"
"s_nop 3\n"
"v_max3_f32 v252, v160, v161, v162\n"
"s_nop 2\n"
"v_max3_f32 v253, v163, v164, v165\n"
"v_max3_f32 v252, v252, v166, v167\n"
"v_max_f32 v252, v252, v253\n"
"v_mov_b32 v253, v252\n"
"s_nop 1\n"
"v_permlane32_swap_b32 v252, v253\n"
"v_max_f32 v252, v252, v253\n"
"v_mov_b32 v253, v252\n"
"s_nop 1\n"
"v_permlane16_swap_b32 v252, v253\n"
"v_max_f32 v252, v252, v253\n"
"v_sub_f32 v160, v160, v252\n"
"v_sub_f32 v161, v161, v252\n"
"v_sub_f32 v162, v162, v252\n"
"v_sub_f32 v163, v163, v252\n"
"v_sub_f32 v164, v164, v252\n"
"v_sub_f32 v165, v165, v252\n"
"v_sub_f32 v166, v166, v252\n"
"v_sub_f32 v167, v167, v252\n"
"v_sub_f32 v192, 0, v252\n"
"v_sub_f32 v193, 0, v252\n"
"v_mfma_f32_16x16x32_bf16 v[168:171], a[192:195], v[16:19], 0\n"
"v_sub_f32 v194, 0, v252\n"
"v_sub_f32 v195, 0, v252\n"
"v_exp_f32 v160, v160\n"
"v_mfma_f32_16x16x32_bf16 v[168:171], a[196:199], v[20:23], v[168:171]\n"
"v_exp_f32 v161, v161\n"
"v_exp_f32 v162, v162\n"
"v_add_f32 v216, v160, v161\n"
"v_mfma_f32_16x16x32_bf16 v[168:171], a[200:203], v[24:27], v[168:171]\n"
"v_exp_f32 v163, v163\n"
"v_cvt_pk_bf16_f32 v184, v160, v161\n"
"v_add_f32 v216, v216, v162\n"
"v_mfma_f32_16x16x32_bf16 v[168:171], a[204:207], v[28:31], v[168:171]\n"
"v_exp_f32 v164, v164\n"
"v_add_f32 v216, v216, v163\n"
"v_cvt_pk_bf16_f32 v185, v162, v163\n"
"v_mfma_f32_16x16x32_bf16 v[172:175], a[208:211], v[16:19], 0\n"
"v_exp_f32 v165, v165\n"
"v_add_f32 v216, v216, v164\n"
"v_exp_f32 v166, v166\n"
"v_mfma_f32_16x16x32_bf16 v[172:175], a[212:215], v[20:23], v[172:175]\n"
"v_add_f32 v216, v216, v165\n"
"v_cvt_pk_bf16_f32 v186, v164, v165\n"
"v_exp_f32 v167, v167\n"
"v_mfma_f32_16x16x32_bf16 v[172:175], a[216:219], v[24:27], v[172:175]\n"
"v_add_f32 v216, v216, v166\n"
"v_cvt_pk_bf16_f32 v187, v166, v167\n"
"v_add_f32 v216, v216, v167\n"
"v_mfma_f32_16x16x32_bf16 v[172:175], a[220:223], v[28:31], v[172:175]\n"
"ds_read_b128 v[96:99], v222 offset:16384\n"
"v_mfma_f32_16x16x32_bf16 a[0:3], a[224:227], v[184:187], 0\n"
"v_mfma_f32_16x16x32_bf16 a[24:27], a[228:231], v[184:187], 0\n"
"ds_read_b128 v[100:103], v223 offset:16384\n"
"v_max3_f32 v252, v168, v169, v170\n"
"s_nop 2\n"
"v_max3_f32 v253, v171, v172, v173\n"
"v_max3_f32 v252, v252, v174, v175\n"
"v_mfma_f32_16x16x32_bf16 a[48:51], a[232:235], v[184:187], 0\n"
"v_max_f32 v252, v252, v253\n"
"v_mov_b32 v253, v252\n"
"s_nop 1\n"
"v_permlane32_swap_b32 v252, v253\n"
"v_mfma_f32_16x16x32_bf16 a[72:75], a[236:239], v[184:187], 0\n"
"v_max_f32 v252, v252, v253\n"
"v_mov_b32 v253, v252\n"
"s_nop 1\n"
"v_permlane16_swap_b32 v252, v253\n"
"v_mfma_f32_16x16x32_bf16 a[96:99], a[240:243], v[184:187], 0\n"
"v_max_f32 v252, v252, v253\n"
"v_sub_f32 v168, v168, v252\n"
"v_sub_f32 v169, v169, v252\n"
"v_mfma_f32_16x16x32_bf16 a[120:123], a[244:247], v[184:187], 0\n"
"ds_read_b64_tr_b16 v[128:129], v226 offset:16384\n"
"v_sub_f32 v170, v170, v252\n"
"v_sub_f32 v171, v171, v252\n"
"v_sub_f32 v172, v172, v252\n"
"v_mfma_f32_16x16x32_bf16 a[144:147], a[248:251], v[184:187], 0\n"
"v_sub_f32 v173, v173, v252\n"
"v_sub_f32 v174, v174, v252\n"
"v_sub_f32 v175, v175, v252\n"
"v_mfma_f32_16x16x32_bf16 a[168:171], a[252:255], v[184:187], 0\n"
"ds_read_b64_tr_b16 v[130:131], v226 offset:17408\n"
"v_sub_f32 v196, 0, v252\n"
"v_sub_f32 v197, 0, v252\n"
"v_mfma_f32_16x16x32_bf16 v[176:179], a[192:195], v[32:35], 0\n"
"v_sub_f32 v198, 0, v252\n"
"v_sub_f32 v199, 0, v252\n"
"v_exp_f32 v168, v168\n"
"v_mfma_f32_16x16x32_bf16 v[176:179], a[196:199], v[36:39], v[176:179]\n"
"v_exp_f32 v169, v169\n"
"v_exp_f32 v170, v170\n"
"v_add_f32 v217, v168, v169\n"
"v_mfma_f32_16x16x32_bf16 v[176:179], a[200:203], v[40:43], v[176:179]\n"
"v_exp_f32 v171, v171\n"
"v_cvt_pk_bf16_f32 v188, v168, v169\n"
"v_add_f32 v217, v217, v170\n"
"v_mfma_f32_16x16x32_bf16 v[176:179], a[204:207], v[44:47], v[176:179]\n"
"v_exp_f32 v172, v172\n"
"v_add_f32 v217, v217, v171\n"
"v_cvt_pk_bf16_f32 v189, v170, v171\n"
"v_mfma_f32_16x16x32_bf16 v[180:183], a[208:211], v[32:35], 0\n"
"v_exp_f32 v173, v173\n"
"v_add_f32 v217, v217, v172\n"
"v_exp_f32 v174, v174\n"
"v_mfma_f32_16x16x32_bf16 v[180:183], a[212:215], v[36:39], v[180:183]\n"
"v_add_f32 v217, v217, v173\n"
"v_cvt_pk_bf16_f32 v190, v172, v173\n"
"v_exp_f32 v175, v175\n"
"v_mfma_f32_16x16x32_bf16 v[180:183], a[216:219], v[40:43], v[180:183]\n"
"v_add_f32 v217, v217, v174\n"
"v_cvt_pk_bf16_f32 v191, v174, v175\n"
"v_add_f32 v217, v217, v175\n"
"v_mfma_f32_16x16x32_bf16 v[180:183], a[220:223], v[44:47], v[180:183]\n"
"ds_read_b128 v[104:107], v224 offset:16384\n"
"v_mfma_f32_16x16x32_bf16 a[4:7], a[224:227], v[188:191], 0\n"
"v_mfma_f32_16x16x32_bf16 a[28:31], a[228:231], v[188:191], 0\n"
"ds_read_b128 v[108:111], v225 offset:16384\n"
"v_max3_f32 v252, v176, v177, v178\n"
"s_nop 2\n"
"v_max3_f32 v253, v179, v180, v181\n"
"v_max3_f32 v252, v252, v182, v183\n"
"v_mfma_f32_16x16x32_bf16 a[52:55], a[232:235], v[188:191], 0\n"
"v_max_f32 v252, v252, v253\n"
"v_mov_b32 v253, v252\n"
"s_nop 1\n"
"v_permlane32_swap_b32 v252, v253\n"
"v_mfma_f32_16x16x32_bf16 a[76:79], a[236:239], v[188:191], 0\n"
"v_max_f32 v252, v252, v253\n"
"v_mov_b32 v253, v252\n"
"s_nop 1\n"
"v_permlane16_swap_b32 v252, v253\n"
"v_mfma_f32_16x16x32_bf16 a[100:103], a[240:243], v[188:191], 0\n"
"v_max_f32 v252, v252, v253\n"
"v_sub_f32 v176, v176, v252\n"
"v_sub_f32 v177, v177, v252\n"
"v_mfma_f32_16x16x32_bf16 a[124:127], a[244:247], v[188:191], 0\n"
"ds_read_b64_tr_b16 v[132:133], v227 offset:16384\n"
"v_sub_f32 v178, v178, v252\n"
"v_sub_f32 v179, v179, v252\n"
"v_sub_f32 v180, v180, v252\n"
"v_mfma_f32_16x16x32_bf16 a[148:151], a[248:251], v[188:191], 0\n"
"v_sub_f32 v181, v181, v252\n"
"v_sub_f32 v182, v182, v252\n"
"v_sub_f32 v183, v183, v252\n"
"v_mfma_f32_16x16x32_bf16 a[172:175], a[252:255], v[188:191], 0\n"
"ds_read_b64_tr_b16 v[134:135], v227 offset:17408\n"
"v_sub_f32 v200, 0, v252\n"
"v_sub_f32 v201, 0, v252\n"
"v_mfma_f32_16x16x32_bf16 v[160:163], a[192:195], v[48:51], 0\n"
"v_sub_f32 v202, 0, v252\n"
"v_sub_f32 v203, 0, v252\n"
"v_exp_f32 v176, v176\n"
"v_mfma_f32_16x16x32_bf16 v[160:163], a[196:199], v[52:55], v[160:163]\n"
"v_exp_f32 v177, v177\n"
"v_exp_f32 v178, v178\n"
"v_add_f32 v218, v176, v177\n"
"v_mfma_f32_16x16x32_bf16 v[160:163], a[200:203], v[56:59], v[160:163]\n"
"v_exp_f32 v179, v179\n"
"v_cvt_pk_bf16_f32 v184, v176, v177\n"
"v_add_f32 v218, v218, v178\n"
"v_mfma_f32_16x16x32_bf16 v[160:163], a[204:207], v[60:63], v[160:163]\n"
"v_exp_f32 v180, v180\n"
"v_add_f32 v218, v218, v179\n"
"v_cvt_pk_bf16_f32 v185, v178, v179\n"
"v_mfma_f32_16x16x32_bf16 v[164:167], a[208:211], v[48:51], 0\n"
"v_exp_f32 v181, v181\n"
"v_add_f32 v218, v218, v180\n"
"v_exp_f32 v182, v182\n"
"v_mfma_f32_16x16x32_bf16 v[164:167], a[212:215], v[52:55], v[164:167]\n"
"v_add_f32 v218, v218, v181\n"
"v_cvt_pk_bf16_f32 v186, v180, v181\n"
"v_exp_f32 v183, v183\n"
"v_mfma_f32_16x16x32_bf16 v[164:167], a[216:219], v[56:59], v[164:167]\n"
"v_add_f32 v218, v218, v182\n"
"v_cvt_pk_bf16_f32 v187, v182, v183\n"
"v_add_f32 v218, v218, v183\n"
"v_mfma_f32_16x16x32_bf16 v[164:167], a[220:223], v[60:63], v[164:167]\n"
"ds_read_b128 v[112:115], v222 offset:17408\n"
"v_mfma_f32_16x16x32_bf16 a[8:11], a[224:227], v[184:187], 0\n"
"v_mfma_f32_16x16x32_bf16 a[32:35], a[228:231], v[184:187], 0\n"
"ds_read_b128 v[116:119], v223 offset:17408\n"
"v_max3_f32 v252, v160, v161, v162\n"
"s_nop 2\n"
"v_max3_f32 v253, v163, v164, v165\n"
"v_max3_f32 v252, v252, v166, v167\n"
"v_mfma_f32_16x16x32_bf16 a[56:59], a[232:235], v[184:187], 0\n"
"v_max_f32 v252, v252, v253\n"
"v_mov_b32 v253, v252\n"
"s_nop 1\n"
"v_permlane32_swap_b32 v252, v253\n"
"v_mfma_f32_16x16x32_bf16 a[80:83], a[236:239], v[184:187], 0\n"
"v_max_f32 v252, v252, v253\n"
"v_mov_b32 v253, v252\n"
"s_nop 1\n"
"v_permlane16_swap_b32 v252, v253\n"
"v_mfma_f32_16x16x32_bf16 a[104:107], a[240:243], v[184:187], 0\n"
"v_max_f32 v252, v252, v253\n"
"v_sub_f32 v160, v160, v252\n"
"v_sub_f32 v161, v161, v252\n"
"v_mfma_f32_16x16x32_bf16 a[128:131], a[244:247], v[184:187], 0\n"
"ds_read_b64_tr_b16 v[136:137], v228 offset:16384\n"
"v_sub_f32 v162, v162, v252\n"
"v_sub_f32 v163, v163, v252\n"
"v_sub_f32 v164, v164, v252\n"
"v_mfma_f32_16x16x32_bf16 a[152:155], a[248:251], v[184:187], 0\n"
"v_sub_f32 v165, v165, v252\n"
"v_sub_f32 v166, v166, v252\n"
"v_sub_f32 v167, v167, v252\n"
"v_mfma_f32_16x16x32_bf16 a[176:179], a[252:255], v[184:187], 0\n"
"ds_read_b64_tr_b16 v[138:139], v228 offset:17408\n"
"v_sub_f32 v204, 0, v252\n"
"v_sub_f32 v205, 0, v252\n"
"v_mfma_f32_16x16x32_bf16 v[168:171], a[192:195], v[64:67], 0\n"
"v_sub_f32 v206, 0, v252\n"
"v_sub_f32 v207, 0, v252\n"
"v_exp_f32 v160, v160\n"
"v_mfma_f32_16x16x32_bf16 v[168:171], a[196:199], v[68:71], v[168:171]\n"
"v_exp_f32 v161, v161\n"
"v_exp_f32 v162, v162\n"
"v_add_f32 v219, v160, v161\n"
"v_mfma_f32_16x16x32_bf16 v[168:171], a[200:203], v[72:75], v[168:171]\n"
"v_exp_f32 v163, v163\n"
"v_cvt_pk_bf16_f32 v188, v160, v161\n"
"v_add_f32 v219, v219, v162\n"
"v_mfma_f32_16x16x32_bf16 v[168:171], a[204:207], v[76:79], v[168:171]\n"
"v_exp_f32 v164, v164\n"
"v_add_f32 v219, v219, v163\n"
"v_cvt_pk_bf16_f32 v189, v162, v163\n"
"v_mfma_f32_16x16x32_bf16 v[172:175], a[208:211], v[64:67], 0\n"
"v_exp_f32 v165, v165\n"
"v_add_f32 v219, v219, v164\n"
"v_exp_f32 v166, v166\n"
"v_mfma_f32_16x16x32_bf16 v[172:175], a[212:215], v[68:71], v[172:175]\n"
"v_add_f32 v219, v219, v165\n"
"v_cvt_pk_bf16_f32 v190, v164, v165\n"
"v_exp_f32 v167, v167\n"
"v_mfma_f32_16x16x32_bf16 v[172:175], a[216:219], v[72:75], v[172:175]\n"
"v_add_f32 v219, v219, v166\n"
"v_cvt_pk_bf16_f32 v191, v166, v167\n"
"v_add_f32 v219, v219, v167\n"
"v_mfma_f32_16x16x32_bf16 v[172:175], a[220:223], v[76:79], v[172:175]\n"
"ds_read_b128 v[120:123], v224 offset:17408\n"
"v_mfma_f32_16x16x32_bf16 a[12:15], a[224:227], v[188:191], 0\n"
"v_mfma_f32_16x16x32_bf16 a[36:39], a[228:231], v[188:191], 0\n"
"ds_read_b128 v[124:127], v225 offset:17408\n"
"v_max3_f32 v252, v168, v169, v170\n"
"s_nop 2\n"
"v_max3_f32 v253, v171, v172, v173\n"
"v_max3_f32 v252, v252, v174, v175\n"
"v_mfma_f32_16x16x32_bf16 a[60:63], a[232:235], v[188:191], 0\n"
"v_max_f32 v252, v252, v253\n"
"v_mov_b32 v253, v252\n"
"s_nop 1\n"
"v_permlane32_swap_b32 v252, v253\n"
"v_mfma_f32_16x16x32_bf16 a[84:87], a[236:239], v[188:191], 0\n"
"v_max_f32 v252, v252, v253\n"
"v_mov_b32 v253, v252\n"
"s_nop 1\n"
"v_permlane16_swap_b32 v252, v253\n"
"v_mfma_f32_16x16x32_bf16 a[108:111], a[240:243], v[188:191], 0\n"
"v_max_f32 v252, v252, v253\n"
"v_sub_f32 v168, v168, v252\n"
"v_sub_f32 v169, v169, v252\n"
"v_mfma_f32_16x16x32_bf16 a[132:135], a[244:247], v[188:191], 0\n"
"ds_read_b64_tr_b16 v[140:141], v229 offset:16384\n"
"v_sub_f32 v170, v170, v252\n"
"v_sub_f32 v171, v171, v252\n"
"v_sub_f32 v172, v172, v252\n"
"v_mfma_f32_16x16x32_bf16 a[156:159], a[248:251], v[188:191], 0\n"
"v_sub_f32 v173, v173, v252\n"
"v_sub_f32 v174, v174, v252\n"
"v_sub_f32 v175, v175, v252\n"
"v_mfma_f32_16x16x32_bf16 a[180:183], a[252:255], v[188:191], 0\n"
"ds_read_b64_tr_b16 v[142:143], v229 offset:17408\n"
"v_sub_f32 v208, 0, v252\n"
"v_sub_f32 v209, 0, v252\n"
"v_mfma_f32_16x16x32_bf16 v[176:179], a[192:195], v[80:83], 0\n"
"v_sub_f32 v210, 0, v252\n"
"v_sub_f32 v211, 0, v252\n"
"v_exp_f32 v168, v168\n"
"v_mfma_f32_16x16x32_bf16 v[176:179], a[196:199], v[84:87], v[176:179]\n"
"v_exp_f32 v169, v169\n"
"v_exp_f32 v170, v170\n"
"v_add_f32 v220, v168, v169\n"
"v_mfma_f32_16x16x32_bf16 v[176:179], a[200:203], v[88:91], v[176:179]\n"
"v_exp_f32 v171, v171\n"
"v_cvt_pk_bf16_f32 v184, v168, v169\n"
"v_add_f32 v220, v220, v170\n"
"v_mfma_f32_16x16x32_bf16 v[176:179], a[204:207], v[92:95], v[176:179]\n"
"v_exp_f32 v172, v172\n"
"v_add_f32 v220, v220, v171\n"
"v_cvt_pk_bf16_f32 v185, v170, v171\n"
"v_mfma_f32_16x16x32_bf16 v[180:183], a[208:211], v[80:83], 0\n"
"v_exp_f32 v173, v173\n"
"v_add_f32 v220, v220, v172\n"
"v_exp_f32 v174, v174\n"
"v_mfma_f32_16x16x32_bf16 v[180:183], a[212:215], v[84:87], v[180:183]\n"
"v_add_f32 v220, v220, v173\n"
"v_cvt_pk_bf16_f32 v186, v172, v173\n"
"v_exp_f32 v175, v175\n"
"v_mfma_f32_16x16x32_bf16 v[180:183], a[216:219], v[88:91], v[180:183]\n"
"v_add_f32 v220, v220, v174\n"
"v_cvt_pk_bf16_f32 v187, v174, v175\n"
"v_add_f32 v220, v220, v175\n"
"v_mfma_f32_16x16x32_bf16 v[180:183], a[220:223], v[92:95], v[180:183]\n"
"ds_read_b64_tr_b16 v[144:145], v230 offset:16384\n"
"v_mfma_f32_16x16x32_bf16 a[16:19], a[224:227], v[184:187], 0\n"
"v_mfma_f32_16x16x32_bf16 a[40:43], a[228:231], v[184:187], 0\n"
"ds_read_b64_tr_b16 v[146:147], v230 offset:17408\n"
"v_max3_f32 v252, v176, v177, v178\n"
"s_nop 2\n"
"v_max3_f32 v253, v179, v180, v181\n"
"v_max3_f32 v252, v252, v182, v183\n"
"v_mfma_f32_16x16x32_bf16 a[64:67], a[232:235], v[184:187], 0\n"
"v_max_f32 v252, v252, v253\n"
"v_mov_b32 v253, v252\n"
"s_nop 1\n"
"v_permlane32_swap_b32 v252, v253\n"
"v_mfma_f32_16x16x32_bf16 a[88:91], a[236:239], v[184:187], 0\n"
"v_max_f32 v252, v252, v253\n"
"v_mov_b32 v253, v252\n"
"s_nop 1\n"
"v_permlane16_swap_b32 v252, v253\n"
"v_mfma_f32_16x16x32_bf16 a[112:115], a[240:243], v[184:187], 0\n"
"v_max_f32 v252, v252, v253\n"
"v_sub_f32 v176, v176, v252\n"
"v_sub_f32 v177, v177, v252\n"
"v_mfma_f32_16x16x32_bf16 a[136:139], a[244:247], v[184:187], 0\n"
"ds_read_b64_tr_b16 v[148:149], v231 offset:16384\n"
"v_sub_f32 v178, v178, v252\n"
"v_sub_f32 v179, v179, v252\n"
"v_sub_f32 v180, v180, v252\n"
"v_mfma_f32_16x16x32_bf16 a[160:163], a[248:251], v[184:187], 0\n"
"v_sub_f32 v181, v181, v252\n"
"v_sub_f32 v182, v182, v252\n"
"v_sub_f32 v183, v183, v252\n"
"v_mfma_f32_16x16x32_bf16 a[184:187], a[252:255], v[184:187], 0\n"
"ds_read_b64_tr_b16 v[150:151], v231 offset:17408\n"
"v_sub_f32 v212, 0, v252\n"
"v_sub_f32 v213, 0, v252\n"
"s_waitcnt lgkmcnt(8)\n"
"v_mfma_f32_16x16x32_bf16 v[160:163], v[96:99], v[0:3], v[192:195]\n"
"v_sub_f32 v214, 0, v252\n"
"v_sub_f32 v215, 0, v252\n"
"v_exp_f32 v176, v176\n"
"v_mfma_f32_16x16x32_bf16 v[160:163], v[100:103], v[4:7], v[160:163]\n"
"v_exp_f32 v177, v177\n"
"v_exp_f32 v178, v178\n"
"v_add_f32 v221, v176, v177\n"
"v_mfma_f32_16x16x32_bf16 v[160:163], v[104:107], v[8:11], v[160:163]\n"
"v_exp_f32 v179, v179\n"
"v_cvt_pk_bf16_f32 v188, v176, v177\n"
"v_add_f32 v221, v221, v178\n"
"v_mfma_f32_16x16x32_bf16 v[160:163], v[108:111], v[12:15], v[160:163]\n"
"ds_read_b64_tr_b16 v[152:153], v232 offset:16384\n"
"v_exp_f32 v180, v180\n"
"v_add_f32 v221, v221, v179\n"
"v_cvt_pk_bf16_f32 v189, v178, v179\n"
"v_mfma_f32_16x16x32_bf16 v[164:167], v[112:115], v[0:3], v[192:195]\n"
"v_exp_f32 v181, v181\n"
"v_add_f32 v221, v221, v180\n"
"v_exp_f32 v182, v182\n"
"v_mfma_f32_16x16x32_bf16 v[164:167], v[116:119], v[4:7], v[164:167]\n"
"ds_read_b64_tr_b16 v[154:155], v232 offset:17408\n"
"v_add_f32 v221, v221, v181\n"
"v_cvt_pk_bf16_f32 v190, v180, v181\n"
"v_exp_f32 v183, v183\n"
"s_waitcnt lgkmcnt(9)\n"
"v_mfma_f32_16x16x32_bf16 v[164:167], v[120:123], v[8:11], v[164:167]\n"
"v_add_f32 v221, v221, v182\n"
"v_cvt_pk_bf16_f32 v191, v182, v183\n"
"v_add_f32 v221, v221, v183\n"
"s_waitcnt lgkmcnt(8)\n"
"v_mfma_f32_16x16x32_bf16 v[164:167], v[124:127], v[12:15], v[164:167]\n"
"ds_read_b64_tr_b16 v[156:157], v233 offset:16384\n"
"v_mfma_f32_16x16x32_bf16 a[20:23], a[224:227], v[188:191], 0\n"
"v_exp_f32 v160, v160\n"
"v_exp_f32 v161, v161\n"
"v_mfma_f32_16x16x32_bf16 a[44:47], a[228:231], v[188:191], 0\n"
"ds_read_b64_tr_b16 v[158:159], v233 offset:17408\n"
"v_add_f32 v216, v216, v160\n"
"v_mfma_f32_16x16x32_bf16 a[68:71], a[232:235], v[188:191], 0\n"
"v_exp_f32 v162, v162\n"
"v_mfma_f32_16x16x32_bf16 a[92:95], a[236:239], v[188:191], 0\n"
"s_mov_b32 s65, 3\n"
"v_add_f32 v216, v216, v161\n"
"v_cvt_pk_bf16_f32 v184, v160, v161\n"
"v_mfma_f32_16x16x32_bf16 a[116:119], a[240:243], v[188:191], 0\n"
"v_exp_f32 v163, v163\n"
"v_mfma_f32_16x16x32_bf16 a[140:143], a[244:247], v[188:191], 0\n"
"v_add_f32 v216, v216, v162\n"
"v_mfma_f32_16x16x32_bf16 a[164:167], a[248:251], v[188:191], 0\n"
"v_exp_f32 v164, v164\n"
"v_mfma_f32_16x16x32_bf16 a[188:191], a[252:255], v[188:191], 0\n"
"v_add_f32 v216, v216, v163\n"
".Lf_at:\n"
"s_waitcnt vmcnt(0) lgkmcnt(0)\n"
"s_barrier\n"
"v_mfma_f32_16x16x32_bf16 v[168:171], v[96:99], v[16:19], v[196:199]\n"
"v_exp_f32 v165, v165\n"
"v_mfma_f32_16x16x32_bf16 v[168:171], v[100:103], v[20:23], v[168:171]\n"
"s_add_u32 m0, s57, 0\n"
"v_add_f32 v216, v216, v164\n"
"v_cvt_pk_bf16_f32 v185, v162, v163\n"
"v_mfma_f32_16x16x32_bf16 v[168:171], v[104:107], v[24:27], v[168:171]\n"
"buffer_load_dwordx4 v234, s[36:39], s56 offen offset:0 nt lds\n"
"v_exp_f32 v166, v166\n"
"v_mfma_f32_16x16x32_bf16 v[168:171], v[108:111], v[28:31], v[168:171]\n"
"v_add_f32 v216, v216, v165\n"
"v_cvt_pk_bf16_f32 v186, v164, v165\n"
"v_mfma_f32_16x16x32_bf16 v[172:175], v[112:115], v[16:19], v[196:199]\n"
"v_exp_f32 v167, v167\n"
"v_mfma_f32_16x16x32_bf16 v[172:175], v[116:119], v[20:23], v[172:175]\n"
"v_cvt_pk_bf16_f32 v187, v166, v167\n"
"v_add_f32 v216, v216, v166\n"
"v_mfma_f32_16x16x32_bf16 v[172:175], v[120:123], v[24:27], v[172:175]\n"
"buffer_load_dwordx4 v234, s[36:39], s56 offen offset:1024 nt lds\n"
"v_exp_f32 v168, v168\n"
"v_mfma_f32_16x16x32_bf16 v[172:175], v[124:127], v[28:31], v[172:175]\n"
"ds_read_b128 a[192:195], v222 offset:32768\n"
"v_add_f32 v216, v216, v167\n"
"v_mfma_f32_16x16x32_bf16 a[0:3], v[128:131], v[184:187], a[0:3]\n"
"v_exp_f32 v169, v169\n"
"v_mfma_f32_16x16x32_bf16 a[24:27], v[132:135], v[184:187], a[24:27]\n"
"ds_read_b128 a[196:199], v223 offset:32768\n"
"v_add_f32 v217, v217, v168\n"
"v_mfma_f32_16x16x32_bf16 a[48:51], v[136:139], v[184:187], a[48:51]\n"
"buffer_load_dwordx4 v235, s[36:39], s56 offen offset:2048 nt lds\n"
"v_exp_f32 v170, v170\n"
"v_mfma_f32_16x16x32_bf16 a[72:75], v[140:143], v[184:187], a[72:75]\n"
"v_add_f32 v217, v217, v169\n"
"v_cvt_pk_bf16_f32 v188, v168, v169\n"
"v_mfma_f32_16x16x32_bf16 a[96:99], v[144:147], v[184:187], a[96:99]\n"
"v_exp_f32 v171, v171\n"
"v_mfma_f32_16x16x32_bf16 a[120:123], v[148:151], v[184:187], a[120:123]\n"
"ds_read_b64_tr_b16 a[224:225], v226 offset:32768\n"
"v_add_f32 v217, v217, v170\n"
"v_mfma_f32_16x16x32_bf16 a[144:147], v[152:155], v[184:187], a[144:147]\n"
"buffer_load_dwordx4 v235, s[36:39], s56 offen offset:3072 nt lds\n"
"v_exp_f32 v172, v172\n"
"v_mfma_f32_16x16x32_bf16 a[168:171], v[156:159], v[184:187], a[168:171]\n"
"ds_read_b64_tr_b16 a[226:227], v226 offset:33792\n"
"v_add_f32 v217, v217, v171\n"
"v_mfma_f32_16x16x32_bf16 v[176:179], v[96:99], v[32:35], v[200:203]\n"
"v_exp_f32 v173, v173\n"
"v_mfma_f32_16x16x32_bf16 v[176:179], v[100:103], v[36:39], v[176:179]\n"
"s_add_u32 m0, s58, 0\n"
"v_add_f32 v217, v217, v172\n"
"v_cvt_pk_bf16_f32 v189, v170, v171\n"
"v_mfma_f32_16x16x32_bf16 v[176:179], v[104:107], v[40:43], v[176:179]\n"
"buffer_load_dwordx4 v236, s[40:43], s56 offen offset:0 nt lds\n"
"v_exp_f32 v174, v174\n"
"v_mfma_f32_16x16x32_bf16 v[176:179], v[108:111], v[44:47], v[176:179]\n"
"v_add_f32 v217, v217, v173\n"
"v_cvt_pk_bf16_f32 v190, v172, v173\n"
"v_mfma_f32_16x16x32_bf16 v[180:183], v[112:115], v[32:35], v[200:203]\n"
"v_exp_f32 v175, v175\n"
"v_mfma_f32_16x16x32_bf16 v[180:183], v[116:119], v[36:39], v[180:183]\n"
"v_cvt_pk_bf16_f32 v191, v174, v175\n"
"v_add_f32 v217, v217, v174\n"
"v_mfma_f32_16x16x32_bf16 v[180:183], v[120:123], v[40:43], v[180:183]\n"
"buffer_load_dwordx4 v236, s[40:43], s56 offen offset:1024 nt lds\n"
"v_exp_f32 v176, v176\n"
"v_mfma_f32_16x16x32_bf16 v[180:183], v[124:127], v[44:47], v[180:183]\n"
"ds_read_b128 a[200:203], v224 offset:32768\n"
"v_add_f32 v217, v217, v175\n"
"v_mfma_f32_16x16x32_bf16 a[4:7], v[128:131], v[188:191], a[4:7]\n"
"v_exp_f32 v177, v177\n"
"v_mfma_f32_16x16x32_bf16 a[28:31], v[132:135], v[188:191], a[28:31]\n"
"ds_read_b128 a[204:207], v225 offset:32768\n"
"v_add_f32 v218, v218, v176\n"
"v_mfma_f32_16x16x32_bf16 a[52:55], v[136:139], v[188:191], a[52:55]\n"
"buffer_load_dwordx4 v237, s[40:43], s56 offen offset:2048 nt lds\n"
"v_exp_f32 v178, v178\n"
"v_mfma_f32_16x16x32_bf16 a[76:79], v[140:143], v[188:191], a[76:79]\n"
"v_add_f32 v218, v218, v177\n"
"v_cvt_pk_bf16_f32 v184, v176, v177\n"
"v_mfma_f32_16x16x32_bf16 a[100:103], v[144:147], v[188:191], a[100:103]\n"
"v_exp_f32 v179, v179\n"
"v_mfma_f32_16x16x32_bf16 a[124:127], v[148:151], v[188:191], a[124:127]\n"
"ds_read_b64_tr_b16 a[228:229], v227 offset:32768\n"
"v_add_f32 v218, v218, v178\n"
"v_mfma_f32_16x16x32_bf16 a[148:151], v[152:155], v[188:191], a[148:151]\n"
"buffer_load_dwordx4 v237, s[40:43], s56 offen offset:3072 nt lds\n"
"v_exp_f32 v180, v180\n"
"v_mfma_f32_16x16x32_bf16 a[172:175], v[156:159], v[188:191], a[172:175]\n"
"ds_read_b64_tr_b16 a[230:231], v227 offset:33792\n"
"v_add_f32 v218, v218, v179\n"
"v_mfma_f32_16x16x32_bf16 v[160:163], v[96:99], v[48:51], v[204:207]\n"
"s_add_u32 s56, s56, 8192\n"
"s_and_b32 s56, s56, 0x1ffff\n"
"v_exp_f32 v181, v181\n"
"v_mfma_f32_16x16x32_bf16 v[160:163], v[100:103], v[52:55], v[160:163]\n"
"s_add_u32 m0, s57, 16384\n"
"v_add_f32 v218, v218, v180\n"
"v_cvt_pk_bf16_f32 v185, v178, v179\n"
"v_mfma_f32_16x16x32_bf16 v[160:163], v[104:107], v[56:59], v[160:163]\n"
"buffer_load_dwordx4 v234, s[36:39], s56 offen offset:0 nt lds\n"
"v_exp_f32 v182, v182\n"
"v_mfma_f32_16x16x32_bf16 v[160:163], v[108:111], v[60:63], v[160:163]\n"
"v_add_f32 v218, v218, v181\n"
"v_cvt_pk_bf16_f32 v186, v180, v181\n"
"v_mfma_f32_16x16x32_bf16 v[164:167], v[112:115], v[48:51], v[204:207]\n"
"v_exp_f32 v183, v183\n"
"v_mfma_f32_16x16x32_bf16 v[164:167], v[116:119], v[52:55], v[164:167]\n"
"v_cvt_pk_bf16_f32 v187, v182, v183\n"
"v_add_f32 v218, v218, v182\n"
"v_mfma_f32_16x16x32_bf16 v[164:167], v[120:123], v[56:59], v[164:167]\n"
"buffer_load_dwordx4 v234, s[36:39], s56 offen offset:1024 nt lds\n"
"v_exp_f32 v160, v160\n"
"v_mfma_f32_16x16x32_bf16 v[164:167], v[124:127], v[60:63], v[164:167]\n"
"ds_read_b128 a[208:211], v222 offset:33792\n"
"v_add_f32 v218, v218, v183\n"
"v_mfma_f32_16x16x32_bf16 a[8:11], v[128:131], v[184:187], a[8:11]\n"
"v_exp_f32 v161, v161\n"
"v_mfma_f32_16x16x32_bf16 a[32:35], v[132:135], v[184:187], a[32:35]\n"
"ds_read_b128 a[212:215], v223 offset:33792\n"
"v_add_f32 v219, v219, v160\n"
"v_mfma_f32_16x16x32_bf16 a[56:59], v[136:139], v[184:187], a[56:59]\n"
"buffer_load_dwordx4 v235, s[36:39], s56 offen offset:2048 nt lds\n"
"v_exp_f32 v162, v162\n"
"v_mfma_f32_16x16x32_bf16 a[80:83], v[140:143], v[184:187], a[80:83]\n"
"v_add_f32 v219, v219, v161\n"
"v_cvt_pk_bf16_f32 v188, v160, v161\n"
"v_mfma_f32_16x16x32_bf16 a[104:107], v[144:147], v[184:187], a[104:107]\n"
"v_exp_f32 v163, v163\n"
"v_mfma_f32_16x16x32_bf16 a[128:131], v[148:151], v[184:187], a[128:131]\n"
"ds_read_b64_tr_b16 a[232:233], v228 offset:32768\n"
"v_add_f32 v219, v219, v162\n"
"v_mfma_f32_16x16x32_bf16 a[152:155], v[152:155], v[184:187], a[152:155]\n"
"buffer_load_dwordx4 v235, s[36:39], s56 offen offset:3072 nt lds\n"
"v_exp_f32 v164, v164\n"
"v_mfma_f32_16x16x32_bf16 a[176:179], v[156:159], v[184:187], a[176:179]\n"
"ds_read_b64_tr_b16 a[234:235], v228 offset:33792\n"
"v_add_f32 v219, v219, v163\n"
"v_mfma_f32_16x16x32_bf16 v[168:171], v[96:99], v[64:67], v[208:211]\n"
"v_exp_f32 v165, v165\n"
"v_mfma_f32_16x16x32_bf16 v[168:171], v[100:103], v[68:71], v[168:171]\n"
"s_add_u32 m0, s58, 16384\n"
"v_add_f32 v219, v219, v164\n"
"v_cvt_pk_bf16_f32 v189, v162, v163\n"
"v_mfma_f32_16x16x32_bf16 v[168:171], v[104:107], v[72:75], v[168:171]\n"
"buffer_load_dwordx4 v236, s[40:43], s56 offen offset:0 nt lds\n"
"v_exp_f32 v166, v166\n"
"v_mfma_f32_16x16x32_bf16 v[168:171], v[108:111], v[76:79], v[168:171]\n"
"v_add_f32 v219, v219, v165\n"
"v_cvt_pk_bf16_f32 v190, v164, v165\n"
"v_mfma_f32_16x16x32_bf16 v[172:175], v[112:115], v[64:67], v[208:211]\n"
"v_exp_f32 v167, v167\n"
"v_mfma_f32_16x16x32_bf16 v[172:175], v[116:119], v[68:71], v[172:175]\n"
"v_cvt_pk_bf16_f32 v191, v166, v167\n"
"v_add_f32 v219, v219, v166\n"
"v_mfma_f32_16x16x32_bf16 v[172:175], v[120:123], v[72:75], v[172:175]\n"
"buffer_load_dwordx4 v236, s[40:43], s56 offen offset:1024 nt lds\n"
"v_exp_f32 v168, v168\n"
"v_mfma_f32_16x16x32_bf16 v[172:175], v[124:127], v[76:79], v[172:175]\n"
"ds_read_b128 a[216:219], v224 offset:33792\n"
"v_add_f32 v219, v219, v167\n"
"v_mfma_f32_16x16x32_bf16 a[12:15], v[128:131], v[188:191], a[12:15]\n"
"v_exp_f32 v169, v169\n"
"v_mfma_f32_16x16x32_bf16 a[36:39], v[132:135], v[188:191], a[36:39]\n"
"ds_read_b128 a[220:223], v225 offset:33792\n"
"v_add_f32 v220, v220, v168\n"
"v_mfma_f32_16x16x32_bf16 a[60:63], v[136:139], v[188:191], a[60:63]\n"
"buffer_load_dwordx4 v237, s[40:43], s56 offen offset:2048 nt lds\n"
"v_exp_f32 v170, v170\n"
"v_mfma_f32_16x16x32_bf16 a[84:87], v[140:143], v[188:191], a[84:87]\n"
"v_add_f32 v220, v220, v169\n"
"v_cvt_pk_bf16_f32 v184, v168, v169\n"
"v_mfma_f32_16x16x32_bf16 a[108:111], v[144:147], v[188:191], a[108:111]\n"
"v_exp_f32 v171, v171\n"
"v_mfma_f32_16x16x32_bf16 a[132:135], v[148:151], v[188:191], a[132:135]\n"
"ds_read_b64_tr_b16 a[236:237], v229 offset:32768\n"
"v_add_f32 v220, v220, v170\n"
"v_mfma_f32_16x16x32_bf16 a[156:159], v[152:155], v[188:191], a[156:159]\n"
"buffer_load_dwordx4 v237, s[40:43], s56 offen offset:3072 nt lds\n"
"v_exp_f32 v172, v172\n"
"v_mfma_f32_16x16x32_bf16 a[180:183], v[156:159], v[188:191], a[180:183]\n"
"ds_read_b64_tr_b16 a[238:239], v229 offset:33792\n"
"s_add_u32 s56, s56, 8192\n"
"s_and_b32 s56, s56, 0x1ffff\n"
"v_add_f32 v220, v220, v171\n"
"v_mfma_f32_16x16x32_bf16 v[176:179], v[96:99], v[80:83], v[212:215]\n"
"v_exp_f32 v173, v173\n"
"v_mfma_f32_16x16x32_bf16 v[176:179], v[100:103], v[84:87], v[176:179]\n"
"v_add_f32 v220, v220, v172\n"
"v_cvt_pk_bf16_f32 v185, v170, v171\n"
"v_mfma_f32_16x16x32_bf16 v[176:179], v[104:107], v[88:91], v[176:179]\n"
"v_exp_f32 v174, v174\n"
"v_mfma_f32_16x16x32_bf16 v[176:179], v[108:111], v[92:95], v[176:179]\n"
"v_add_f32 v220, v220, v173\n"
"v_cvt_pk_bf16_f32 v186, v172, v173\n"
"v_mfma_f32_16x16x32_bf16 v[180:183], v[112:115], v[80:83], v[212:215]\n"
"v_exp_f32 v175, v175\n"
"v_mfma_f32_16x16x32_bf16 v[180:183], v[116:119], v[84:87], v[180:183]\n"
"v_cvt_pk_bf16_f32 v187, v174, v175\n"
"v_add_f32 v220, v220, v174\n"
"v_mfma_f32_16x16x32_bf16 v[180:183], v[120:123], v[88:91], v[180:183]\n"
"v_exp_f32 v176, v176\n"
"v_mfma_f32_16x16x32_bf16 v[180:183], v[124:127], v[92:95], v[180:183]\n"
"ds_read_b64_tr_b16 a[240:241], v230 offset:32768\n"
"v_add_f32 v220, v220, v175\n"
"v_mfma_f32_16x16x32_bf16 a[16:19], v[128:131], v[184:187], a[16:19]\n"
"v_exp_f32 v177, v177\n"
"v_mfma_f32_16x16x32_bf16 a[40:43], v[132:135], v[184:187], a[40:43]\n"
"ds_read_b64_tr_b16 a[242:243], v230 offset:33792\n"
"v_add_f32 v221, v221, v176\n"
"v_mfma_f32_16x16x32_bf16 a[64:67], v[136:139], v[184:187], a[64:67]\n"
"v_exp_f32 v178, v178\n"
"v_mfma_f32_16x16x32_bf16 a[88:91], v[140:143], v[184:187], a[88:91]\n"
"v_add_f32 v221, v221, v177\n"
"v_cvt_pk_bf16_f32 v188, v176, v177\n"
"v_mfma_f32_16x16x32_bf16 a[112:115], v[144:147], v[184:187], a[112:115]\n"
"v_exp_f32 v179, v179\n"
"v_mfma_f32_16x16x32_bf16 a[136:139], v[148:151], v[184:187], a[136:139]\n"
"ds_read_b64_tr_b16 a[244:245], v231 offset:32768\n"
"v_add_f32 v221, v221, v178\n"
"v_mfma_f32_16x16x32_bf16 a[160:163], v[152:155], v[184:187], a[160:163]\n"
"v_exp_f32 v180, v180\n"
"v_mfma_f32_16x16x32_bf16 a[184:187], v[156:159], v[184:187], a[184:187]\n"
"ds_read_b64_tr_b16 a[246:247], v231 offset:33792\n"
"v_add_f32 v221, v221, v179\n"
"s_waitcnt lgkmcnt(8)\n"
"v_mfma_f32_16x16x32_bf16 v[160:163], a[192:195], v[0:3], v[192:195]\n"
"v_exp_f32 v181, v181\n"
"v_mfma_f32_16x16x32_bf16 v[160:163], a[196:199], v[4:7], v[160:163]\n"
"v_add_f32 v221, v221, v180\n"
"v_cvt_pk_bf16_f32 v189, v178, v179\n"
"v_mfma_f32_16x16x32_bf16 v[160:163], a[200:203], v[8:11], v[160:163]\n"
"v_exp_f32 v182, v182\n"
"v_mfma_f32_16x16x32_bf16 v[160:163], a[204:207], v[12:15], v[160:163]\n"
"ds_read_b64_tr_b16 a[248:249], v232 offset:32768\n"
"v_add_f32 v221, v221, v181\n"
"v_cvt_pk_bf16_f32 v190, v180, v181\n"
"v_mfma_f32_16x16x32_bf16 v[164:167], a[208:211], v[0:3], v[192:195]\n"
"v_exp_f32 v183, v183\n"
"v_mfma_f32_16x16x32_bf16 v[164:167], a[212:215], v[4:7], v[164:167]\n"
"ds_read_b64_tr_b16 a[250:251], v232 offset:33792\n"
"v_cvt_pk_bf16_f32 v191, v182, v183\n"
"v_add_f32 v221, v221, v182\n"
"s_waitcnt lgkmcnt(9)\n"
"v_mfma_f32_16x16x32_bf16 v[164:167], a[216:219], v[8:11], v[164:167]\n"
"v_exp_f32 v160, v160\n"
"s_waitcnt lgkmcnt(8)\n"
"v_mfma_f32_16x16x32_bf16 v[164:167], a[220:223], v[12:15], v[164:167]\n"
"ds_read_b64_tr_b16 a[252:253], v233 offset:32768\n"
"v_add_f32 v221, v221, v183\n"
"v_mfma_f32_16x16x32_bf16 a[20:23], v[128:131], v[188:191], a[20:23]\n"
"v_exp_f32 v161, v161\n"
"v_mfma_f32_16x16x32_bf16 a[44:47], v[132:135], v[188:191], a[44:47]\n"
"ds_read_b64_tr_b16 a[254:255], v233 offset:33792\n"
"v_add_f32 v216, v216, v160\n"
"v_mfma_f32_16x16x32_bf16 a[68:71], v[136:139], v[188:191], a[68:71]\n"
"v_exp_f32 v162, v162\n"
"v_mfma_f32_16x16x32_bf16 a[92:95], v[140:143], v[188:191], a[92:95]\n"
"v_add_f32 v216, v216, v161\n"
"v_cvt_pk_bf16_f32 v184, v160, v161\n"
"v_mfma_f32_16x16x32_bf16 a[116:119], v[144:147], v[188:191], a[116:119]\n"
"v_exp_f32 v163, v163\n"
"v_mfma_f32_16x16x32_bf16 a[140:143], v[148:151], v[188:191], a[140:143]\n"
"v_add_f32 v216, v216, v162\n"
"v_mfma_f32_16x16x32_bf16 a[164:167], v[152:155], v[188:191], a[164:167]\n"
"v_exp_f32 v164, v164\n"
"v_mfma_f32_16x16x32_bf16 a[188:191], v[156:159], v[188:191], a[188:191]\n"
"v_add_f32 v216, v216, v163\n"
"s_waitcnt lgkmcnt(0)\n"
"v_mfma_f32_16x16x32_bf16 v[168:171], a[192:195], v[16:19], v[196:199]\n"
"v_exp_f32 v165, v165\n"
"v_mfma_f32_16x16x32_bf16 v[168:171], a[196:199], v[20:23], v[168:171]\n"
"v_add_f32 v216, v216, v164\n"
"v_cvt_pk_bf16_f32 v185, v162, v163\n"
"v_mfma_f32_16x16x32_bf16 v[168:171], a[200:203], v[24:27], v[168:171]\n"
"v_exp_f32 v166, v166\n"
"v_mfma_f32_16x16x32_bf16 v[168:171], a[204:207], v[28:31], v[168:171]\n"
"v_add_f32 v216, v216, v165\n"
"v_cvt_pk_bf16_f32 v186, v164, v165\n"
"v_mfma_f32_16x16x32_bf16 v[172:175], a[208:211], v[16:19], v[196:199]\n"
"v_exp_f32 v167, v167\n"
"v_mfma_f32_16x16x32_bf16 v[172:175], a[212:215], v[20:23], v[172:175]\n"
"v_cvt_pk_bf16_f32 v187, v166, v167\n"
"v_add_f32 v216, v216, v166\n"
"v_mfma_f32_16x16x32_bf16 v[172:175], a[216:219], v[24:27], v[172:175]\n"
"v_exp_f32 v168, v168\n"
"v_mfma_f32_16x16x32_bf16 v[172:175], a[220:223], v[28:31], v[172:175]\n"
"ds_read_b128 v[96:99], v222 offset:49152\n"
"v_add_f32 v216, v216, v167\n"
"v_mfma_f32_16x16x32_bf16 a[0:3], a[224:227], v[184:187], a[0:3]\n"
"v_exp_f32 v169, v169\n"
"v_mfma_f32_16x16x32_bf16 a[24:27], a[228:231], v[184:187], a[24:27]\n"
"ds_read_b128 v[100:103], v223 offset:49152\n"
"v_add_f32 v217, v217, v168\n"
"v_mfma_f32_16x16x32_bf16 a[48:51], a[232:235], v[184:187], a[48:51]\n"
"v_exp_f32 v170, v170\n"
"v_mfma_f32_16x16x32_bf16 a[72:75], a[236:239], v[184:187], a[72:75]\n"
"v_add_f32 v217, v217, v169\n"
"v_cvt_pk_bf16_f32 v188, v168, v169\n"
"v_mfma_f32_16x16x32_bf16 a[96:99], a[240:243], v[184:187], a[96:99]\n"
"v_exp_f32 v171, v171\n"
"v_mfma_f32_16x16x32_bf16 a[120:123], a[244:247], v[184:187], a[120:123]\n"
"ds_read_b64_tr_b16 v[128:129], v226 offset:49152\n"
"v_add_f32 v217, v217, v170\n"
"v_mfma_f32_16x16x32_bf16 a[144:147], a[248:251], v[184:187], a[144:147]\n"
"v_exp_f32 v172, v172\n"
"v_mfma_f32_16x16x32_bf16 a[168:171], a[252:255], v[184:187], a[168:171]\n"
"ds_read_b64_tr_b16 v[130:131], v226 offset:50176\n"
"v_add_f32 v217, v217, v171\n"
"v_mfma_f32_16x16x32_bf16 v[176:179], a[192:195], v[32:35], v[200:203]\n"
"v_exp_f32 v173, v173\n"
"v_mfma_f32_16x16x32_bf16 v[176:179], a[196:199], v[36:39], v[176:179]\n"
"v_add_f32 v217, v217, v172\n"
"v_cvt_pk_bf16_f32 v189, v170, v171\n"
"v_mfma_f32_16x16x32_bf16 v[176:179], a[200:203], v[40:43], v[176:179]\n"
"v_exp_f32 v174, v174\n"
"v_mfma_f32_16x16x32_bf16 v[176:179], a[204:207], v[44:47], v[176:179]\n"
"v_add_f32 v217, v217, v173\n"
"v_cvt_pk_bf16_f32 v190, v172, v173\n"
"v_mfma_f32_16x16x32_bf16 v[180:183], a[208:211], v[32:35], v[200:203]\n"
"v_exp_f32 v175, v175\n"
"v_mfma_f32_16x16x32_bf16 v[180:183], a[212:215], v[36:39], v[180:183]\n"
"v_cvt_pk_bf16_f32 v191, v174, v175\n"
"v_add_f32 v217, v217, v174\n"
"v_mfma_f32_16x16x32_bf16 v[180:183], a[216:219], v[40:43], v[180:183]\n"
"v_exp_f32 v176, v176\n"
"v_mfma_f32_16x16x32_bf16 v[180:183], a[220:223], v[44:47], v[180:183]\n"
"ds_read_b128 v[104:107], v224 offset:49152\n"
"v_add_f32 v217, v217, v175\n"
"v_mfma_f32_16x16x32_bf16 a[4:7], a[224:227], v[188:191], a[4:7]\n"
"v_exp_f32 v177, v177\n"
"v_mfma_f32_16x16x32_bf16 a[28:31], a[228:231], v[188:191], a[28:31]\n"
"ds_read_b128 v[108:111], v225 offset:49152\n"
"v_add_f32 v218, v218, v176\n"
"v_mfma_f32_16x16x32_bf16 a[52:55], a[232:235], v[188:191], a[52:55]\n"
"v_exp_f32 v178, v178\n"
"v_mfma_f32_16x16x32_bf16 a[76:79], a[236:239], v[188:191], a[76:79]\n"
"v_add_f32 v218, v218, v177\n"
"v_cvt_pk_bf16_f32 v184, v176, v177\n"
"v_mfma_f32_16x16x32_bf16 a[100:103], a[240:243], v[188:191], a[100:103]\n"
"v_exp_f32 v179, v179\n"
"v_mfma_f32_16x16x32_bf16 a[124:127], a[244:247], v[188:191], a[124:127]\n"
"ds_read_b64_tr_b16 v[132:133], v227 offset:49152\n"
"v_add_f32 v218, v218, v178\n"
"v_mfma_f32_16x16x32_bf16 a[148:151], a[248:251], v[188:191], a[148:151]\n"
"v_exp_f32 v180, v180\n"
"v_mfma_f32_16x16x32_bf16 a[172:175], a[252:255], v[188:191], a[172:175]\n"
"ds_read_b64_tr_b16 v[134:135], v227 offset:50176\n"
"v_add_f32 v218, v218, v179\n"
"v_mfma_f32_16x16x32_bf16 v[160:163], a[192:195], v[48:51], v[204:207]\n"
"v_exp_f32 v181, v181\n"
"v_mfma_f32_16x16x32_bf16 v[160:163], a[196:199], v[52:55], v[160:163]\n"
"v_add_f32 v218, v218, v180\n"
"v_cvt_pk_bf16_f32 v185, v178, v179\n"
"v_mfma_f32_16x16x32_bf16 v[160:163], a[200:203], v[56:59], v[160:163]\n"
"v_exp_f32 v182, v182\n"
"v_mfma_f32_16x16x32_bf16 v[160:163], a[204:207], v[60:63], v[160:163]\n"
"v_add_f32 v218, v218, v181\n"
"v_cvt_pk_bf16_f32 v186, v180, v181\n"
"v_mfma_f32_16x16x32_bf16 v[164:167], a[208:211], v[48:51], v[204:207]\n"
"v_exp_f32 v183, v183\n"
"v_mfma_f32_16x16x32_bf16 v[164:167], a[212:215], v[52:55], v[164:167]\n"
"v_cvt_pk_bf16_f32 v187, v182, v183\n"
"v_add_f32 v218, v218, v182\n"
"v_mfma_f32_16x16x32_bf16 v[164:167], a[216:219], v[56:59], v[164:167]\n"
"v_exp_f32 v160, v160\n"
"v_mfma_f32_16x16x32_bf16 v[164:167], a[220:223], v[60:63], v[164:167]\n"
"ds_read_b128 v[112:115], v222 offset:50176\n"
"v_add_f32 v218, v218, v183\n"
"v_mfma_f32_16x16x32_bf16 a[8:11], a[224:227], v[184:187], a[8:11]\n"
"v_exp_f32 v161, v161\n"
"v_mfma_f32_16x16x32_bf16 a[32:35], a[228:231], v[184:187], a[32:35]\n"
"ds_read_b128 v[116:119], v223 offset:50176\n"
"v_add_f32 v219, v219, v160\n"
"v_mfma_f32_16x16x32_bf16 a[56:59], a[232:235], v[184:187], a[56:59]\n"
"v_exp_f32 v162, v162\n"
"v_mfma_f32_16x16x32_bf16 a[80:83], a[236:239], v[184:187], a[80:83]\n"
"v_add_f32 v219, v219, v161\n"
"v_cvt_pk_bf16_f32 v188, v160, v161\n"
"v_mfma_f32_16x16x32_bf16 a[104:107], a[240:243], v[184:187], a[104:107]\n"
"v_exp_f32 v163, v163\n"
"v_mfma_f32_16x16x32_bf16 a[128:131], a[244:247], v[184:187], a[128:131]\n"
"ds_read_b64_tr_b16 v[136:137], v228 offset:49152\n"
"v_add_f32 v219, v219, v162\n"
"v_mfma_f32_16x16x32_bf16 a[152:155], a[248:251], v[184:187], a[152:155]\n"
"v_exp_f32 v164, v164\n"
"v_mfma_f32_16x16x32_bf16 a[176:179], a[252:255], v[184:187], a[176:179]\n"
"ds_read_b64_tr_b16 v[138:139], v228 offset:50176\n"
"v_add_f32 v219, v219, v163\n"
"v_mfma_f32_16x16x32_bf16 v[168:171], a[192:195], v[64:67], v[208:211]\n"
"v_exp_f32 v165, v165\n"
"v_mfma_f32_16x16x32_bf16 v[168:171], a[196:199], v[68:71], v[168:171]\n"
"v_add_f32 v219, v219, v164\n"
"v_cvt_pk_bf16_f32 v189, v162, v163\n"
"v_mfma_f32_16x16x32_bf16 v[168:171], a[200:203], v[72:75], v[168:171]\n"
"v_exp_f32 v166, v166\n"
"v_mfma_f32_16x16x32_bf16 v[168:171], a[204:207], v[76:79], v[168:171]\n"
"v_add_f32 v219, v219, v165\n"
"v_cvt_pk_bf16_f32 v190, v164, v165\n"
"v_mfma_f32_16x16x32_bf16 v[172:175], a[208:211], v[64:67], v[208:211]\n"
"v_exp_f32 v167, v167\n"
"v_mfma_f32_16x16x32_bf16 v[172:175], a[212:215], v[68:71], v[172:175]\n"
"v_cvt_pk_bf16_f32 v191, v166, v167\n"
"v_add_f32 v219, v219, v166\n"
"v_mfma_f32_16x16x32_bf16 v[172:175], a[216:219], v[72:75], v[172:175]\n"
"v_exp_f32 v168, v168\n"
"v_mfma_f32_16x16x32_bf16 v[172:175], a[220:223], v[76:79], v[172:175]\n"
"ds_read_b128 v[120:123], v224 offset:50176\n"
"v_add_f32 v219, v219, v167\n"
"v_mfma_f32_16x16x32_bf16 a[12:15], a[224:227], v[188:191], a[12:15]\n"
"v_exp_f32 v169, v169\n"
"v_mfma_f32_16x16x32_bf16 a[36:39], a[228:231], v[188:191], a[36:39]\n"
"ds_read_b128 v[124:127], v225 offset:50176\n"
"v_add_f32 v220, v220, v168\n"
"v_mfma_f32_16x16x32_bf16 a[60:63], a[232:235], v[188:191], a[60:63]\n"
"v_exp_f32 v170, v170\n"
"v_mfma_f32_16x16x32_bf16 a[84:87], a[236:239], v[188:191], a[84:87]\n"
"v_add_f32 v220, v220, v169\n"
"v_cvt_pk_bf16_f32 v184, v168, v169\n"
"v_mfma_f32_16x16x32_bf16 a[108:111], a[240:243], v[188:191], a[108:111]\n"
"v_exp_f32 v171, v171\n"
"v_mfma_f32_16x16x32_bf16 a[132:135], a[244:247], v[188:191], a[132:135]\n"
"ds_read_b64_tr_b16 v[140:141], v229 offset:49152\n"
"v_add_f32 v220, v220, v170\n"
"v_mfma_f32_16x16x32_bf16 a[156:159], a[248:251], v[188:191], a[156:159]\n"
"v_exp_f32 v172, v172\n"
"v_mfma_f32_16x16x32_bf16 a[180:183], a[252:255], v[188:191], a[180:183]\n"
"ds_read_b64_tr_b16 v[142:143], v229 offset:50176\n"
"v_add_f32 v220, v220, v171\n"
"v_mfma_f32_16x16x32_bf16 v[176:179], a[192:195], v[80:83], v[212:215]\n"
"v_exp_f32 v173, v173\n"
"v_mfma_f32_16x16x32_bf16 v[176:179], a[196:199], v[84:87], v[176:179]\n"
"v_add_f32 v220, v220, v172\n"
"v_cvt_pk_bf16_f32 v185, v170, v171\n"
"v_mfma_f32_16x16x32_bf16 v[176:179], a[200:203], v[88:91], v[176:179]\n"
"v_exp_f32 v174, v174\n"
"v_mfma_f32_16x16x32_bf16 v[176:179], a[204:207], v[92:95], v[176:179]\n"
"v_add_f32 v220, v220, v173\n"
"v_cvt_pk_bf16_f32 v186, v172, v173\n"
"v_mfma_f32_16x16x32_bf16 v[180:183], a[208:211], v[80:83], v[212:215]\n"
"v_exp_f32 v175, v175\n"
"v_mfma_f32_16x16x32_bf16 v[180:183], a[212:215], v[84:87], v[180:183]\n"
"v_cvt_pk_bf16_f32 v187, v174, v175\n"
"v_add_f32 v220, v220, v174\n"
"v_mfma_f32_16x16x32_bf16 v[180:183], a[216:219], v[88:91], v[180:183]\n"
"v_exp_f32 v176, v176\n"
"v_mfma_f32_16x16x32_bf16 v[180:183], a[220:223], v[92:95], v[180:183]\n"
"ds_read_b64_tr_b16 v[144:145], v230 offset:49152\n"
"v_add_f32 v220, v220, v175\n"
"v_mfma_f32_16x16x32_bf16 a[16:19], a[224:227], v[184:187], a[16:19]\n"
"v_exp_f32 v177, v177\n"
"v_mfma_f32_16x16x32_bf16 a[40:43], a[228:231], v[184:187], a[40:43]\n"
"ds_read_b64_tr_b16 v[146:147], v230 offset:50176\n"
"v_add_f32 v221, v221, v176\n"
"v_mfma_f32_16x16x32_bf16 a[64:67], a[232:235], v[184:187], a[64:67]\n"
"v_exp_f32 v178, v178\n"
"v_mfma_f32_16x16x32_bf16 a[88:91], a[236:239], v[184:187], a[88:91]\n"
"v_add_f32 v221, v221, v177\n"
"v_cvt_pk_bf16_f32 v188, v176, v177\n"
"v_mfma_f32_16x16x32_bf16 a[112:115], a[240:243], v[184:187], a[112:115]\n"
"v_exp_f32 v179, v179\n"
"v_mfma_f32_16x16x32_bf16 a[136:139], a[244:247], v[184:187], a[136:139]\n"
"ds_read_b64_tr_b16 v[148:149], v231 offset:49152\n"
"v_add_f32 v221, v221, v178\n"
"v_mfma_f32_16x16x32_bf16 a[160:163], a[248:251], v[184:187], a[160:163]\n"
"v_exp_f32 v180, v180\n"
"v_mfma_f32_16x16x32_bf16 a[184:187], a[252:255], v[184:187], a[184:187]\n"
"ds_read_b64_tr_b16 v[150:151], v231 offset:50176\n"
"v_add_f32 v221, v221, v179\n"
"s_waitcnt lgkmcnt(8)\n"
"v_mfma_f32_16x16x32_bf16 v[160:163], v[96:99], v[0:3], v[192:195]\n"
"v_exp_f32 v181, v181\n"
"v_mfma_f32_16x16x32_bf16 v[160:163], v[100:103], v[4:7], v[160:163]\n"
"v_add_f32 v221, v221, v180\n"
"v_cvt_pk_bf16_f32 v189, v178, v179\n"
"v_mfma_f32_16x16x32_bf16 v[160:163], v[104:107], v[8:11], v[160:163]\n"
"v_exp_f32 v182, v182\n"
"v_mfma_f32_16x16x32_bf16 v[160:163], v[108:111], v[12:15], v[160:163]\n"
"ds_read_b64_tr_b16 v[152:153], v232 offset:49152\n"
"v_add_f32 v221, v221, v181\n"
"v_cvt_pk_bf16_f32 v190, v180, v181\n"
"v_mfma_f32_16x16x32_bf16 v[164:167], v[112:115], v[0:3], v[192:195]\n"
"v_exp_f32 v183, v183\n"
"v_mfma_f32_16x16x32_bf16 v[164:167], v[116:119], v[4:7], v[164:167]\n"
"ds_read_b64_tr_b16 v[154:155], v232 offset:50176\n"
"v_cvt_pk_bf16_f32 v191, v182, v183\n"
"v_add_f32 v221, v221, v182\n"
"s_waitcnt lgkmcnt(9)\n"
"v_mfma_f32_16x16x32_bf16 v[164:167], v[120:123], v[8:11], v[164:167]\n"
"v_exp_f32 v160, v160\n"
"s_waitcnt lgkmcnt(8)\n"
"v_mfma_f32_16x16x32_bf16 v[164:167], v[124:127], v[12:15], v[164:167]\n"
"ds_read_b64_tr_b16 v[156:157], v233 offset:49152\n"
"v_add_f32 v221, v221, v183\n"
"v_mfma_f32_16x16x32_bf16 a[20:23], a[224:227], v[188:191], a[20:23]\n"
"v_exp_f32 v161, v161\n"
"v_mfma_f32_16x16x32_bf16 a[44:47], a[228:231], v[188:191], a[44:47]\n"
"ds_read_b64_tr_b16 v[158:159], v233 offset:50176\n"
"v_add_f32 v216, v216, v160\n"
"v_mfma_f32_16x16x32_bf16 a[68:71], a[232:235], v[188:191], a[68:71]\n"
"v_exp_f32 v162, v162\n"
"v_mfma_f32_16x16x32_bf16 a[92:95], a[236:239], v[188:191], a[92:95]\n"
"v_add_f32 v216, v216, v161\n"
"v_cvt_pk_bf16_f32 v184, v160, v161\n"
"v_mfma_f32_16x16x32_bf16 a[116:119], a[240:243], v[188:191], a[116:119]\n"
"v_exp_f32 v163, v163\n"
"v_mfma_f32_16x16x32_bf16 a[140:143], a[244:247], v[188:191], a[140:143]\n"
"v_add_f32 v216, v216, v162\n"
"v_mfma_f32_16x16x32_bf16 a[164:167], a[248:251], v[188:191], a[164:167]\n"
"v_exp_f32 v164, v164\n"
"v_mfma_f32_16x16x32_bf16 a[188:191], a[252:255], v[188:191], a[188:191]\n"
"v_add_f32 v216, v216, v163\n"
"s_waitcnt vmcnt(0) lgkmcnt(0)\n"
"s_barrier\n"
"v_mfma_f32_16x16x32_bf16 v[168:171], v[96:99], v[16:19], v[196:199]\n"
"v_exp_f32 v165, v165\n"
"v_mfma_f32_16x16x32_bf16 v[168:171], v[100:103], v[20:23], v[168:171]\n"
"s_add_u32 m0, s57, 32768\n"
"v_add_f32 v216, v216, v164\n"
"v_cvt_pk_bf16_f32 v185, v162, v163\n"
"v_mfma_f32_16x16x32_bf16 v[168:171], v[104:107], v[24:27], v[168:171]\n"
"buffer_load_dwordx4 v234, s[36:39], s56 offen offset:0 nt lds\n"
"v_exp_f32 v166, v166\n"
"v_mfma_f32_16x16x32_bf16 v[168:171], v[108:111], v[28:31], v[168:171]\n"
"v_add_f32 v216, v216, v165\n"
"v_cvt_pk_bf16_f32 v186, v164, v165\n"
"v_mfma_f32_16x16x32_bf16 v[172:175], v[112:115], v[16:19], v[196:199]\n"
"v_exp_f32 v167, v167\n"
"v_mfma_f32_16x16x32_bf16 v[172:175], v[116:119], v[20:23], v[172:175]\n"
"v_cvt_pk_bf16_f32 v187, v166, v167\n"
"v_add_f32 v216, v216, v166\n"
"v_mfma_f32_16x16x32_bf16 v[172:175], v[120:123], v[24:27], v[172:175]\n"
"buffer_load_dwordx4 v234, s[36:39], s56 offen offset:1024 nt lds\n"
"v_exp_f32 v168, v168\n"
"v_mfma_f32_16x16x32_bf16 v[172:175], v[124:127], v[28:31], v[172:175]\n"
"ds_read_b128 a[192:195], v222 offset:0\n"
"v_add_f32 v216, v216, v167\n"
"v_mfma_f32_16x16x32_bf16 a[0:3], v[128:131], v[184:187], a[0:3]\n"
"v_exp_f32 v169, v169\n"
"v_mfma_f32_16x16x32_bf16 a[24:27], v[132:135], v[184:187], a[24:27]\n"
"ds_read_b128 a[196:199], v223 offset:0\n"
"v_add_f32 v217, v217, v168\n"
"v_mfma_f32_16x16x32_bf16 a[48:51], v[136:139], v[184:187], a[48:51]\n"
"buffer_load_dwordx4 v235, s[36:39], s56 offen offset:2048 nt lds\n"
"v_exp_f32 v170, v170\n"
"v_mfma_f32_16x16x32_bf16 a[72:75], v[140:143], v[184:187], a[72:75]\n"
"v_add_f32 v217, v217, v169\n"
"v_cvt_pk_bf16_f32 v188, v168, v169\n"
"v_mfma_f32_16x16x32_bf16 a[96:99], v[144:147], v[184:187], a[96:99]\n"
"v_exp_f32 v171, v171\n"
"v_mfma_f32_16x16x32_bf16 a[120:123], v[148:151], v[184:187], a[120:123]\n"
"ds_read_b64_tr_b16 a[224:225], v226 offset:0\n"
"v_add_f32 v217, v217, v170\n"
"v_mfma_f32_16x16x32_bf16 a[144:147], v[152:155], v[184:187], a[144:147]\n"
"buffer_load_dwordx4 v235, s[36:39], s56 offen offset:3072 nt lds\n"
"v_exp_f32 v172, v172\n"
"v_mfma_f32_16x16x32_bf16 a[168:171], v[156:159], v[184:187], a[168:171]\n"
"ds_read_b64_tr_b16 a[226:227], v226 offset:1024\n"
"v_add_f32 v217, v217, v171\n"
"v_mfma_f32_16x16x32_bf16 v[176:179], v[96:99], v[32:35], v[200:203]\n"
"v_exp_f32 v173, v173\n"
"v_mfma_f32_16x16x32_bf16 v[176:179], v[100:103], v[36:39], v[176:179]\n"
"s_add_u32 m0, s58, 32768\n"
"v_add_f32 v217, v217, v172\n"
"v_cvt_pk_bf16_f32 v189, v170, v171\n"
"v_mfma_f32_16x16x32_bf16 v[176:179], v[104:107], v[40:43], v[176:179]\n"
"buffer_load_dwordx4 v236, s[40:43], s56 offen offset:0 nt lds\n"
"v_exp_f32 v174, v174\n"
"v_mfma_f32_16x16x32_bf16 v[176:179], v[108:111], v[44:47], v[176:179]\n"
"v_add_f32 v217, v217, v173\n"
"v_cvt_pk_bf16_f32 v190, v172, v173\n"
"v_mfma_f32_16x16x32_bf16 v[180:183], v[112:115], v[32:35], v[200:203]\n"
"v_exp_f32 v175, v175\n"
"v_mfma_f32_16x16x32_bf16 v[180:183], v[116:119], v[36:39], v[180:183]\n"
"v_cvt_pk_bf16_f32 v191, v174, v175\n"
"v_add_f32 v217, v217, v174\n"
"v_mfma_f32_16x16x32_bf16 v[180:183], v[120:123], v[40:43], v[180:183]\n"
"buffer_load_dwordx4 v236, s[40:43], s56 offen offset:1024 nt lds\n"
"v_exp_f32 v176, v176\n"
"v_mfma_f32_16x16x32_bf16 v[180:183], v[124:127], v[44:47], v[180:183]\n"
"ds_read_b128 a[200:203], v224 offset:0\n"
"v_add_f32 v217, v217, v175\n"
"v_mfma_f32_16x16x32_bf16 a[4:7], v[128:131], v[188:191], a[4:7]\n"
"v_exp_f32 v177, v177\n"
"v_mfma_f32_16x16x32_bf16 a[28:31], v[132:135], v[188:191], a[28:31]\n"
"ds_read_b128 a[204:207], v225 offset:0\n"
"v_add_f32 v218, v218, v176\n"
"v_mfma_f32_16x16x32_bf16 a[52:55], v[136:139], v[188:191], a[52:55]\n"
"buffer_load_dwordx4 v237, s[40:43], s56 offen offset:2048 nt lds\n"
"v_exp_f32 v178, v178\n"
"v_mfma_f32_16x16x32_bf16 a[76:79], v[140:143], v[188:191], a[76:79]\n"
"v_add_f32 v218, v218, v177\n"
"v_cvt_pk_bf16_f32 v184, v176, v177\n"
"v_mfma_f32_16x16x32_bf16 a[100:103], v[144:147], v[188:191], a[100:103]\n"
"v_exp_f32 v179, v179\n"
"v_mfma_f32_16x16x32_bf16 a[124:127], v[148:151], v[188:191], a[124:127]\n"
"ds_read_b64_tr_b16 a[228:229], v227 offset:0\n"
"v_add_f32 v218, v218, v178\n"
"v_mfma_f32_16x16x32_bf16 a[148:151], v[152:155], v[188:191], a[148:151]\n"
"buffer_load_dwordx4 v237, s[40:43], s56 offen offset:3072 nt lds\n"
"v_exp_f32 v180, v180\n"
"v_mfma_f32_16x16x32_bf16 a[172:175], v[156:159], v[188:191], a[172:175]\n"
"ds_read_b64_tr_b16 a[230:231], v227 offset:1024\n"
"v_add_f32 v218, v218, v179\n"
"v_mfma_f32_16x16x32_bf16 v[160:163], v[96:99], v[48:51], v[204:207]\n"
"s_add_u32 s56, s56, 8192\n"
"s_and_b32 s56, s56, 0x1ffff\n"
"v_exp_f32 v181, v181\n"
"v_mfma_f32_16x16x32_bf16 v[160:163], v[100:103], v[52:55], v[160:163]\n"
"s_add_u32 m0, s57, 49152\n"
"v_add_f32 v218, v218, v180\n"
"v_cvt_pk_bf16_f32 v185, v178, v179\n"
"v_mfma_f32_16x16x32_bf16 v[160:163], v[104:107], v[56:59], v[160:163]\n"
"buffer_load_dwordx4 v234, s[36:39], s56 offen offset:0 nt lds\n"
"v_exp_f32 v182, v182\n"
"v_mfma_f32_16x16x32_bf16 v[160:163], v[108:111], v[60:63], v[160:163]\n"
"v_add_f32 v218, v218, v181\n"
"v_cvt_pk_bf16_f32 v186, v180, v181\n"
"v_mfma_f32_16x16x32_bf16 v[164:167], v[112:115], v[48:51], v[204:207]\n"
"v_exp_f32 v183, v183\n"
"v_mfma_f32_16x16x32_bf16 v[164:167], v[116:119], v[52:55], v[164:167]\n"
"v_cvt_pk_bf16_f32 v187, v182, v183\n"
"v_add_f32 v218, v218, v182\n"
"v_mfma_f32_16x16x32_bf16 v[164:167], v[120:123], v[56:59], v[164:167]\n"
"buffer_load_dwordx4 v234, s[36:39], s56 offen offset:1024 nt lds\n"
"v_exp_f32 v160, v160\n"
"v_mfma_f32_16x16x32_bf16 v[164:167], v[124:127], v[60:63], v[164:167]\n"
"ds_read_b128 a[208:211], v222 offset:1024\n"
"v_add_f32 v218, v218, v183\n"
"v_mfma_f32_16x16x32_bf16 a[8:11], v[128:131], v[184:187], a[8:11]\n"
"v_exp_f32 v161, v161\n"
"v_mfma_f32_16x16x32_bf16 a[32:35], v[132:135], v[184:187], a[32:35]\n"
"ds_read_b128 a[212:215], v223 offset:1024\n"
"v_add_f32 v219, v219, v160\n"
"v_mfma_f32_16x16x32_bf16 a[56:59], v[136:139], v[184:187], a[56:59]\n"
"buffer_load_dwordx4 v235, s[36:39], s56 offen offset:2048 nt lds\n"
"v_exp_f32 v162, v162\n"
"v_mfma_f32_16x16x32_bf16 a[80:83], v[140:143], v[184:187], a[80:83]\n"
"v_add_f32 v219, v219, v161\n"
"v_cvt_pk_bf16_f32 v188, v160, v161\n"
"v_mfma_f32_16x16x32_bf16 a[104:107], v[144:147], v[184:187], a[104:107]\n"
"v_exp_f32 v163, v163\n"
"v_mfma_f32_16x16x32_bf16 a[128:131], v[148:151], v[184:187], a[128:131]\n"
"ds_read_b64_tr_b16 a[232:233], v228 offset:0\n"
"v_add_f32 v219, v219, v162\n"
"v_mfma_f32_16x16x32_bf16 a[152:155], v[152:155], v[184:187], a[152:155]\n"
"buffer_load_dwordx4 v235, s[36:39], s56 offen offset:3072 nt lds\n"
"v_exp_f32 v164, v164\n"
"v_mfma_f32_16x16x32_bf16 a[176:179], v[156:159], v[184:187], a[176:179]\n"
"ds_read_b64_tr_b16 a[234:235], v228 offset:1024\n"
"v_add_f32 v219, v219, v163\n"
"v_mfma_f32_16x16x32_bf16 v[168:171], v[96:99], v[64:67], v[208:211]\n"
"v_exp_f32 v165, v165\n"
"v_mfma_f32_16x16x32_bf16 v[168:171], v[100:103], v[68:71], v[168:171]\n"
"s_add_u32 m0, s58, 49152\n"
"v_add_f32 v219, v219, v164\n"
"v_cvt_pk_bf16_f32 v189, v162, v163\n"
"v_mfma_f32_16x16x32_bf16 v[168:171], v[104:107], v[72:75], v[168:171]\n"
"buffer_load_dwordx4 v236, s[40:43], s56 offen offset:0 nt lds\n"
"v_exp_f32 v166, v166\n"
"v_mfma_f32_16x16x32_bf16 v[168:171], v[108:111], v[76:79], v[168:171]\n"
"v_add_f32 v219, v219, v165\n"
"v_cvt_pk_bf16_f32 v190, v164, v165\n"
"v_mfma_f32_16x16x32_bf16 v[172:175], v[112:115], v[64:67], v[208:211]\n"
"v_exp_f32 v167, v167\n"
"v_mfma_f32_16x16x32_bf16 v[172:175], v[116:119], v[68:71], v[172:175]\n"
"v_cvt_pk_bf16_f32 v191, v166, v167\n"
"v_add_f32 v219, v219, v166\n"
"v_mfma_f32_16x16x32_bf16 v[172:175], v[120:123], v[72:75], v[172:175]\n"
"buffer_load_dwordx4 v236, s[40:43], s56 offen offset:1024 nt lds\n"
"v_exp_f32 v168, v168\n"
"v_mfma_f32_16x16x32_bf16 v[172:175], v[124:127], v[76:79], v[172:175]\n"
"ds_read_b128 a[216:219], v224 offset:1024\n"
"v_add_f32 v219, v219, v167\n"
"v_mfma_f32_16x16x32_bf16 a[12:15], v[128:131], v[188:191], a[12:15]\n"
"v_exp_f32 v169, v169\n"
"v_mfma_f32_16x16x32_bf16 a[36:39], v[132:135], v[188:191], a[36:39]\n"
"ds_read_b128 a[220:223], v225 offset:1024\n"
"v_add_f32 v220, v220, v168\n"
"v_mfma_f32_16x16x32_bf16 a[60:63], v[136:139], v[188:191], a[60:63]\n"
"buffer_load_dwordx4 v237, s[40:43], s56 offen offset:2048 nt lds\n"
"v_exp_f32 v170, v170\n"
"v_mfma_f32_16x16x32_bf16 a[84:87], v[140:143], v[188:191], a[84:87]\n"
"v_add_f32 v220, v220, v169\n"
"v_cvt_pk_bf16_f32 v184, v168, v169\n"
"v_mfma_f32_16x16x32_bf16 a[108:111], v[144:147], v[188:191], a[108:111]\n"
"v_exp_f32 v171, v171\n"
"v_mfma_f32_16x16x32_bf16 a[132:135], v[148:151], v[188:191], a[132:135]\n"
"ds_read_b64_tr_b16 a[236:237], v229 offset:0\n"
"v_add_f32 v220, v220, v170\n"
"v_mfma_f32_16x16x32_bf16 a[156:159], v[152:155], v[188:191], a[156:159]\n"
"buffer_load_dwordx4 v237, s[40:43], s56 offen offset:3072 nt lds\n"
"v_exp_f32 v172, v172\n"
"v_mfma_f32_16x16x32_bf16 a[180:183], v[156:159], v[188:191], a[180:183]\n"
"ds_read_b64_tr_b16 a[238:239], v229 offset:1024\n"
"s_add_u32 s56, s56, 8192\n"
"s_and_b32 s56, s56, 0x1ffff\n"
"v_add_f32 v220, v220, v171\n"
"v_mfma_f32_16x16x32_bf16 v[176:179], v[96:99], v[80:83], v[212:215]\n"
"v_exp_f32 v173, v173\n"
"v_mfma_f32_16x16x32_bf16 v[176:179], v[100:103], v[84:87], v[176:179]\n"
"v_add_f32 v220, v220, v172\n"
"v_cvt_pk_bf16_f32 v185, v170, v171\n"
"v_mfma_f32_16x16x32_bf16 v[176:179], v[104:107], v[88:91], v[176:179]\n"
"v_exp_f32 v174, v174\n"
"v_mfma_f32_16x16x32_bf16 v[176:179], v[108:111], v[92:95], v[176:179]\n"
"v_add_f32 v220, v220, v173\n"
"v_cvt_pk_bf16_f32 v186, v172, v173\n"
"v_mfma_f32_16x16x32_bf16 v[180:183], v[112:115], v[80:83], v[212:215]\n"
"v_exp_f32 v175, v175\n"
"v_mfma_f32_16x16x32_bf16 v[180:183], v[116:119], v[84:87], v[180:183]\n"
"v_cvt_pk_bf16_f32 v187, v174, v175\n"
"v_add_f32 v220, v220, v174\n"
"v_mfma_f32_16x16x32_bf16 v[180:183], v[120:123], v[88:91], v[180:183]\n"
"v_exp_f32 v176, v176\n"
"v_mfma_f32_16x16x32_bf16 v[180:183], v[124:127], v[92:95], v[180:183]\n"
"ds_read_b64_tr_b16 a[240:241], v230 offset:0\n"
"v_add_f32 v220, v220, v175\n"
"v_mfma_f32_16x16x32_bf16 a[16:19], v[128:131], v[184:187], a[16:19]\n"
"v_exp_f32 v177, v177\n"
"v_mfma_f32_16x16x32_bf16 a[40:43], v[132:135], v[184:187], a[40:43]\n"
"ds_read_b64_tr_b16 a[242:243], v230 offset:1024\n"
"v_add_f32 v221, v221, v176\n"
"v_mfma_f32_16x16x32_bf16 a[64:67], v[136:139], v[184:187], a[64:67]\n"
"v_exp_f32 v178, v178\n"
"v_mfma_f32_16x16x32_bf16 a[88:91], v[140:143], v[184:187], a[88:91]\n"
"v_add_f32 v221, v221, v177\n"
"v_cvt_pk_bf16_f32 v188, v176, v177\n"
"v_mfma_f32_16x16x32_bf16 a[112:115], v[144:147], v[184:187], a[112:115]\n"
"v_exp_f32 v179, v179\n"
"v_mfma_f32_16x16x32_bf16 a[136:139], v[148:151], v[184:187], a[136:139]\n"
"ds_read_b64_tr_b16 a[244:245], v231 offset:0\n"
"v_add_f32 v221, v221, v178\n"
"v_mfma_f32_16x16x32_bf16 a[160:163], v[152:155], v[184:187], a[160:163]\n"
"v_exp_f32 v180, v180\n"
"v_mfma_f32_16x16x32_bf16 a[184:187], v[156:159], v[184:187], a[184:187]\n"
"ds_read_b64_tr_b16 a[246:247], v231 offset:1024\n"
"v_add_f32 v221, v221, v179\n"
"s_waitcnt lgkmcnt(8)\n"
"v_mfma_f32_16x16x32_bf16 v[160:163], a[192:195], v[0:3], v[192:195]\n"
"v_exp_f32 v181, v181\n"
"v_mfma_f32_16x16x32_bf16 v[160:163], a[196:199], v[4:7], v[160:163]\n"
"v_add_f32 v221, v221, v180\n"
"v_cvt_pk_bf16_f32 v189, v178, v179\n"
"v_mfma_f32_16x16x32_bf16 v[160:163], a[200:203], v[8:11], v[160:163]\n"
"v_exp_f32 v182, v182\n"
"v_mfma_f32_16x16x32_bf16 v[160:163], a[204:207], v[12:15], v[160:163]\n"
"ds_read_b64_tr_b16 a[248:249], v232 offset:0\n"
"v_add_f32 v221, v221, v181\n"
"v_cvt_pk_bf16_f32 v190, v180, v181\n"
"v_mfma_f32_16x16x32_bf16 v[164:167], a[208:211], v[0:3], v[192:195]\n"
"v_exp_f32 v183, v183\n"
"v_mfma_f32_16x16x32_bf16 v[164:167], a[212:215], v[4:7], v[164:167]\n"
"ds_read_b64_tr_b16 a[250:251], v232 offset:1024\n"
"v_cvt_pk_bf16_f32 v191, v182, v183\n"
"v_add_f32 v221, v221, v182\n"
"s_waitcnt lgkmcnt(9)\n"
"v_mfma_f32_16x16x32_bf16 v[164:167], a[216:219], v[8:11], v[164:167]\n"
"v_exp_f32 v160, v160\n"
"s_waitcnt lgkmcnt(8)\n"
"v_mfma_f32_16x16x32_bf16 v[164:167], a[220:223], v[12:15], v[164:167]\n"
"ds_read_b64_tr_b16 a[252:253], v233 offset:0\n"
"v_add_f32 v221, v221, v183\n"
"v_mfma_f32_16x16x32_bf16 a[20:23], v[128:131], v[188:191], a[20:23]\n"
"v_exp_f32 v161, v161\n"
"v_mfma_f32_16x16x32_bf16 a[44:47], v[132:135], v[188:191], a[44:47]\n"
"ds_read_b64_tr_b16 a[254:255], v233 offset:1024\n"
"v_add_f32 v216, v216, v160\n"
"v_mfma_f32_16x16x32_bf16 a[68:71], v[136:139], v[188:191], a[68:71]\n"
"v_exp_f32 v162, v162\n"
"v_mfma_f32_16x16x32_bf16 a[92:95], v[140:143], v[188:191], a[92:95]\n"
"v_add_f32 v216, v216, v161\n"
"v_cvt_pk_bf16_f32 v184, v160, v161\n"
"v_mfma_f32_16x16x32_bf16 a[116:119], v[144:147], v[188:191], a[116:119]\n"
"v_exp_f32 v163, v163\n"
"v_mfma_f32_16x16x32_bf16 a[140:143], v[148:151], v[188:191], a[140:143]\n"
"v_add_f32 v216, v216, v162\n"
"v_mfma_f32_16x16x32_bf16 a[164:167], v[152:155], v[188:191], a[164:167]\n"
"v_exp_f32 v164, v164\n"
"v_mfma_f32_16x16x32_bf16 a[188:191], v[156:159], v[188:191], a[188:191]\n"
"v_add_f32 v216, v216, v163\n"
"s_waitcnt lgkmcnt(0)\n"
"v_mfma_f32_16x16x32_bf16 v[168:171], a[192:195], v[16:19], v[196:199]\n"
"v_exp_f32 v165, v165\n"
"v_mfma_f32_16x16x32_bf16 v[168:171], a[196:199], v[20:23], v[168:171]\n"
"v_add_f32 v216, v216, v164\n"
"v_cvt_pk_bf16_f32 v185, v162, v163\n"
"v_mfma_f32_16x16x32_bf16 v[168:171], a[200:203], v[24:27], v[168:171]\n"
"v_exp_f32 v166, v166\n"
"v_mfma_f32_16x16x32_bf16 v[168:171], a[204:207], v[28:31], v[168:171]\n"
"v_add_f32 v216, v216, v165\n"
"v_cvt_pk_bf16_f32 v186, v164, v165\n"
"v_mfma_f32_16x16x32_bf16 v[172:175], a[208:211], v[16:19], v[196:199]\n"
"v_exp_f32 v167, v167\n"
"v_mfma_f32_16x16x32_bf16 v[172:175], a[212:215], v[20:23], v[172:175]\n"
"v_cvt_pk_bf16_f32 v187, v166, v167\n"
"v_add_f32 v216, v216, v166\n"
"v_mfma_f32_16x16x32_bf16 v[172:175], a[216:219], v[24:27], v[172:175]\n"
"v_exp_f32 v168, v168\n"
"v_mfma_f32_16x16x32_bf16 v[172:175], a[220:223], v[28:31], v[172:175]\n"
"ds_read_b128 v[96:99], v222 offset:16384\n"
"v_add_f32 v216, v216, v167\n"
"v_mfma_f32_16x16x32_bf16 a[0:3], a[224:227], v[184:187], a[0:3]\n"
"v_exp_f32 v169, v169\n"
"v_mfma_f32_16x16x32_bf16 a[24:27], a[228:231], v[184:187], a[24:27]\n"
"ds_read_b128 v[100:103], v223 offset:16384\n"
"v_add_f32 v217, v217, v168\n"
"v_mfma_f32_16x16x32_bf16 a[48:51], a[232:235], v[184:187], a[48:51]\n"
"v_exp_f32 v170, v170\n"
"v_mfma_f32_16x16x32_bf16 a[72:75], a[236:239], v[184:187], a[72:75]\n"
"v_add_f32 v217, v217, v169\n"
"v_cvt_pk_bf16_f32 v188, v168, v169\n"
"v_mfma_f32_16x16x32_bf16 a[96:99], a[240:243], v[184:187], a[96:99]\n"
"v_exp_f32 v171, v171\n"
"v_mfma_f32_16x16x32_bf16 a[120:123], a[244:247], v[184:187], a[120:123]\n"
"ds_read_b64_tr_b16 v[128:129], v226 offset:16384\n"
"v_add_f32 v217, v217, v170\n"
"v_mfma_f32_16x16x32_bf16 a[144:147], a[248:251], v[184:187], a[144:147]\n"
"v_exp_f32 v172, v172\n"
"v_mfma_f32_16x16x32_bf16 a[168:171], a[252:255], v[184:187], a[168:171]\n"
"ds_read_b64_tr_b16 v[130:131], v226 offset:17408\n"
"v_add_f32 v217, v217, v171\n"
"v_mfma_f32_16x16x32_bf16 v[176:179], a[192:195], v[32:35], v[200:203]\n"
"v_exp_f32 v173, v173\n"
"v_mfma_f32_16x16x32_bf16 v[176:179], a[196:199], v[36:39], v[176:179]\n"
"v_add_f32 v217, v217, v172\n"
"v_cvt_pk_bf16_f32 v189, v170, v171\n"
"v_mfma_f32_16x16x32_bf16 v[176:179], a[200:203], v[40:43], v[176:179]\n"
"v_exp_f32 v174, v174\n"
"v_mfma_f32_16x16x32_bf16 v[176:179], a[204:207], v[44:47], v[176:179]\n"
"v_add_f32 v217, v217, v173\n"
"v_cvt_pk_bf16_f32 v190, v172, v173\n"
"v_mfma_f32_16x16x32_bf16 v[180:183], a[208:211], v[32:35], v[200:203]\n"
"v_exp_f32 v175, v175\n"
"v_mfma_f32_16x16x32_bf16 v[180:183], a[212:215], v[36:39], v[180:183]\n"
"v_cvt_pk_bf16_f32 v191, v174, v175\n"
"v_add_f32 v217, v217, v174\n"
"v_mfma_f32_16x16x32_bf16 v[180:183], a[216:219], v[40:43], v[180:183]\n"
"v_exp_f32 v176, v176\n"
"v_mfma_f32_16x16x32_bf16 v[180:183], a[220:223], v[44:47], v[180:183]\n"
"ds_read_b128 v[104:107], v224 offset:16384\n"
"v_add_f32 v217, v217, v175\n"
"v_mfma_f32_16x16x32_bf16 a[4:7], a[224:227], v[188:191], a[4:7]\n"
"v_exp_f32 v177, v177\n"
"v_mfma_f32_16x16x32_bf16 a[28:31], a[228:231], v[188:191], a[28:31]\n"
"ds_read_b128 v[108:111], v225 offset:16384\n"
"v_add_f32 v218, v218, v176\n"
"v_mfma_f32_16x16x32_bf16 a[52:55], a[232:235], v[188:191], a[52:55]\n"
"v_exp_f32 v178, v178\n"
"v_mfma_f32_16x16x32_bf16 a[76:79], a[236:239], v[188:191], a[76:79]\n"
"v_add_f32 v218, v218, v177\n"
"v_cvt_pk_bf16_f32 v184, v176, v177\n"
"v_mfma_f32_16x16x32_bf16 a[100:103], a[240:243], v[188:191], a[100:103]\n"
"v_exp_f32 v179, v179\n"
"v_mfma_f32_16x16x32_bf16 a[124:127], a[244:247], v[188:191], a[124:127]\n"
"ds_read_b64_tr_b16 v[132:133], v227 offset:16384\n"
"v_add_f32 v218, v218, v178\n"
"v_mfma_f32_16x16x32_bf16 a[148:151], a[248:251], v[188:191], a[148:151]\n"
"v_exp_f32 v180, v180\n"
"v_mfma_f32_16x16x32_bf16 a[172:175], a[252:255], v[188:191], a[172:175]\n"
"ds_read_b64_tr_b16 v[134:135], v227 offset:17408\n"
"v_add_f32 v218, v218, v179\n"
"v_mfma_f32_16x16x32_bf16 v[160:163], a[192:195], v[48:51], v[204:207]\n"
"v_exp_f32 v181, v181\n"
"v_mfma_f32_16x16x32_bf16 v[160:163], a[196:199], v[52:55], v[160:163]\n"
"v_add_f32 v218, v218, v180\n"
"v_cvt_pk_bf16_f32 v185, v178, v179\n"
"v_mfma_f32_16x16x32_bf16 v[160:163], a[200:203], v[56:59], v[160:163]\n"
"v_exp_f32 v182, v182\n"
"v_mfma_f32_16x16x32_bf16 v[160:163], a[204:207], v[60:63], v[160:163]\n"
"v_add_f32 v218, v218, v181\n"
"v_cvt_pk_bf16_f32 v186, v180, v181\n"
"v_mfma_f32_16x16x32_bf16 v[164:167], a[208:211], v[48:51], v[204:207]\n"
"v_exp_f32 v183, v183\n"
"v_mfma_f32_16x16x32_bf16 v[164:167], a[212:215], v[52:55], v[164:167]\n"
"v_cvt_pk_bf16_f32 v187, v182, v183\n"
"v_add_f32 v218, v218, v182\n"
"v_mfma_f32_16x16x32_bf16 v[164:167], a[216:219], v[56:59], v[164:167]\n"
"v_exp_f32 v160, v160\n"
"v_mfma_f32_16x16x32_bf16 v[164:167], a[220:223], v[60:63], v[164:167]\n"
"ds_read_b128 v[112:115], v222 offset:17408\n"
"v_add_f32 v218, v218, v183\n"
"v_mfma_f32_16x16x32_bf16 a[8:11], a[224:227], v[184:187], a[8:11]\n"
"v_exp_f32 v161, v161\n"
"v_mfma_f32_16x16x32_bf16 a[32:35], a[228:231], v[184:187], a[32:35]\n"
"ds_read_b128 v[116:119], v223 offset:17408\n"
"v_add_f32 v219, v219, v160\n"
"v_mfma_f32_16x16x32_bf16 a[56:59], a[232:235], v[184:187], a[56:59]\n"
"v_exp_f32 v162, v162\n"
"v_mfma_f32_16x16x32_bf16 a[80:83], a[236:239], v[184:187], a[80:83]\n"
"v_add_f32 v219, v219, v161\n"
"v_cvt_pk_bf16_f32 v188, v160, v161\n"
"v_mfma_f32_16x16x32_bf16 a[104:107], a[240:243], v[184:187], a[104:107]\n"
"v_exp_f32 v163, v163\n"
"v_mfma_f32_16x16x32_bf16 a[128:131], a[244:247], v[184:187], a[128:131]\n"
"ds_read_b64_tr_b16 v[136:137], v228 offset:16384\n"
"v_add_f32 v219, v219, v162\n"
"v_mfma_f32_16x16x32_bf16 a[152:155], a[248:251], v[184:187], a[152:155]\n"
"v_exp_f32 v164, v164\n"
"v_mfma_f32_16x16x32_bf16 a[176:179], a[252:255], v[184:187], a[176:179]\n"
"ds_read_b64_tr_b16 v[138:139], v228 offset:17408\n"
"v_add_f32 v219, v219, v163\n"
"v_mfma_f32_16x16x32_bf16 v[168:171], a[192:195], v[64:67], v[208:211]\n"
"v_exp_f32 v165, v165\n"
"v_mfma_f32_16x16x32_bf16 v[168:171], a[196:199], v[68:71], v[168:171]\n"
"v_add_f32 v219, v219, v164\n"
"v_cvt_pk_bf16_f32 v189, v162, v163\n"
"v_mfma_f32_16x16x32_bf16 v[168:171], a[200:203], v[72:75], v[168:171]\n"
"v_exp_f32 v166, v166\n"
"v_mfma_f32_16x16x32_bf16 v[168:171], a[204:207], v[76:79], v[168:171]\n"
"v_add_f32 v219, v219, v165\n"
"v_cvt_pk_bf16_f32 v190, v164, v165\n"
"v_mfma_f32_16x16x32_bf16 v[172:175], a[208:211], v[64:67], v[208:211]\n"
"v_exp_f32 v167, v167\n"
"v_mfma_f32_16x16x32_bf16 v[172:175], a[212:215], v[68:71], v[172:175]\n"
"v_cvt_pk_bf16_f32 v191, v166, v167\n"
"v_add_f32 v219, v219, v166\n"
"v_mfma_f32_16x16x32_bf16 v[172:175], a[216:219], v[72:75], v[172:175]\n"
"v_exp_f32 v168, v168\n"
"v_mfma_f32_16x16x32_bf16 v[172:175], a[220:223], v[76:79], v[172:175]\n"
"ds_read_b128 v[120:123], v224 offset:17408\n"
"v_add_f32 v219, v219, v167\n"
"v_mfma_f32_16x16x32_bf16 a[12:15], a[224:227], v[188:191], a[12:15]\n"
"v_exp_f32 v169, v169\n"
"v_mfma_f32_16x16x32_bf16 a[36:39], a[228:231], v[188:191], a[36:39]\n"
"ds_read_b128 v[124:127], v225 offset:17408\n"
"v_add_f32 v220, v220, v168\n"
"v_mfma_f32_16x16x32_bf16 a[60:63], a[232:235], v[188:191], a[60:63]\n"
"v_exp_f32 v170, v170\n"
"v_mfma_f32_16x16x32_bf16 a[84:87], a[236:239], v[188:191], a[84:87]\n"
"v_add_f32 v220, v220, v169\n"
"v_cvt_pk_bf16_f32 v184, v168, v169\n"
"v_mfma_f32_16x16x32_bf16 a[108:111], a[240:243], v[188:191], a[108:111]\n"
"v_exp_f32 v171, v171\n"
"v_mfma_f32_16x16x32_bf16 a[132:135], a[244:247], v[188:191], a[132:135]\n"
"ds_read_b64_tr_b16 v[140:141], v229 offset:16384\n"
"v_add_f32 v220, v220, v170\n"
"v_mfma_f32_16x16x32_bf16 a[156:159], a[248:251], v[188:191], a[156:159]\n"
"v_exp_f32 v172, v172\n"
"v_mfma_f32_16x16x32_bf16 a[180:183], a[252:255], v[188:191], a[180:183]\n"
"ds_read_b64_tr_b16 v[142:143], v229 offset:17408\n"
"v_add_f32 v220, v220, v171\n"
"v_mfma_f32_16x16x32_bf16 v[176:179], a[192:195], v[80:83], v[212:215]\n"
"v_exp_f32 v173, v173\n"
"v_mfma_f32_16x16x32_bf16 v[176:179], a[196:199], v[84:87], v[176:179]\n"
"v_add_f32 v220, v220, v172\n"
"v_cvt_pk_bf16_f32 v185, v170, v171\n"
"v_mfma_f32_16x16x32_bf16 v[176:179], a[200:203], v[88:91], v[176:179]\n"
"v_exp_f32 v174, v174\n"
"v_mfma_f32_16x16x32_bf16 v[176:179], a[204:207], v[92:95], v[176:179]\n"
"v_add_f32 v220, v220, v173\n"
"v_cvt_pk_bf16_f32 v186, v172, v173\n"
"v_mfma_f32_16x16x32_bf16 v[180:183], a[208:211], v[80:83], v[212:215]\n"
"v_exp_f32 v175, v175\n"
"v_mfma_f32_16x16x32_bf16 v[180:183], a[212:215], v[84:87], v[180:183]\n"
"v_cvt_pk_bf16_f32 v187, v174, v175\n"
"v_add_f32 v220, v220, v174\n"
"v_mfma_f32_16x16x32_bf16 v[180:183], a[216:219], v[88:91], v[180:183]\n"
"v_exp_f32 v176, v176\n"
"v_mfma_f32_16x16x32_bf16 v[180:183], a[220:223], v[92:95], v[180:183]\n"
"ds_read_b64_tr_b16 v[144:145], v230 offset:16384\n"
"v_add_f32 v220, v220, v175\n"
"v_mfma_f32_16x16x32_bf16 a[16:19], a[224:227], v[184:187], a[16:19]\n"
"v_exp_f32 v177, v177\n"
"v_mfma_f32_16x16x32_bf16 a[40:43], a[228:231], v[184:187], a[40:43]\n"
"ds_read_b64_tr_b16 v[146:147], v230 offset:17408\n"
"v_add_f32 v221, v221, v176\n"
"v_mfma_f32_16x16x32_bf16 a[64:67], a[232:235], v[184:187], a[64:67]\n"
"v_exp_f32 v178, v178\n"
"v_mfma_f32_16x16x32_bf16 a[88:91], a[236:239], v[184:187], a[88:91]\n"
"v_add_f32 v221, v221, v177\n"
"v_cvt_pk_bf16_f32 v188, v176, v177\n"
"v_mfma_f32_16x16x32_bf16 a[112:115], a[240:243], v[184:187], a[112:115]\n"
"v_exp_f32 v179, v179\n"
"v_mfma_f32_16x16x32_bf16 a[136:139], a[244:247], v[184:187], a[136:139]\n"
"ds_read_b64_tr_b16 v[148:149], v231 offset:16384\n"
"v_add_f32 v221, v221, v178\n"
"v_mfma_f32_16x16x32_bf16 a[160:163], a[248:251], v[184:187], a[160:163]\n"
"v_exp_f32 v180, v180\n"
"v_mfma_f32_16x16x32_bf16 a[184:187], a[252:255], v[184:187], a[184:187]\n"
"ds_read_b64_tr_b16 v[150:151], v231 offset:17408\n"
"v_add_f32 v221, v221, v179\n"
"s_waitcnt lgkmcnt(8)\n"
"v_mfma_f32_16x16x32_bf16 v[160:163], v[96:99], v[0:3], v[192:195]\n"
"v_exp_f32 v181, v181\n"
"v_mfma_f32_16x16x32_bf16 v[160:163], v[100:103], v[4:7], v[160:163]\n"
"v_add_f32 v221, v221, v180\n"
"v_cvt_pk_bf16_f32 v189, v178, v179\n"
"v_mfma_f32_16x16x32_bf16 v[160:163], v[104:107], v[8:11], v[160:163]\n"
"v_exp_f32 v182, v182\n"
"v_mfma_f32_16x16x32_bf16 v[160:163], v[108:111], v[12:15], v[160:163]\n"
"ds_read_b64_tr_b16 v[152:153], v232 offset:16384\n"
"v_add_f32 v221, v221, v181\n"
"v_cvt_pk_bf16_f32 v190, v180, v181\n"
"v_mfma_f32_16x16x32_bf16 v[164:167], v[112:115], v[0:3], v[192:195]\n"
"v_exp_f32 v183, v183\n"
"v_mfma_f32_16x16x32_bf16 v[164:167], v[116:119], v[4:7], v[164:167]\n"
"ds_read_b64_tr_b16 v[154:155], v232 offset:17408\n"
"v_cvt_pk_bf16_f32 v191, v182, v183\n"
"v_add_f32 v221, v221, v182\n"
"s_waitcnt lgkmcnt(9)\n"
"v_mfma_f32_16x16x32_bf16 v[164:167], v[120:123], v[8:11], v[164:167]\n"
"v_exp_f32 v160, v160\n"
"s_waitcnt lgkmcnt(8)\n"
"v_mfma_f32_16x16x32_bf16 v[164:167], v[124:127], v[12:15], v[164:167]\n"
"ds_read_b64_tr_b16 v[156:157], v233 offset:16384\n"
"v_add_f32 v221, v221, v183\n"
"v_mfma_f32_16x16x32_bf16 a[20:23], a[224:227], v[188:191], a[20:23]\n"
"v_exp_f32 v161, v161\n"
"v_mfma_f32_16x16x32_bf16 a[44:47], a[228:231], v[188:191], a[44:47]\n"
"ds_read_b64_tr_b16 v[158:159], v233 offset:17408\n"
"v_add_f32 v216, v216, v160\n"
"v_mfma_f32_16x16x32_bf16 a[68:71], a[232:235], v[188:191], a[68:71]\n"
"v_exp_f32 v162, v162\n"
"v_mfma_f32_16x16x32_bf16 a[92:95], a[236:239], v[188:191], a[92:95]\n"
"s_sub_u32 s65, s65, 1\n"
"v_add_f32 v216, v216, v161\n"
"v_cvt_pk_bf16_f32 v184, v160, v161\n"
"v_mfma_f32_16x16x32_bf16 a[116:119], a[240:243], v[188:191], a[116:119]\n"
"s_cmp_lg_u32 s65, 0\n"
"v_exp_f32 v163, v163\n"
"v_mfma_f32_16x16x32_bf16 a[140:143], a[244:247], v[188:191], a[140:143]\n"
"v_add_f32 v216, v216, v162\n"
"v_mfma_f32_16x16x32_bf16 a[164:167], a[248:251], v[188:191], a[164:167]\n"
"v_exp_f32 v164, v164\n"
"v_mfma_f32_16x16x32_bf16 a[188:191], a[252:255], v[188:191], a[188:191]\n"
"v_add_f32 v216, v216, v163\n"
"s_cbranch_scc1 .Lf_at\n"
"s_cmp_lg_u32 s66, 0\n"
"s_cbranch_scc1 .Lf_last\n"
"s_waitcnt vmcnt(0) lgkmcnt(0)\n"
"s_barrier\n"
"v_mfma_f32_16x16x32_bf16 v[168:171], v[96:99], v[16:19], v[196:199]\n"
"s_add_u32 s36, s36, 0x20000\n"
"s_addc_u32 s37, s37, 0\n"
"v_exp_f32 v165, v165\n"
"v_mfma_f32_16x16x32_bf16 v[168:171], v[100:103], v[20:23], v[168:171]\n"
"s_add_u32 s40, s40, 0x20000\n"
"s_addc_u32 s41, s41, 0\n"
"v_add_f32 v216, v216, v164\n"
"v_cvt_pk_bf16_f32 v185, v162, v163\n"
"v_mfma_f32_16x16x32_bf16 v[168:171], v[104:107], v[24:27], v[168:171]\n"
"s_add_u32 m0, s57, 0\n"
"v_exp_f32 v166, v166\n"
"v_mfma_f32_16x16x32_bf16 v[168:171], v[108:111], v[28:31], v[168:171]\n"
"buffer_load_dwordx4 v234, s[36:39], s56 offen offset:0 nt lds\n"
"v_add_f32 v216, v216, v165\n"
"v_cvt_pk_bf16_f32 v186, v164, v165\n"
"v_mfma_f32_16x16x32_bf16 v[172:175], v[112:115], v[16:19], v[196:199]\n"
"v_exp_f32 v167, v167\n"
"v_mfma_f32_16x16x32_bf16 v[172:175], v[116:119], v[20:23], v[172:175]\n"
"v_cvt_pk_bf16_f32 v187, v166, v167\n"
"v_add_f32 v216, v216, v166\n"
"v_mfma_f32_16x16x32_bf16 v[172:175], v[120:123], v[24:27], v[172:175]\n"
"v_exp_f32 v168, v168\n"
"v_mfma_f32_16x16x32_bf16 v[172:175], v[124:127], v[28:31], v[172:175]\n"
"ds_read_b128 a[192:195], v222 offset:32768\n"
"buffer_load_dwordx4 v234, s[36:39], s56 offen offset:1024 nt lds\n"
"v_add_f32 v216, v216, v167\n"
"v_mfma_f32_16x16x32_bf16 a[0:3], v[128:131], v[184:187], a[0:3]\n"
"v_exp_f32 v169, v169\n"
"v_mfma_f32_16x16x32_bf16 a[24:27], v[132:135], v[184:187], a[24:27]\n"
"ds_read_b128 a[196:199], v223 offset:32768\n"
"v_add_f32 v217, v217, v168\n"
"v_mfma_f32_16x16x32_bf16 a[48:51], v[136:139], v[184:187], a[48:51]\n"
"v_exp_f32 v170, v170\n"
"v_mfma_f32_16x16x32_bf16 a[72:75], v[140:143], v[184:187], a[72:75]\n"
"buffer_load_dwordx4 v235, s[36:39], s56 offen offset:2048 nt lds\n"
"v_add_f32 v217, v217, v169\n"
"v_cvt_pk_bf16_f32 v188, v168, v169\n"
"v_mfma_f32_16x16x32_bf16 a[96:99], v[144:147], v[184:187], a[96:99]\n"
"v_exp_f32 v171, v171\n"
"v_mfma_f32_16x16x32_bf16 a[120:123], v[148:151], v[184:187], a[120:123]\n"
"ds_read_b64_tr_b16 a[224:225], v226 offset:32768\n"
"v_add_f32 v217, v217, v170\n"
"v_mfma_f32_16x16x32_bf16 a[144:147], v[152:155], v[184:187], a[144:147]\n"
"v_exp_f32 v172, v172\n"
"v_mfma_f32_16x16x32_bf16 a[168:171], v[156:159], v[184:187], a[168:171]\n"
"ds_read_b64_tr_b16 a[226:227], v226 offset:33792\n"
"buffer_load_dwordx4 v235, s[36:39], s56 offen offset:3072 nt lds\n"
"v_add_f32 v217, v217, v171\n"
"v_mfma_f32_16x16x32_bf16 v[176:179], v[96:99], v[32:35], v[200:203]\n"
"v_exp_f32 v173, v173\n"
"v_mfma_f32_16x16x32_bf16 v[176:179], v[100:103], v[36:39], v[176:179]\n"
"s_add_u32 s56, s56, 8192\n"
"s_and_b32 s56, s56, 0x1ffff\n"
"v_add_f32 v217, v217, v172\n"
"v_cvt_pk_bf16_f32 v189, v170, v171\n"
"v_mfma_f32_16x16x32_bf16 v[176:179], v[104:107], v[40:43], v[176:179]\n"
"s_add_u32 m0, s57, 16384\n"
"v_exp_f32 v174, v174\n"
"v_mfma_f32_16x16x32_bf16 v[176:179], v[108:111], v[44:47], v[176:179]\n"
"buffer_load_dwordx4 v234, s[36:39], s56 offen offset:0 nt lds\n"
"v_add_f32 v217, v217, v173\n"
"v_cvt_pk_bf16_f32 v190, v172, v173\n"
"v_mfma_f32_16x16x32_bf16 v[180:183], v[112:115], v[32:35], v[200:203]\n"
"v_exp_f32 v175, v175\n"
"v_mfma_f32_16x16x32_bf16 v[180:183], v[116:119], v[36:39], v[180:183]\n"
"v_cvt_pk_bf16_f32 v191, v174, v175\n"
"v_add_f32 v217, v217, v174\n"
"v_mfma_f32_16x16x32_bf16 v[180:183], v[120:123], v[40:43], v[180:183]\n"
"v_exp_f32 v176, v176\n"
"v_mfma_f32_16x16x32_bf16 v[180:183], v[124:127], v[44:47], v[180:183]\n"
"ds_read_b128 a[200:203], v224 offset:32768\n"
"buffer_load_dwordx4 v234, s[36:39], s56 offen offset:1024 nt lds\n"
"v_add_f32 v217, v217, v175\n"
"v_mfma_f32_16x16x32_bf16 a[4:7], v[128:131], v[188:191], a[4:7]\n"
"v_exp_f32 v177, v177\n"
"v_mfma_f32_16x16x32_bf16 a[28:31], v[132:135], v[188:191], a[28:31]\n"
"ds_read_b128 a[204:207], v225 offset:32768\n"
"v_add_f32 v218, v218, v176\n"
"v_mfma_f32_16x16x32_bf16 a[52:55], v[136:139], v[188:191], a[52:55]\n"
"v_exp_f32 v178, v178\n"
"v_mfma_f32_16x16x32_bf16 a[76:79], v[140:143], v[188:191], a[76:79]\n"
"buffer_load_dwordx4 v235, s[36:39], s56 offen offset:2048 nt lds\n"
"v_add_f32 v218, v218, v177\n"
"v_cvt_pk_bf16_f32 v184, v176, v177\n"
"v_mfma_f32_16x16x32_bf16 a[100:103], v[144:147], v[188:191], a[100:103]\n"
"v_exp_f32 v179, v179\n"
"v_mfma_f32_16x16x32_bf16 a[124:127], v[148:151], v[188:191], a[124:127]\n"
"ds_read_b64_tr_b16 a[228:229], v227 offset:32768\n"
"v_add_f32 v218, v218, v178\n"
"v_mfma_f32_16x16x32_bf16 a[148:151], v[152:155], v[188:191], a[148:151]\n"
"v_exp_f32 v180, v180\n"
"v_mfma_f32_16x16x32_bf16 a[172:175], v[156:159], v[188:191], a[172:175]\n"
"ds_read_b64_tr_b16 a[230:231], v227 offset:33792\n"
"buffer_load_dwordx4 v235, s[36:39], s56 offen offset:3072 nt lds\n"
"v_add_f32 v218, v218, v179\n"
"v_mfma_f32_16x16x32_bf16 v[160:163], v[96:99], v[48:51], v[204:207]\n"
"v_exp_f32 v181, v181\n"
"v_mfma_f32_16x16x32_bf16 v[160:163], v[100:103], v[52:55], v[160:163]\n"
"s_add_u32 s56, s56, 0x1e000\n"
"s_and_b32 s56, s56, 0x1ffff\n"
"v_add_f32 v218, v218, v180\n"
"v_cvt_pk_bf16_f32 v185, v178, v179\n"
"v_mfma_f32_16x16x32_bf16 v[160:163], v[104:107], v[56:59], v[160:163]\n"
"s_add_u32 m0, s58, 0\n"
"v_exp_f32 v182, v182\n"
"v_mfma_f32_16x16x32_bf16 v[160:163], v[108:111], v[60:63], v[160:163]\n"
"buffer_load_dwordx4 v236, s[40:43], s56 offen offset:0 nt lds\n"
"v_add_f32 v218, v218, v181\n"
"v_cvt_pk_bf16_f32 v186, v180, v181\n"
"v_mfma_f32_16x16x32_bf16 v[164:167], v[112:115], v[48:51], v[204:207]\n"
"v_exp_f32 v183, v183\n"
"v_mfma_f32_16x16x32_bf16 v[164:167], v[116:119], v[52:55], v[164:167]\n"
"v_cvt_pk_bf16_f32 v187, v182, v183\n"
"v_add_f32 v218, v218, v182\n"
"v_mfma_f32_16x16x32_bf16 v[164:167], v[120:123], v[56:59], v[164:167]\n"
"v_exp_f32 v160, v160\n"
"v_mfma_f32_16x16x32_bf16 v[164:167], v[124:127], v[60:63], v[164:167]\n"
"ds_read_b128 a[208:211], v222 offset:33792\n"
"buffer_load_dwordx4 v236, s[40:43], s56 offen offset:1024 nt lds\n"
"v_add_f32 v218, v218, v183\n"
"v_mfma_f32_16x16x32_bf16 a[8:11], v[128:131], v[184:187], a[8:11]\n"
"v_exp_f32 v161, v161\n"
"v_mfma_f32_16x16x32_bf16 a[32:35], v[132:135], v[184:187], a[32:35]\n"
"ds_read_b128 a[212:215], v223 offset:33792\n"
"v_add_f32 v219, v219, v160\n"
"v_mfma_f32_16x16x32_bf16 a[56:59], v[136:139], v[184:187], a[56:59]\n"
"v_exp_f32 v162, v162\n"
"v_mfma_f32_16x16x32_bf16 a[80:83], v[140:143], v[184:187], a[80:83]\n"
"buffer_load_dwordx4 v237, s[40:43], s56 offen offset:2048 nt lds\n"
"v_add_f32 v219, v219, v161\n"
"v_cvt_pk_bf16_f32 v188, v160, v161\n"
"v_mfma_f32_16x16x32_bf16 a[104:107], v[144:147], v[184:187], a[104:107]\n"
"v_exp_f32 v163, v163\n"
"v_mfma_f32_16x16x32_bf16 a[128:131], v[148:151], v[184:187], a[128:131]\n"
"ds_read_b64_tr_b16 a[232:233], v228 offset:32768\n"
"v_add_f32 v219, v219, v162\n"
"v_mfma_f32_16x16x32_bf16 a[152:155], v[152:155], v[184:187], a[152:155]\n"
"v_exp_f32 v164, v164\n"
"v_mfma_f32_16x16x32_bf16 a[176:179], v[156:159], v[184:187], a[176:179]\n"
"ds_read_b64_tr_b16 a[234:235], v228 offset:33792\n"
"buffer_load_dwordx4 v237, s[40:43], s56 offen offset:3072 nt lds\n"
"v_add_f32 v219, v219, v163\n"
"v_mfma_f32_16x16x32_bf16 v[168:171], v[96:99], v[64:67], v[208:211]\n"
"s_add_u32 s56, s56, 8192\n"
"s_and_b32 s56, s56, 0x1ffff\n"
"v_exp_f32 v165, v165\n"
"v_mfma_f32_16x16x32_bf16 v[168:171], v[100:103], v[68:71], v[168:171]\n"
"s_add_u32 s72, s72, 512\n"
"s_mov_b32 s61, s72\n"
"v_add_f32 v219, v219, v164\n"
"v_cvt_pk_bf16_f32 v189, v162, v163\n"
"v_mfma_f32_16x16x32_bf16 v[168:171], v[104:107], v[72:75], v[168:171]\n"
"s_add_u32 s62, s72, 65536\n"
"s_mov_b32 s59, s71\n"
"v_exp_f32 v166, v166\n"
"v_mfma_f32_16x16x32_bf16 v[168:171], v[108:111], v[76:79], v[168:171]\n"
"s_add_u32 s60, s71, 36864\n"
"v_add_f32 v219, v219, v165\n"
"v_cvt_pk_bf16_f32 v190, v164, v165\n"
"v_mfma_f32_16x16x32_bf16 v[172:175], v[112:115], v[64:67], v[208:211]\n"
"v_exp_f32 v167, v167\n"
"v_mfma_f32_16x16x32_bf16 v[172:175], v[116:119], v[68:71], v[172:175]\n"
"v_cvt_pk_bf16_f32 v191, v166, v167\n"
"v_add_f32 v219, v219, v166\n"
"v_mfma_f32_16x16x32_bf16 v[172:175], v[120:123], v[72:75], v[172:175]\n"
"v_exp_f32 v168, v168\n"
"v_mfma_f32_16x16x32_bf16 v[172:175], v[124:127], v[76:79], v[172:175]\n"
"ds_read_b128 a[216:219], v224 offset:33792\n"
"v_add_f32 v219, v219, v167\n"
"v_mfma_f32_16x16x32_bf16 a[12:15], v[128:131], v[188:191], a[12:15]\n"
"v_exp_f32 v169, v169\n"
"v_mfma_f32_16x16x32_bf16 a[36:39], v[132:135], v[188:191], a[36:39]\n"
"ds_read_b128 a[220:223], v225 offset:33792\n"
"v_add_f32 v220, v220, v168\n"
"v_mfma_f32_16x16x32_bf16 a[60:63], v[136:139], v[188:191], a[60:63]\n"
"v_exp_f32 v170, v170\n"
"v_mfma_f32_16x16x32_bf16 a[84:87], v[140:143], v[188:191], a[84:87]\n"
"v_add_f32 v220, v220, v169\n"
"v_cvt_pk_bf16_f32 v184, v168, v169\n"
"v_mfma_f32_16x16x32_bf16 a[108:111], v[144:147], v[188:191], a[108:111]\n"
"v_exp_f32 v171, v171\n"
"v_mfma_f32_16x16x32_bf16 a[132:135], v[148:151], v[188:191], a[132:135]\n"
"ds_read_b64_tr_b16 a[236:237], v229 offset:32768\n"
"v_add_f32 v220, v220, v170\n"
"v_mfma_f32_16x16x32_bf16 a[156:159], v[152:155], v[188:191], a[156:159]\n"
"v_exp_f32 v172, v172\n"
"v_mfma_f32_16x16x32_bf16 a[180:183], v[156:159], v[188:191], a[180:183]\n"
"ds_read_b64_tr_b16 a[238:239], v229 offset:33792\n"
"v_add_f32 v220, v220, v171\n"
"v_mfma_f32_16x16x32_bf16 v[176:179], v[96:99], v[80:83], v[212:215]\n"
"v_exp_f32 v173, v173\n"
"v_mfma_f32_16x16x32_bf16 v[176:179], v[100:103], v[84:87], v[176:179]\n"
"v_add_f32 v220, v220, v172\n"
"v_cvt_pk_bf16_f32 v185, v170, v171\n"
"v_mfma_f32_16x16x32_bf16 v[176:179], v[104:107], v[88:91], v[176:179]\n"
"v_exp_f32 v174, v174\n"
"v_mfma_f32_16x16x32_bf16 v[176:179], v[108:111], v[92:95], v[176:179]\n"
"v_add_f32 v220, v220, v173\n"
"v_cvt_pk_bf16_f32 v186, v172, v173\n"
"v_mfma_f32_16x16x32_bf16 v[180:183], v[112:115], v[80:83], v[212:215]\n"
"v_exp_f32 v175, v175\n"
"v_mfma_f32_16x16x32_bf16 v[180:183], v[116:119], v[84:87], v[180:183]\n"
"v_cvt_pk_bf16_f32 v187, v174, v175\n"
"v_add_f32 v220, v220, v174\n"
"v_mfma_f32_16x16x32_bf16 v[180:183], v[120:123], v[88:91], v[180:183]\n"
"v_exp_f32 v176, v176\n"
"v_mfma_f32_16x16x32_bf16 v[180:183], v[124:127], v[92:95], v[180:183]\n"
"ds_read_b64_tr_b16 a[240:241], v230 offset:32768\n"
"v_add_f32 v220, v220, v175\n"
"v_mfma_f32_16x16x32_bf16 a[16:19], v[128:131], v[184:187], a[16:19]\n"
"v_exp_f32 v177, v177\n"
"v_mfma_f32_16x16x32_bf16 a[40:43], v[132:135], v[184:187], a[40:43]\n"
"ds_read_b64_tr_b16 a[242:243], v230 offset:33792\n"
"v_add_f32 v221, v221, v176\n"
"v_mfma_f32_16x16x32_bf16 a[64:67], v[136:139], v[184:187], a[64:67]\n"
"v_exp_f32 v178, v178\n"
"v_mfma_f32_16x16x32_bf16 a[88:91], v[140:143], v[184:187], a[88:91]\n"
"v_add_f32 v221, v221, v177\n"
"v_cvt_pk_bf16_f32 v188, v176, v177\n"
"v_mfma_f32_16x16x32_bf16 a[112:115], v[144:147], v[184:187], a[112:115]\n"
"v_exp_f32 v179, v179\n"
"v_mfma_f32_16x16x32_bf16 a[136:139], v[148:151], v[184:187], a[136:139]\n"
"ds_read_b64_tr_b16 a[244:245], v231 offset:32768\n"
"v_add_f32 v221, v221, v178\n"
"v_mfma_f32_16x16x32_bf16 a[160:163], v[152:155], v[184:187], a[160:163]\n"
"v_exp_f32 v180, v180\n"
"v_mfma_f32_16x16x32_bf16 a[184:187], v[156:159], v[184:187], a[184:187]\n"
"ds_read_b64_tr_b16 a[246:247], v231 offset:33792\n"
"v_add_f32 v221, v221, v179\n"
"s_waitcnt lgkmcnt(8)\n"
"v_mfma_f32_16x16x32_bf16 v[160:163], a[192:195], v[0:3], v[192:195]\n"
"v_exp_f32 v181, v181\n"
"v_mfma_f32_16x16x32_bf16 v[160:163], a[196:199], v[4:7], v[160:163]\n"
"v_add_f32 v221, v221, v180\n"
"v_cvt_pk_bf16_f32 v189, v178, v179\n"
"v_mfma_f32_16x16x32_bf16 v[160:163], a[200:203], v[8:11], v[160:163]\n"
"v_exp_f32 v182, v182\n"
"v_mfma_f32_16x16x32_bf16 v[160:163], a[204:207], v[12:15], v[160:163]\n"
"ds_read_b64_tr_b16 a[248:249], v232 offset:32768\n"
"v_add_f32 v221, v221, v181\n"
"v_cvt_pk_bf16_f32 v190, v180, v181\n"
"v_mfma_f32_16x16x32_bf16 v[164:167], a[208:211], v[0:3], v[192:195]\n"
"v_exp_f32 v183, v183\n"
"v_mfma_f32_16x16x32_bf16 v[164:167], a[212:215], v[4:7], v[164:167]\n"
"ds_read_b64_tr_b16 a[250:251], v232 offset:33792\n"
"v_cvt_pk_bf16_f32 v191, v182, v183\n"
"v_add_f32 v221, v221, v182\n"
"s_waitcnt lgkmcnt(9)\n"
"v_mfma_f32_16x16x32_bf16 v[164:167], a[216:219], v[8:11], v[164:167]\n"
"v_exp_f32 v160, v160\n"
"s_waitcnt lgkmcnt(8)\n"
"v_mfma_f32_16x16x32_bf16 v[164:167], a[220:223], v[12:15], v[164:167]\n"
"ds_read_b64_tr_b16 a[252:253], v233 offset:32768\n"
"v_add_f32 v221, v221, v183\n"
"v_mfma_f32_16x16x32_bf16 a[20:23], v[128:131], v[188:191], a[20:23]\n"
"v_exp_f32 v161, v161\n"
"v_mfma_f32_16x16x32_bf16 a[44:47], v[132:135], v[188:191], a[44:47]\n"
"ds_read_b64_tr_b16 a[254:255], v233 offset:33792\n"
"v_add_f32 v216, v216, v160\n"
"v_mfma_f32_16x16x32_bf16 a[68:71], v[136:139], v[188:191], a[68:71]\n"
"v_exp_f32 v162, v162\n"
"v_mfma_f32_16x16x32_bf16 a[92:95], v[140:143], v[188:191], a[92:95]\n"
"v_add_f32 v216, v216, v161\n"
"v_cvt_pk_bf16_f32 v184, v160, v161\n"
"v_mfma_f32_16x16x32_bf16 a[116:119], v[144:147], v[188:191], a[116:119]\n"
"v_exp_f32 v163, v163\n"
"v_mfma_f32_16x16x32_bf16 a[140:143], v[148:151], v[188:191], a[140:143]\n"
"v_add_f32 v216, v216, v162\n"
"v_mfma_f32_16x16x32_bf16 a[164:167], v[152:155], v[188:191], a[164:167]\n"
"v_exp_f32 v164, v164\n"
"v_mfma_f32_16x16x32_bf16 a[188:191], v[156:159], v[188:191], a[188:191]\n"
"v_add_f32 v216, v216, v163\n"
"s_waitcnt lgkmcnt(0)\n"
"v_mfma_f32_16x16x32_bf16 v[168:171], a[192:195], v[16:19], v[196:199]\n"
"v_exp_f32 v165, v165\n"
"v_mfma_f32_16x16x32_bf16 v[168:171], a[196:199], v[20:23], v[168:171]\n"
"s_add_u32 m0, s64, 131072\n"
"v_add_f32 v216, v216, v164\n"
"v_cvt_pk_bf16_f32 v185, v162, v163\n"
"v_mfma_f32_16x16x32_bf16 v[168:171], a[200:203], v[24:27], v[168:171]\n"
"buffer_load_dwordx4 v244, s[48:51], s61 offen offset:0 lds\n"
"v_exp_f32 v166, v166\n"
"v_mfma_f32_16x16x32_bf16 v[168:171], a[204:207], v[28:31], v[168:171]\n"
"v_add_f32 v216, v216, v165\n"
"v_cvt_pk_bf16_f32 v186, v164, v165\n"
"v_mfma_f32_16x16x32_bf16 v[172:175], a[208:211], v[16:19], v[196:199]\n"
"v_exp_f32 v167, v167\n"
"v_mfma_f32_16x16x32_bf16 v[172:175], a[212:215], v[20:23], v[172:175]\n"
"v_cvt_pk_bf16_f32 v187, v166, v167\n"
"v_add_f32 v216, v216, v166\n"
"v_mfma_f32_16x16x32_bf16 v[172:175], a[216:219], v[24:27], v[172:175]\n"
"buffer_load_dwordx4 v245, s[48:51], s61 offen offset:1024 lds\n"
"v_exp_f32 v168, v168\n"
"v_mfma_f32_16x16x32_bf16 v[172:175], a[220:223], v[28:31], v[172:175]\n"
"ds_read_b128 v[96:99], v222 offset:49152\n"
"v_add_f32 v216, v216, v167\n"
"v_mfma_f32_16x16x32_bf16 a[0:3], a[224:227], v[184:187], a[0:3]\n"
"v_exp_f32 v169, v169\n"
"v_mfma_f32_16x16x32_bf16 a[24:27], a[228:231], v[184:187], a[24:27]\n"
"ds_read_b128 v[100:103], v223 offset:49152\n"
"v_add_f32 v217, v217, v168\n"
"v_mfma_f32_16x16x32_bf16 a[48:51], a[232:235], v[184:187], a[48:51]\n"
"buffer_load_dwordx4 v246, s[48:51], s61 offen offset:2048 lds\n"
"v_exp_f32 v170, v170\n"
"v_mfma_f32_16x16x32_bf16 a[72:75], a[236:239], v[184:187], a[72:75]\n"
"v_add_f32 v217, v217, v169\n"
"v_cvt_pk_bf16_f32 v188, v168, v169\n"
"v_mfma_f32_16x16x32_bf16 a[96:99], a[240:243], v[184:187], a[96:99]\n"
"v_exp_f32 v171, v171\n"
"v_mfma_f32_16x16x32_bf16 a[120:123], a[244:247], v[184:187], a[120:123]\n"
"ds_read_b64_tr_b16 v[128:129], v226 offset:49152\n"
"v_add_f32 v217, v217, v170\n"
"v_mfma_f32_16x16x32_bf16 a[144:147], a[248:251], v[184:187], a[144:147]\n"
"buffer_load_dwordx4 v247, s[48:51], s61 offen offset:3072 lds\n"
"v_exp_f32 v172, v172\n"
"v_mfma_f32_16x16x32_bf16 a[168:171], a[252:255], v[184:187], a[168:171]\n"
"ds_read_b64_tr_b16 v[130:131], v226 offset:50176\n"
"v_add_f32 v217, v217, v171\n"
"v_mfma_f32_16x16x32_bf16 v[176:179], a[192:195], v[32:35], v[200:203]\n"
"v_exp_f32 v173, v173\n"
"v_mfma_f32_16x16x32_bf16 v[176:179], a[196:199], v[36:39], v[176:179]\n"
"s_add_u32 m0, s64, 135168\n"
"v_add_f32 v217, v217, v172\n"
"v_cvt_pk_bf16_f32 v189, v170, v171\n"
"v_mfma_f32_16x16x32_bf16 v[176:179], a[200:203], v[40:43], v[176:179]\n"
"buffer_load_dwordx4 v244, s[48:51], s62 offen offset:0 lds\n"
"v_exp_f32 v174, v174\n"
"v_mfma_f32_16x16x32_bf16 v[176:179], a[204:207], v[44:47], v[176:179]\n"
"v_add_f32 v217, v217, v173\n"
"v_cvt_pk_bf16_f32 v190, v172, v173\n"
"v_mfma_f32_16x16x32_bf16 v[180:183], a[208:211], v[32:35], v[200:203]\n"
"v_exp_f32 v175, v175\n"
"v_mfma_f32_16x16x32_bf16 v[180:183], a[212:215], v[36:39], v[180:183]\n"
"v_cvt_pk_bf16_f32 v191, v174, v175\n"
"v_add_f32 v217, v217, v174\n"
"v_mfma_f32_16x16x32_bf16 v[180:183], a[216:219], v[40:43], v[180:183]\n"
"buffer_load_dwordx4 v245, s[48:51], s62 offen offset:1024 lds\n"
"v_exp_f32 v176, v176\n"
"v_mfma_f32_16x16x32_bf16 v[180:183], a[220:223], v[44:47], v[180:183]\n"
"ds_read_b128 v[104:107], v224 offset:49152\n"
"v_add_f32 v217, v217, v175\n"
"v_mfma_f32_16x16x32_bf16 a[4:7], a[224:227], v[188:191], a[4:7]\n"
"v_exp_f32 v177, v177\n"
"v_mfma_f32_16x16x32_bf16 a[28:31], a[228:231], v[188:191], a[28:31]\n"
"ds_read_b128 v[108:111], v225 offset:49152\n"
"v_add_f32 v218, v218, v176\n"
"v_mfma_f32_16x16x32_bf16 a[52:55], a[232:235], v[188:191], a[52:55]\n"
"buffer_load_dwordx4 v246, s[48:51], s62 offen offset:2048 lds\n"
"v_exp_f32 v178, v178\n"
"v_mfma_f32_16x16x32_bf16 a[76:79], a[236:239], v[188:191], a[76:79]\n"
"v_add_f32 v218, v218, v177\n"
"v_cvt_pk_bf16_f32 v184, v176, v177\n"
"v_mfma_f32_16x16x32_bf16 a[100:103], a[240:243], v[188:191], a[100:103]\n"
"v_exp_f32 v179, v179\n"
"v_mfma_f32_16x16x32_bf16 a[124:127], a[244:247], v[188:191], a[124:127]\n"
"ds_read_b64_tr_b16 v[132:133], v227 offset:49152\n"
"v_add_f32 v218, v218, v178\n"
"v_mfma_f32_16x16x32_bf16 a[148:151], a[248:251], v[188:191], a[148:151]\n"
"buffer_load_dwordx4 v247, s[48:51], s62 offen offset:3072 lds\n"
"v_exp_f32 v180, v180\n"
"v_mfma_f32_16x16x32_bf16 a[172:175], a[252:255], v[188:191], a[172:175]\n"
"ds_read_b64_tr_b16 v[134:135], v227 offset:50176\n"
"s_add_u32 s61, s61, 524288\n"
"s_add_u32 s62, s62, 524288\n"
"v_add_f32 v218, v218, v179\n"
"v_mfma_f32_16x16x32_bf16 v[160:163], a[192:195], v[48:51], v[204:207]\n"
"s_mov_b32 s78, s56\n"
"s_add_u32 s76, s76, 512\n"
"v_exp_f32 v181, v181\n"
"v_mfma_f32_16x16x32_bf16 v[160:163], a[196:199], v[52:55], v[160:163]\n"
"v_mbcnt_lo_u32_b32 v253, -1, 0\n"
"v_mbcnt_hi_u32_b32 v253, -1, v253\n"
"v_add_f32 v218, v218, v180\n"
"v_cvt_pk_bf16_f32 v185, v178, v179\n"
"v_mfma_f32_16x16x32_bf16 v[160:163], a[200:203], v[56:59], v[160:163]\n"
"v_lshrrev_b32 v252, 3, v253\n"
"v_lshlrev_b32 v252, 19, v252\n"
"v_exp_f32 v182, v182\n"
"v_mfma_f32_16x16x32_bf16 v[160:163], a[204:207], v[60:63], v[160:163]\n"
"v_and_b32 v253, 7, v253\n"
"v_lshl_add_u32 v252, v253, 7, v252\n"
"v_add_f32 v218, v218, v181\n"
"v_cvt_pk_bf16_f32 v186, v180, v181\n"
"v_mfma_f32_16x16x32_bf16 v[164:167], a[208:211], v[48:51], v[204:207]\n"
"v_lshrrev_b32 v253, 2, v253\n"
"v_mul_u32_u24 v253, 7680, v253\n"
"v_exp_f32 v183, v183\n"
"v_mfma_f32_16x16x32_bf16 v[164:167], a[212:215], v[52:55], v[164:167]\n"
"v_add_u32 v252, v252, v253\n"
"s_add_u32 s73, s76, 1048576\n"
"v_cvt_pk_bf16_f32 v187, v182, v183\n"
"v_add_f32 v218, v218, v182\n"
"v_mfma_f32_16x16x32_bf16 v[164:167], a[216:219], v[56:59], v[164:167]\n"
"s_bfm_b64 exec, 56, 0\n"
"buffer_load_dword v255, v252, s[48:51], s73 offen\n"
"s_mov_b64 exec, -1\n"
"v_mbcnt_lo_u32_b32 v252, -1, 0\n"
"v_exp_f32 v160, v160\n"
"v_mfma_f32_16x16x32_bf16 v[164:167], a[220:223], v[60:63], v[164:167]\n"
"ds_read_b128 v[112:115], v222 offset:50176\n"
"v_mbcnt_hi_u32_b32 v252, -1, v252\n"
"v_lshlrev_b32 v252, 4, v252\n"
"v_add_f32 v218, v218, v183\n"
"v_mfma_f32_16x16x32_bf16 a[8:11], a[224:227], v[184:187], a[8:11]\n"
"v_exp_f32 v161, v161\n"
"v_mfma_f32_16x16x32_bf16 a[32:35], a[228:231], v[184:187], a[32:35]\n"
"ds_read_b128 v[116:119], v223 offset:50176\n"
"v_add_f32 v219, v219, v160\n"
"v_mfma_f32_16x16x32_bf16 a[56:59], a[232:235], v[184:187], a[56:59]\n"
"v_exp_f32 v162, v162\n"
"v_mfma_f32_16x16x32_bf16 a[80:83], a[236:239], v[184:187], a[80:83]\n"
"v_add_f32 v219, v219, v161\n"
"v_cvt_pk_bf16_f32 v188, v160, v161\n"
"v_mfma_f32_16x16x32_bf16 a[104:107], a[240:243], v[184:187], a[104:107]\n"
"v_exp_f32 v163, v163\n"
"v_mfma_f32_16x16x32_bf16 a[128:131], a[244:247], v[184:187], a[128:131]\n"
"ds_read_b64_tr_b16 v[136:137], v228 offset:49152\n"
"v_add_f32 v219, v219, v162\n"
"v_mfma_f32_16x16x32_bf16 a[152:155], a[248:251], v[184:187], a[152:155]\n"
"v_exp_f32 v164, v164\n"
"v_mfma_f32_16x16x32_bf16 a[176:179], a[252:255], v[184:187], a[176:179]\n"
"ds_read_b64_tr_b16 v[138:139], v228 offset:50176\n"
"v_add_f32 v219, v219, v163\n"
"v_mfma_f32_16x16x32_bf16 v[168:171], a[192:195], v[64:67], v[208:211]\n"
"v_exp_f32 v165, v165\n"
"v_mfma_f32_16x16x32_bf16 v[168:171], a[196:199], v[68:71], v[168:171]\n"
"v_add_f32 v219, v219, v164\n"
"v_cvt_pk_bf16_f32 v189, v162, v163\n"
"v_mfma_f32_16x16x32_bf16 v[168:171], a[200:203], v[72:75], v[168:171]\n"
"v_exp_f32 v166, v166\n"
"v_mfma_f32_16x16x32_bf16 v[168:171], a[204:207], v[76:79], v[168:171]\n"
"v_add_f32 v219, v219, v165\n"
"v_cvt_pk_bf16_f32 v190, v164, v165\n"
"v_mfma_f32_16x16x32_bf16 v[172:175], a[208:211], v[64:67], v[208:211]\n"
"v_exp_f32 v167, v167\n"
"v_mfma_f32_16x16x32_bf16 v[172:175], a[212:215], v[68:71], v[172:175]\n"
"v_cvt_pk_bf16_f32 v191, v166, v167\n"
"v_add_f32 v219, v219, v166\n"
"v_mfma_f32_16x16x32_bf16 v[172:175], a[216:219], v[72:75], v[172:175]\n"
"v_exp_f32 v168, v168\n"
"v_mfma_f32_16x16x32_bf16 v[172:175], a[220:223], v[76:79], v[172:175]\n"
"ds_read_b128 v[120:123], v224 offset:50176\n"
"v_add_f32 v219, v219, v167\n"
"v_mfma_f32_16x16x32_bf16 a[12:15], a[224:227], v[188:191], a[12:15]\n"
"v_exp_f32 v169, v169\n"
"v_mfma_f32_16x16x32_bf16 a[36:39], a[228:231], v[188:191], a[36:39]\n"
"ds_read_b128 v[124:127], v225 offset:50176\n"
"v_add_f32 v220, v220, v168\n"
"v_mfma_f32_16x16x32_bf16 a[60:63], a[232:235], v[188:191], a[60:63]\n"
"v_exp_f32 v170, v170\n"
"v_mfma_f32_16x16x32_bf16 a[84:87], a[236:239], v[188:191], a[84:87]\n"
"v_add_f32 v220, v220, v169\n"
"v_cvt_pk_bf16_f32 v184, v168, v169\n"
"v_mfma_f32_16x16x32_bf16 a[108:111], a[240:243], v[188:191], a[108:111]\n"
"v_exp_f32 v171, v171\n"
"v_mfma_f32_16x16x32_bf16 a[132:135], a[244:247], v[188:191], a[132:135]\n"
"ds_read_b64_tr_b16 v[140:141], v229 offset:49152\n"
"v_add_f32 v220, v220, v170\n"
"v_mfma_f32_16x16x32_bf16 a[156:159], a[248:251], v[188:191], a[156:159]\n"
"v_exp_f32 v172, v172\n"
"v_mfma_f32_16x16x32_bf16 a[180:183], a[252:255], v[188:191], a[180:183]\n"
"ds_read_b64_tr_b16 v[142:143], v229 offset:50176\n"
"v_add_f32 v220, v220, v171\n"
"v_mfma_f32_16x16x32_bf16 v[176:179], a[192:195], v[80:83], v[212:215]\n"
"v_exp_f32 v173, v173\n"
"v_mfma_f32_16x16x32_bf16 v[176:179], a[196:199], v[84:87], v[176:179]\n"
"v_add_f32 v220, v220, v172\n"
"v_cvt_pk_bf16_f32 v185, v170, v171\n"
"v_mfma_f32_16x16x32_bf16 v[176:179], a[200:203], v[88:91], v[176:179]\n"
"v_exp_f32 v174, v174\n"
"v_mfma_f32_16x16x32_bf16 v[176:179], a[204:207], v[92:95], v[176:179]\n"
"v_add_f32 v220, v220, v173\n"
"v_cvt_pk_bf16_f32 v186, v172, v173\n"
"v_mfma_f32_16x16x32_bf16 v[180:183], a[208:211], v[80:83], v[212:215]\n"
"v_exp_f32 v175, v175\n"
"v_mfma_f32_16x16x32_bf16 v[180:183], a[212:215], v[84:87], v[180:183]\n"
"v_cvt_pk_bf16_f32 v187, v174, v175\n"
"v_add_f32 v220, v220, v174\n"
"v_mfma_f32_16x16x32_bf16 v[180:183], a[216:219], v[88:91], v[180:183]\n"
"v_exp_f32 v176, v176\n"
"v_mfma_f32_16x16x32_bf16 v[180:183], a[220:223], v[92:95], v[180:183]\n"
"ds_read_b64_tr_b16 v[144:145], v230 offset:49152\n"
"v_add_f32 v220, v220, v175\n"
"v_mfma_f32_16x16x32_bf16 a[16:19], a[224:227], v[184:187], a[16:19]\n"
"v_exp_f32 v177, v177\n"
"v_mfma_f32_16x16x32_bf16 a[40:43], a[228:231], v[184:187], a[40:43]\n"
"ds_read_b64_tr_b16 v[146:147], v230 offset:50176\n"
"v_add_f32 v221, v221, v176\n"
"v_mfma_f32_16x16x32_bf16 a[64:67], a[232:235], v[184:187], a[64:67]\n"
"v_exp_f32 v178, v178\n"
"v_mfma_f32_16x16x32_bf16 a[88:91], a[236:239], v[184:187], a[88:91]\n"
"v_add_f32 v221, v221, v177\n"
"v_cvt_pk_bf16_f32 v188, v176, v177\n"
"v_mfma_f32_16x16x32_bf16 a[112:115], a[240:243], v[184:187], a[112:115]\n"
"v_exp_f32 v179, v179\n"
"v_mfma_f32_16x16x32_bf16 a[136:139], a[244:247], v[184:187], a[136:139]\n"
"ds_read_b64_tr_b16 v[148:149], v231 offset:49152\n"
"v_add_f32 v221, v221, v178\n"
"v_mfma_f32_16x16x32_bf16 a[160:163], a[248:251], v[184:187], a[160:163]\n"
"v_exp_f32 v180, v180\n"
"v_mfma_f32_16x16x32_bf16 a[184:187], a[252:255], v[184:187], a[184:187]\n"
"ds_read_b64_tr_b16 v[150:151], v231 offset:50176\n"
"v_add_f32 v221, v221, v179\n"
"s_waitcnt lgkmcnt(8)\n"
"v_mfma_f32_16x16x32_bf16 v[160:163], v[96:99], v[0:3], v[192:195]\n"
"v_exp_f32 v181, v181\n"
"v_mfma_f32_16x16x32_bf16 v[160:163], v[100:103], v[4:7], v[160:163]\n"
"v_add_f32 v221, v221, v180\n"
"v_cvt_pk_bf16_f32 v189, v178, v179\n"
"v_mfma_f32_16x16x32_bf16 v[160:163], v[104:107], v[8:11], v[160:163]\n"
"v_exp_f32 v182, v182\n"
"v_mfma_f32_16x16x32_bf16 v[160:163], v[108:111], v[12:15], v[160:163]\n"
"ds_read_b64_tr_b16 v[152:153], v232 offset:49152\n"
"v_add_f32 v221, v221, v181\n"
"v_cvt_pk_bf16_f32 v190, v180, v181\n"
"v_mfma_f32_16x16x32_bf16 v[164:167], v[112:115], v[0:3], v[192:195]\n"
"v_exp_f32 v183, v183\n"
"v_mfma_f32_16x16x32_bf16 v[164:167], v[116:119], v[4:7], v[164:167]\n"
"ds_read_b64_tr_b16 v[154:155], v232 offset:50176\n"
"v_cvt_pk_bf16_f32 v191, v182, v183\n"
"v_add_f32 v221, v221, v182\n"
"s_waitcnt lgkmcnt(9)\n"
"v_mfma_f32_16x16x32_bf16 v[164:167], v[120:123], v[8:11], v[164:167]\n"
"v_exp_f32 v160, v160\n"
"s_waitcnt lgkmcnt(8)\n"
"v_mfma_f32_16x16x32_bf16 v[164:167], v[124:127], v[12:15], v[164:167]\n"
"ds_read_b64_tr_b16 v[156:157], v233 offset:49152\n"
"v_add_f32 v221, v221, v183\n"
"v_mfma_f32_16x16x32_bf16 a[20:23], a[224:227], v[188:191], a[20:23]\n"
"v_exp_f32 v161, v161\n"
"v_mfma_f32_16x16x32_bf16 a[44:47], a[228:231], v[188:191], a[44:47]\n"
"ds_read_b64_tr_b16 v[158:159], v233 offset:50176\n"
"v_add_f32 v216, v216, v160\n"
"v_mfma_f32_16x16x32_bf16 a[68:71], a[232:235], v[188:191], a[68:71]\n"
"v_exp_f32 v162, v162\n"
"v_mfma_f32_16x16x32_bf16 a[92:95], a[236:239], v[188:191], a[92:95]\n"
"v_add_f32 v216, v216, v161\n"
"v_cvt_pk_bf16_f32 v184, v160, v161\n"
"v_mfma_f32_16x16x32_bf16 a[116:119], a[240:243], v[188:191], a[116:119]\n"
"v_exp_f32 v163, v163\n"
"v_mfma_f32_16x16x32_bf16 a[140:143], a[244:247], v[188:191], a[140:143]\n"
"v_add_f32 v216, v216, v162\n"
"v_mfma_f32_16x16x32_bf16 a[164:167], a[248:251], v[188:191], a[164:167]\n"
"v_exp_f32 v164, v164\n"
"v_mfma_f32_16x16x32_bf16 a[188:191], a[252:255], v[188:191], a[188:191]\n"
"v_add_f32 v216, v216, v163\n"
"s_waitcnt lgkmcnt(0)\n"
"s_barrier\n"
"v_mfma_f32_16x16x32_bf16 v[168:171], v[96:99], v[16:19], v[196:199]\n"
"v_exp_f32 v165, v165\n"
"v_mfma_f32_16x16x32_bf16 v[168:171], v[100:103], v[20:23], v[168:171]\n"
"s_add_u32 m0, s63, 106496\n"
"v_add_f32 v216, v216, v164\n"
"v_cvt_pk_bf16_f32 v185, v162, v163\n"
"v_mfma_f32_16x16x32_bf16 v[168:171], v[104:107], v[24:27], v[168:171]\n"
"buffer_load_dwordx4 v248, s[44:47], s59 offen offset:0 lds\n"
"v_exp_f32 v166, v166\n"
"v_mfma_f32_16x16x32_bf16 v[168:171], v[108:111], v[28:31], v[168:171]\n"
"v_add_f32 v216, v216, v165\n"
"v_cvt_pk_bf16_f32 v186, v164, v165\n"
"v_mfma_f32_16x16x32_bf16 v[172:175], v[112:115], v[16:19], v[196:199]\n"
"v_exp_f32 v167, v167\n"
"v_mfma_f32_16x16x32_bf16 v[172:175], v[116:119], v[20:23], v[172:175]\n"
"buffer_load_dwordx4 v249, s[44:47], s59 offen offset:1024 lds\n"
"v_cvt_pk_bf16_f32 v187, v166, v167\n"
"v_add_f32 v216, v216, v166\n"
"v_mfma_f32_16x16x32_bf16 v[172:175], v[120:123], v[24:27], v[172:175]\n"
"v_exp_f32 v168, v168\n"
"v_mfma_f32_16x16x32_bf16 v[172:175], v[124:127], v[28:31], v[172:175]\n"
"v_add_f32 v216, v216, v167\n"
"v_mfma_f32_16x16x32_bf16 a[0:3], v[128:131], v[184:187], a[0:3]\n"
"buffer_load_dwordx4 v250, s[44:47], s59 offen offset:2048 lds\n"
"v_exp_f32 v169, v169\n"
"v_mfma_f32_16x16x32_bf16 a[24:27], v[132:135], v[184:187], a[24:27]\n"
"v_add_f32 v217, v217, v168\n"
"v_mfma_f32_16x16x32_bf16 a[48:51], v[136:139], v[184:187], a[48:51]\n"
"v_exp_f32 v170, v170\n"
"v_mfma_f32_16x16x32_bf16 a[72:75], v[140:143], v[184:187], a[72:75]\n"
"buffer_load_dwordx4 v251, s[44:47], s59 offen offset:3072 lds\n"
"v_add_f32 v217, v217, v169\n"
"v_cvt_pk_bf16_f32 v188, v168, v169\n"
"v_mfma_f32_16x16x32_bf16 a[96:99], v[144:147], v[184:187], a[96:99]\n"
"v_exp_f32 v171, v171\n"
"v_mfma_f32_16x16x32_bf16 a[120:123], v[148:151], v[184:187], a[120:123]\n"
"s_add_u32 m0, s63, 110592\n"
"v_add_f32 v217, v217, v170\n"
"v_mfma_f32_16x16x32_bf16 a[144:147], v[152:155], v[184:187], a[144:147]\n"
"buffer_load_dwordx4 v248, s[44:47], s60 offen offset:0 lds\n"
"v_exp_f32 v172, v172\n"
"v_mfma_f32_16x16x32_bf16 a[168:171], v[156:159], v[184:187], a[168:171]\n"
"v_add_f32 v217, v217, v171\n"
"v_mfma_f32_16x16x32_bf16 v[176:179], v[96:99], v[32:35], v[200:203]\n"
"v_exp_f32 v173, v173\n"
"v_mfma_f32_16x16x32_bf16 v[176:179], v[100:103], v[36:39], v[176:179]\n"
"buffer_load_dwordx4 v249, s[44:47], s60 offen offset:1024 lds\n"
"v_add_f32 v217, v217, v172\n"
"v_cvt_pk_bf16_f32 v189, v170, v171\n"
"v_mfma_f32_16x16x32_bf16 v[176:179], v[104:107], v[40:43], v[176:179]\n"
"s_add_u32 s59, s59, 128\n"
"s_add_u32 s60, s60, 128\n"
"v_exp_f32 v174, v174\n"
"v_mfma_f32_16x16x32_bf16 v[176:179], v[108:111], v[44:47], v[176:179]\n"
"s_add_u32 m0, s64, 32768\n"
"v_add_f32 v217, v217, v173\n"
"v_cvt_pk_bf16_f32 v190, v172, v173\n"
"v_mfma_f32_16x16x32_bf16 v[180:183], v[112:115], v[32:35], v[200:203]\n"
"buffer_load_dwordx4 v244, s[48:51], s61 offen offset:0 lds\n"
"v_exp_f32 v175, v175\n"
"v_mfma_f32_16x16x32_bf16 v[180:183], v[116:119], v[36:39], v[180:183]\n"
"v_cvt_pk_bf16_f32 v191, v174, v175\n"
"v_add_f32 v217, v217, v174\n"
"v_mfma_f32_16x16x32_bf16 v[180:183], v[120:123], v[40:43], v[180:183]\n"
"v_exp_f32 v176, v176\n"
"v_mfma_f32_16x16x32_bf16 v[180:183], v[124:127], v[44:47], v[180:183]\n"
"buffer_load_dwordx4 v245, s[48:51], s61 offen offset:1024 lds\n"
"v_add_f32 v217, v217, v175\n"
"v_mfma_f32_16x16x32_bf16 a[4:7], v[128:131], v[188:191], a[4:7]\n"
"v_exp_f32 v177, v177\n"
"v_mfma_f32_16x16x32_bf16 a[28:31], v[132:135], v[188:191], a[28:31]\n"
"v_add_f32 v218, v218, v176\n"
"v_mfma_f32_16x16x32_bf16 a[52:55], v[136:139], v[188:191], a[52:55]\n"
"buffer_load_dwordx4 v246, s[48:51], s61 offen offset:2048 lds\n"
"v_exp_f32 v178, v178\n"
"v_mfma_f32_16x16x32_bf16 a[76:79], v[140:143], v[188:191], a[76:79]\n"
"v_add_f32 v218, v218, v177\n"
"v_cvt_pk_bf16_f32 v184, v176, v177\n"
"v_mfma_f32_16x16x32_bf16 a[100:103], v[144:147], v[188:191], a[100:103]\n"
"v_exp_f32 v179, v179\n"
"v_mfma_f32_16x16x32_bf16 a[124:127], v[148:151], v[188:191], a[124:127]\n"
"buffer_load_dwordx4 v247, s[48:51], s61 offen offset:3072 lds\n"
"v_add_f32 v218, v218, v178\n"
"v_mfma_f32_16x16x32_bf16 a[148:151], v[152:155], v[188:191], a[148:151]\n"
"v_exp_f32 v180, v180\n"
"v_mfma_f32_16x16x32_bf16 a[172:175], v[156:159], v[188:191], a[172:175]\n"
"s_add_u32 m0, s64, 36864\n"
"v_add_f32 v218, v218, v179\n"
"v_mfma_f32_16x16x32_bf16 v[160:163], v[96:99], v[48:51], v[204:207]\n"
"buffer_load_dwordx4 v244, s[48:51], s62 offen offset:0 lds\n"
"v_exp_f32 v181, v181\n"
"v_mfma_f32_16x16x32_bf16 v[160:163], v[100:103], v[52:55], v[160:163]\n"
"v_add_f32 v218, v218, v180\n"
"v_cvt_pk_bf16_f32 v185, v178, v179\n"
"v_mfma_f32_16x16x32_bf16 v[160:163], v[104:107], v[56:59], v[160:163]\n"
"v_exp_f32 v182, v182\n"
"v_mfma_f32_16x16x32_bf16 v[160:163], v[108:111], v[60:63], v[160:163]\n"
"buffer_load_dwordx4 v245, s[48:51], s62 offen offset:1024 lds\n"
"v_add_f32 v218, v218, v181\n"
"v_cvt_pk_bf16_f32 v186, v180, v181\n"
"v_mfma_f32_16x16x32_bf16 v[164:167], v[112:115], v[48:51], v[204:207]\n"
"v_exp_f32 v183, v183\n"
"v_mfma_f32_16x16x32_bf16 v[164:167], v[116:119], v[52:55], v[164:167]\n"
"v_cvt_pk_bf16_f32 v187, v182, v183\n"
"v_add_f32 v218, v218, v182\n"
"v_mfma_f32_16x16x32_bf16 v[164:167], v[120:123], v[56:59], v[164:167]\n"
"buffer_load_dwordx4 v246, s[48:51], s62 offen offset:2048 lds\n"
"v_exp_f32 v160, v160\n"
"v_mfma_f32_16x16x32_bf16 v[164:167], v[124:127], v[60:63], v[164:167]\n"
"v_add_f32 v218, v218, v183\n"
"v_mfma_f32_16x16x32_bf16 a[8:11], v[128:131], v[184:187], a[8:11]\n"
"v_exp_f32 v161, v161\n"
"v_mfma_f32_16x16x32_bf16 a[32:35], v[132:135], v[184:187], a[32:35]\n"
"buffer_load_dwordx4 v247, s[48:51], s62 offen offset:3072 lds\n"
"v_add_f32 v219, v219, v160\n"
"v_mfma_f32_16x16x32_bf16 a[56:59], v[136:139], v[184:187], a[56:59]\n"
"s_add_u32 s61, s61, 524288\n"
"s_add_u32 s62, s62, 524288\n"
"v_exp_f32 v162, v162\n"
"v_mfma_f32_16x16x32_bf16 a[80:83], v[140:143], v[184:187], a[80:83]\n"
"s_add_u32 m0, s63, 81920\n"
"v_add_f32 v219, v219, v161\n"
"v_cvt_pk_bf16_f32 v188, v160, v161\n"
"v_mfma_f32_16x16x32_bf16 a[104:107], v[144:147], v[184:187], a[104:107]\n"
"buffer_load_dwordx4 v248, s[44:47], s59 offen offset:0 lds\n"
"v_exp_f32 v163, v163\n"
"v_mfma_f32_16x16x32_bf16 a[128:131], v[148:151], v[184:187], a[128:131]\n"
"v_add_f32 v219, v219, v162\n"
"v_mfma_f32_16x16x32_bf16 a[152:155], v[152:155], v[184:187], a[152:155]\n"
"v_exp_f32 v164, v164\n"
"v_mfma_f32_16x16x32_bf16 a[176:179], v[156:159], v[184:187], a[176:179]\n"
"buffer_load_dwordx4 v249, s[44:47], s59 offen offset:1024 lds\n"
"v_add_f32 v219, v219, v163\n"
"v_mfma_f32_16x16x32_bf16 v[168:171], v[96:99], v[64:67], v[208:211]\n"
"v_exp_f32 v165, v165\n"
"v_mfma_f32_16x16x32_bf16 v[168:171], v[100:103], v[68:71], v[168:171]\n"
"v_add_f32 v219, v219, v164\n"
"v_cvt_pk_bf16_f32 v189, v162, v163\n"
"v_mfma_f32_16x16x32_bf16 v[168:171], v[104:107], v[72:75], v[168:171]\n"
"buffer_load_dwordx4 v250, s[44:47], s59 offen offset:2048 lds\n"
"v_exp_f32 v166, v166\n"
"v_mfma_f32_16x16x32_bf16 v[168:171], v[108:111], v[76:79], v[168:171]\n"
"v_add_f32 v219, v219, v165\n"
"v_cvt_pk_bf16_f32 v190, v164, v165\n"
"v_mfma_f32_16x16x32_bf16 v[172:175], v[112:115], v[64:67], v[208:211]\n"
"v_exp_f32 v167, v167\n"
"v_mfma_f32_16x16x32_bf16 v[172:175], v[116:119], v[68:71], v[172:175]\n"
"buffer_load_dwordx4 v251, s[44:47], s59 offen offset:3072 lds\n"
"v_cvt_pk_bf16_f32 v191, v166, v167\n"
"v_add_f32 v219, v219, v166\n"
"v_mfma_f32_16x16x32_bf16 v[172:175], v[120:123], v[72:75], v[172:175]\n"
"v_exp_f32 v168, v168\n"
"v_mfma_f32_16x16x32_bf16 v[172:175], v[124:127], v[76:79], v[172:175]\n"
"s_add_u32 m0, s63, 86016\n"
"v_add_f32 v219, v219, v167\n"
"v_mfma_f32_16x16x32_bf16 a[12:15], v[128:131], v[188:191], a[12:15]\n"
"buffer_load_dwordx4 v248, s[44:47], s60 offen offset:0 lds\n"
"v_exp_f32 v169, v169\n"
"v_mfma_f32_16x16x32_bf16 a[36:39], v[132:135], v[188:191], a[36:39]\n"
"v_add_f32 v220, v220, v168\n"
"v_mfma_f32_16x16x32_bf16 a[60:63], v[136:139], v[188:191], a[60:63]\n"
"v_exp_f32 v170, v170\n"
"v_mfma_f32_16x16x32_bf16 a[84:87], v[140:143], v[188:191], a[84:87]\n"
"buffer_load_dwordx4 v249, s[44:47], s60 offen offset:1024 lds\n"
"v_add_f32 v220, v220, v169\n"
"v_cvt_pk_bf16_f32 v184, v168, v169\n"
"v_mfma_f32_16x16x32_bf16 a[108:111], v[144:147], v[188:191], a[108:111]\n"
"s_add_u32 s59, s59, 128\n"
"s_add_u32 s60, s60, 128\n"
"v_exp_f32 v171, v171\n"
"v_mfma_f32_16x16x32_bf16 a[132:135], v[148:151], v[188:191], a[132:135]\n"
"v_add_f32 v220, v220, v170\n"
"v_mfma_f32_16x16x32_bf16 a[156:159], v[152:155], v[188:191], a[156:159]\n"
"v_exp_f32 v172, v172\n"
"v_mfma_f32_16x16x32_bf16 a[180:183], v[156:159], v[188:191], a[180:183]\n"
"v_add_f32 v220, v220, v171\n"
"v_mfma_f32_16x16x32_bf16 v[176:179], v[96:99], v[80:83], v[212:215]\n"
"v_exp_f32 v173, v173\n"
"v_mfma_f32_16x16x32_bf16 v[176:179], v[100:103], v[84:87], v[176:179]\n"
"v_add_f32 v220, v220, v172\n"
"v_cvt_pk_bf16_f32 v185, v170, v171\n"
"v_mfma_f32_16x16x32_bf16 v[176:179], v[104:107], v[88:91], v[176:179]\n"
"v_exp_f32 v174, v174\n"
"v_mfma_f32_16x16x32_bf16 v[176:179], v[108:111], v[92:95], v[176:179]\n"
"v_add_f32 v220, v220, v173\n"
"v_cvt_pk_bf16_f32 v186, v172, v173\n"
"v_mfma_f32_16x16x32_bf16 v[180:183], v[112:115], v[80:83], v[212:215]\n"
"v_exp_f32 v175, v175\n"
"v_mfma_f32_16x16x32_bf16 v[180:183], v[116:119], v[84:87], v[180:183]\n"
"v_cvt_pk_bf16_f32 v187, v174, v175\n"
"v_add_f32 v220, v220, v174\n"
"v_mfma_f32_16x16x32_bf16 v[180:183], v[120:123], v[88:91], v[180:183]\n"
"v_exp_f32 v176, v176\n"
"v_mfma_f32_16x16x32_bf16 v[180:183], v[124:127], v[92:95], v[180:183]\n"
"v_add_f32 v220, v220, v175\n"
"v_mfma_f32_16x16x32_bf16 a[16:19], v[128:131], v[184:187], a[16:19]\n"
"v_exp_f32 v177, v177\n"
"v_mfma_f32_16x16x32_bf16 a[40:43], v[132:135], v[184:187], a[40:43]\n"
"v_add_f32 v221, v221, v176\n"
"v_mfma_f32_16x16x32_bf16 a[64:67], v[136:139], v[184:187], a[64:67]\n"
"v_exp_f32 v178, v178\n"
"v_mfma_f32_16x16x32_bf16 a[88:91], v[140:143], v[184:187], a[88:91]\n"
"v_add_f32 v221, v221, v177\n"
"v_cvt_pk_bf16_f32 v188, v176, v177\n"
"v_mfma_f32_16x16x32_bf16 a[112:115], v[144:147], v[184:187], a[112:115]\n"
"v_exp_f32 v179, v179\n"
"v_mfma_f32_16x16x32_bf16 a[136:139], v[148:151], v[184:187], a[136:139]\n"
"v_add_f32 v221, v221, v178\n"
"v_mfma_f32_16x16x32_bf16 a[160:163], v[152:155], v[184:187], a[160:163]\n"
"v_exp_f32 v180, v180\n"
"v_mfma_f32_16x16x32_bf16 a[184:187], v[156:159], v[184:187], a[184:187]\n"
"v_add_f32 v221, v221, v179\n"
"v_exp_f32 v181, v181\n"
"v_add_f32 v221, v221, v180\n"
"v_cvt_pk_bf16_f32 v189, v178, v179\n"
"v_exp_f32 v182, v182\n"
"v_add_f32 v221, v221, v181\n"
"v_cvt_pk_bf16_f32 v190, v180, v181\n"
"v_exp_f32 v183, v183\n"
"s_nop 0\n"
"v_cvt_pk_bf16_f32 v191, v182, v183\n"
"v_add_f32 v221, v221, v182\n"
"v_add_f32 v221, v221, v183\n"
"v_mfma_f32_16x16x32_bf16 a[20:23], v[128:131], v[188:191], a[20:23]\n"
"v_mfma_f32_16x16x32_bf16 a[44:47], v[132:135], v[188:191], a[44:47]\n"
"v_mfma_f32_16x16x32_bf16 a[68:71], v[136:139], v[188:191], a[68:71]\n"
"v_mfma_f32_16x16x32_bf16 a[92:95], v[140:143], v[188:191], a[92:95]\n"
"v_mfma_f32_16x16x32_bf16 a[116:119], v[144:147], v[188:191], a[116:119]\n"
"v_mfma_f32_16x16x32_bf16 a[140:143], v[148:151], v[188:191], a[140:143]\n"
"v_mfma_f32_16x16x32_bf16 a[164:167], v[152:155], v[188:191], a[164:167]\n"
"v_mfma_f32_16x16x32_bf16 a[188:191], v[156:159], v[188:191], a[188:191]\n"
"s_waitcnt vmcnt(33)\n"
"v_mov_b32 v253, v216\n"
"s_nop 1\n"
"v_permlane32_swap_b32 v216, v253\n"
"v_add_f32 v216, v216, v253\n"
"v_mov_b32 v253, v216\n"
"s_nop 1\n"
"v_permlane16_swap_b32 v216, v253\n"
"v_add_f32 v216, v216, v253\n"
"v_mov_b32 v253, v217\n"
"s_nop 1\n"
"v_permlane32_swap_b32 v217, v253\n"
"v_add_f32 v217, v217, v253\n"
"v_mov_b32 v253, v217\n"
"s_nop 1\n"
"v_permlane16_swap_b32 v217, v253\n"
"v_add_f32 v217, v217, v253\n"
"v_mov_b32 v253, v218\n"
"s_nop 1\n"
"v_permlane32_swap_b32 v218, v253\n"
"v_add_f32 v218, v218, v253\n"
"v_mov_b32 v253, v218\n"
"s_nop 1\n"
"v_permlane16_swap_b32 v218, v253\n"
"v_add_f32 v218, v218, v253\n"
"v_mov_b32 v253, v219\n"
"s_nop 1\n"
"v_permlane32_swap_b32 v219, v253\n"
"v_add_f32 v219, v219, v253\n"
"v_mov_b32 v253, v219\n"
"s_nop 1\n"
"v_permlane16_swap_b32 v219, v253\n"
"v_add_f32 v219, v219, v253\n"
"v_mov_b32 v253, v220\n"
"s_nop 1\n"
"v_permlane32_swap_b32 v220, v253\n"
"v_add_f32 v220, v220, v253\n"
"v_mov_b32 v253, v220\n"
"s_nop 1\n"
"v_permlane16_swap_b32 v220, v253\n"
"v_add_f32 v220, v220, v253\n"
"v_mov_b32 v253, v221\n"
"s_nop 1\n"
"v_permlane32_swap_b32 v221, v253\n"
"v_add_f32 v221, v221, v253\n"
"v_mov_b32 v253, v221\n"
"s_nop 1\n"
"v_permlane16_swap_b32 v221, v253\n"
"v_add_f32 v221, v221, v253\n"
"v_add_f32 v253, v216, v217\n"
"v_add_f32 v253, v253, v218\n"
"v_add_f32 v253, v253, v219\n"
"v_add_f32 v253, v253, v220\n"
"v_add_f32 v253, v253, v221\n"
"v_cmp_nge_f32 vcc, s70, v253\n"
"s_nop 4\n"
"s_cbranch_vccz .Lf_eaok\n"
"s_getpc_b64 s[80:81]\n"
"s_add_u32 s80, s80, 12\n"
"s_addc_u32 s81, s81, 0\n"
"s_branch .Lf_slow\n"
".Lf_eaok:\n"
"s_mov_b32 s54, 73728\n"
"v_rcp_f32 v192, v216\n"
"v_rcp_f32 v194, v217\n"
"v_rcp_f32 v196, v218\n"
"v_rcp_f32 v198, v219\n"
"v_rcp_f32 v200, v220\n"
"v_rcp_f32 v202, v221\n"
"v_accvgpr_read_b32 v96, a0\n"
"v_accvgpr_read_b32 v97, a1\n"
"v_accvgpr_read_b32 v98, a2\n"
"v_accvgpr_read_b32 v99, a3\n"
"v_accvgpr_read_b32 v100, a24\n"
"v_accvgpr_read_b32 v101, a25\n"
"v_accvgpr_read_b32 v102, a26\n"
"v_accvgpr_read_b32 v103, a27\n"
"v_pk_mul_f32 v[96:97], v[96:97], v[192:193] op_sel_hi:[1,0]\n"
"v_pk_mul_f32 v[98:99], v[98:99], v[192:193] op_sel_hi:[1,0]\n"
"v_pk_mul_f32 v[100:101], v[100:101], v[192:193] op_sel_hi:[1,0]\n"
"v_pk_mul_f32 v[102:103], v[102:103], v[192:193] op_sel_hi:[1,0]\n"
"v_cvt_pk_bf16_f32 v96, v96, v97\n"
"v_cvt_pk_bf16_f32 v97, v98, v99\n"
"v_cvt_pk_bf16_f32 v98, v100, v101\n"
"v_cvt_pk_bf16_f32 v99, v102, v103\n"
"s_add_u32 s75, s67, 0\n"
"buffer_store_dwordx4 v[96:99], v252, s[52:55], s75 offen offset:0 sc1\n"
"v_accvgpr_read_b32 v104, a4\n"
"v_accvgpr_read_b32 v105, a5\n"
"v_accvgpr_read_b32 v106, a6\n"
"v_accvgpr_read_b32 v107, a7\n"
"v_accvgpr_read_b32 v108, a28\n"
"v_accvgpr_read_b32 v109, a29\n"
"v_accvgpr_read_b32 v110, a30\n"
"v_accvgpr_read_b32 v111, a31\n"
"v_pk_mul_f32 v[104:105], v[104:105], v[194:195] op_sel_hi:[1,0]\n"
"v_pk_mul_f32 v[106:107], v[106:107], v[194:195] op_sel_hi:[1,0]\n"
"v_pk_mul_f32 v[108:109], v[108:109], v[194:195] op_sel_hi:[1,0]\n"
"v_pk_mul_f32 v[110:111], v[110:111], v[194:195] op_sel_hi:[1,0]\n"
"v_cvt_pk_bf16_f32 v104, v104, v105\n"
"v_cvt_pk_bf16_f32 v105, v106, v107\n"
"v_cvt_pk_bf16_f32 v106, v108, v109\n"
"v_cvt_pk_bf16_f32 v107, v110, v111\n"
"buffer_store_dwordx4 v[104:107], v252, s[52:55], s75 offen offset:1024 sc1\n"
"v_accvgpr_read_b32 v112, a8\n"
"v_accvgpr_read_b32 v113, a9\n"
"v_accvgpr_read_b32 v114, a10\n"
"v_accvgpr_read_b32 v115, a11\n"
"v_accvgpr_read_b32 v116, a32\n"
"v_accvgpr_read_b32 v117, a33\n"
"v_accvgpr_read_b32 v118, a34\n"
"v_accvgpr_read_b32 v119, a35\n"
"v_pk_mul_f32 v[112:113], v[112:113], v[196:197] op_sel_hi:[1,0]\n"
"v_pk_mul_f32 v[114:115], v[114:115], v[196:197] op_sel_hi:[1,0]\n"
"v_pk_mul_f32 v[116:117], v[116:117], v[196:197] op_sel_hi:[1,0]\n"
"v_pk_mul_f32 v[118:119], v[118:119], v[196:197] op_sel_hi:[1,0]\n"
"v_cvt_pk_bf16_f32 v112, v112, v113\n"
"v_cvt_pk_bf16_f32 v113, v114, v115\n"
"v_cvt_pk_bf16_f32 v114, v116, v117\n"
"v_cvt_pk_bf16_f32 v115, v118, v119\n"
"buffer_store_dwordx4 v[112:115], v252, s[52:55], s75 offen offset:2048 sc1\n"
"v_accvgpr_read_b32 v120, a12\n"
"v_accvgpr_read_b32 v121, a13\n"
"v_accvgpr_read_b32 v122, a14\n"
"v_accvgpr_read_b32 v123, a15\n"
"v_accvgpr_read_b32 v124, a36\n"
"v_accvgpr_read_b32 v125, a37\n"
"v_accvgpr_read_b32 v126, a38\n"
"v_accvgpr_read_b32 v127, a39\n"
"v_pk_mul_f32 v[120:121], v[120:121], v[198:199] op_sel_hi:[1,0]\n"
"v_pk_mul_f32 v[122:123], v[122:123], v[198:199] op_sel_hi:[1,0]\n"
"v_pk_mul_f32 v[124:125], v[124:125], v[198:199] op_sel_hi:[1,0]\n"
"v_pk_mul_f32 v[126:127], v[126:127], v[198:199] op_sel_hi:[1,0]\n"
"v_cvt_pk_bf16_f32 v120, v120, v121\n"
"v_cvt_pk_bf16_f32 v121, v122, v123\n"
"v_cvt_pk_bf16_f32 v122, v124, v125\n"
"v_cvt_pk_bf16_f32 v123, v126, v127\n"
"buffer_store_dwordx4 v[120:123], v252, s[52:55], s75 offen offset:3072 sc1\n"
"v_accvgpr_read_b32 v128, a16\n"
"v_accvgpr_read_b32 v129, a17\n"
"v_accvgpr_read_b32 v130, a18\n"
"v_accvgpr_read_b32 v131, a19\n"
"v_accvgpr_read_b32 v132, a40\n"
"v_accvgpr_read_b32 v133, a41\n"
"v_accvgpr_read_b32 v134, a42\n"
"v_accvgpr_read_b32 v135, a43\n"
"v_pk_mul_f32 v[128:129], v[128:129], v[200:201] op_sel_hi:[1,0]\n"
"v_pk_mul_f32 v[130:131], v[130:131], v[200:201] op_sel_hi:[1,0]\n"
"v_pk_mul_f32 v[132:133], v[132:133], v[200:201] op_sel_hi:[1,0]\n"
"v_pk_mul_f32 v[134:135], v[134:135], v[200:201] op_sel_hi:[1,0]\n"
"v_cvt_pk_bf16_f32 v128, v128, v129\n"
"v_cvt_pk_bf16_f32 v129, v130, v131\n"
"v_cvt_pk_bf16_f32 v130, v132, v133\n"
"v_cvt_pk_bf16_f32 v131, v134, v135\n"
"s_add_u32 s75, s67, 4096\n"
"buffer_store_dwordx4 v[128:131], v252, s[52:55], s75 offen offset:0 sc1\n"
"v_accvgpr_read_b32 v136, a20\n"
"v_accvgpr_read_b32 v137, a21\n"
"v_accvgpr_read_b32 v138, a22\n"
"v_accvgpr_read_b32 v139, a23\n"
"v_accvgpr_read_b32 v140, a44\n"
"v_accvgpr_read_b32 v141, a45\n"
"v_accvgpr_read_b32 v142, a46\n"
"v_accvgpr_read_b32 v143, a47\n"
"v_pk_mul_f32 v[136:137], v[136:137], v[202:203] op_sel_hi:[1,0]\n"
"v_pk_mul_f32 v[138:139], v[138:139], v[202:203] op_sel_hi:[1,0]\n"
"v_pk_mul_f32 v[140:141], v[140:141], v[202:203] op_sel_hi:[1,0]\n"
"v_pk_mul_f32 v[142:143], v[142:143], v[202:203] op_sel_hi:[1,0]\n"
"v_cvt_pk_bf16_f32 v136, v136, v137\n"
"v_cvt_pk_bf16_f32 v137, v138, v139\n"
"v_cvt_pk_bf16_f32 v138, v140, v141\n"
"v_cvt_pk_bf16_f32 v139, v142, v143\n"
"buffer_store_dwordx4 v[136:139], v252, s[52:55], s75 offen offset:1024 sc1\n"
"v_accvgpr_read_b32 v96, a48\n"
"v_accvgpr_read_b32 v97, a49\n"
"v_accvgpr_read_b32 v98, a50\n"
"v_accvgpr_read_b32 v99, a51\n"
"v_accvgpr_read_b32 v100, a72\n"
"v_accvgpr_read_b32 v101, a73\n"
"v_accvgpr_read_b32 v102, a74\n"
"v_accvgpr_read_b32 v103, a75\n"
"v_pk_mul_f32 v[96:97], v[96:97], v[192:193] op_sel_hi:[1,0]\n"
"v_pk_mul_f32 v[98:99], v[98:99], v[192:193] op_sel_hi:[1,0]\n"
"v_pk_mul_f32 v[100:101], v[100:101], v[192:193] op_sel_hi:[1,0]\n"
"v_pk_mul_f32 v[102:103], v[102:103], v[192:193] op_sel_hi:[1,0]\n"
"v_cvt_pk_bf16_f32 v96, v96, v97\n"
"v_cvt_pk_bf16_f32 v97, v98, v99\n"
"v_cvt_pk_bf16_f32 v98, v100, v101\n"
"v_cvt_pk_bf16_f32 v99, v102, v103\n"
"buffer_store_dwordx4 v[96:99], v252, s[52:55], s75 offen offset:2048 sc1\n"
"v_accvgpr_read_b32 v104, a52\n"
"v_accvgpr_read_b32 v105, a53\n"
"v_accvgpr_read_b32 v106, a54\n"
"v_accvgpr_read_b32 v107, a55\n"
"v_accvgpr_read_b32 v108, a76\n"
"v_accvgpr_read_b32 v109, a77\n"
"v_accvgpr_read_b32 v110, a78\n"
"v_accvgpr_read_b32 v111, a79\n"
"v_pk_mul_f32 v[104:105], v[104:105], v[194:195] op_sel_hi:[1,0]\n"
"v_pk_mul_f32 v[106:107], v[106:107], v[194:195] op_sel_hi:[1,0]\n"
"v_pk_mul_f32 v[108:109], v[108:109], v[194:195] op_sel_hi:[1,0]\n"
"v_pk_mul_f32 v[110:111], v[110:111], v[194:195] op_sel_hi:[1,0]\n"
"v_cvt_pk_bf16_f32 v104, v104, v105\n"
"v_cvt_pk_bf16_f32 v105, v106, v107\n"
"v_cvt_pk_bf16_f32 v106, v108, v109\n"
"v_cvt_pk_bf16_f32 v107, v110, v111\n"
"buffer_store_dwordx4 v[104:107], v252, s[52:55], s75 offen offset:3072 sc1\n"
"v_accvgpr_read_b32 v112, a56\n"
"v_accvgpr_read_b32 v113, a57\n"
"v_accvgpr_read_b32 v114, a58\n"
"v_accvgpr_read_b32 v115, a59\n"
"v_accvgpr_read_b32 v116, a80\n"
"v_accvgpr_read_b32 v117, a81\n"
"v_accvgpr_read_b32 v118, a82\n"
"v_accvgpr_read_b32 v119, a83\n"
"v_pk_mul_f32 v[112:113], v[112:113], v[196:197] op_sel_hi:[1,0]\n"
"v_pk_mul_f32 v[114:115], v[114:115], v[196:197] op_sel_hi:[1,0]\n"
"v_pk_mul_f32 v[116:117], v[116:117], v[196:197] op_sel_hi:[1,0]\n"
"v_pk_mul_f32 v[118:119], v[118:119], v[196:197] op_sel_hi:[1,0]\n"
"v_cvt_pk_bf16_f32 v112, v112, v113\n"
"v_cvt_pk_bf16_f32 v113, v114, v115\n"
"v_cvt_pk_bf16_f32 v114, v116, v117\n"
"v_cvt_pk_bf16_f32 v115, v118, v119\n"
"s_add_u32 s75, s67, 8192\n"
"buffer_store_dwordx4 v[112:115], v252, s[52:55], s75 offen offset:0 sc1\n"
"v_accvgpr_read_b32 v120, a60\n"
"v_accvgpr_read_b32 v121, a61\n"
"v_accvgpr_read_b32 v122, a62\n"
"v_accvgpr_read_b32 v123, a63\n"
"v_accvgpr_read_b32 v124, a84\n"
"v_accvgpr_read_b32 v125, a85\n"
"v_accvgpr_read_b32 v126, a86\n"
"v_accvgpr_read_b32 v127, a87\n"
"v_pk_mul_f32 v[120:121], v[120:121], v[198:199] op_sel_hi:[1,0]\n"
"v_pk_mul_f32 v[122:123], v[122:123], v[198:199] op_sel_hi:[1,0]\n"
"v_pk_mul_f32 v[124:125], v[124:125], v[198:199] op_sel_hi:[1,0]\n"
"v_pk_mul_f32 v[126:127], v[126:127], v[198:199] op_sel_hi:[1,0]\n"
"v_cvt_pk_bf16_f32 v120, v120, v121\n"
"v_cvt_pk_bf16_f32 v121, v122, v123\n"
"v_cvt_pk_bf16_f32 v122, v124, v125\n"
"v_cvt_pk_bf16_f32 v123, v126, v127\n"
"buffer_store_dwordx4 v[120:123], v252, s[52:55], s75 offen offset:1024 sc1\n"
"v_accvgpr_read_b32 v128, a64\n"
"v_accvgpr_read_b32 v129, a65\n"
"v_accvgpr_read_b32 v130, a66\n"
"v_accvgpr_read_b32 v131, a67\n"
"v_accvgpr_read_b32 v132, a88\n"
"v_accvgpr_read_b32 v133, a89\n"
"v_accvgpr_read_b32 v134, a90\n"
"v_accvgpr_read_b32 v135, a91\n"
"v_pk_mul_f32 v[128:129], v[128:129], v[200:201] op_sel_hi:[1,0]\n"
"v_pk_mul_f32 v[130:131], v[130:131], v[200:201] op_sel_hi:[1,0]\n"
"v_pk_mul_f32 v[132:133], v[132:133], v[200:201] op_sel_hi:[1,0]\n"
"v_pk_mul_f32 v[134:135], v[134:135], v[200:201] op_sel_hi:[1,0]\n"
"v_cvt_pk_bf16_f32 v128, v128, v129\n"
"v_cvt_pk_bf16_f32 v129, v130, v131\n"
"v_cvt_pk_bf16_f32 v130, v132, v133\n"
"v_cvt_pk_bf16_f32 v131, v134, v135\n"
"buffer_store_dwordx4 v[128:131], v252, s[52:55], s75 offen offset:2048 sc1\n"
"v_accvgpr_read_b32 v136, a68\n"
"v_accvgpr_read_b32 v137, a69\n"
"v_accvgpr_read_b32 v138, a70\n"
"v_accvgpr_read_b32 v139, a71\n"
"v_accvgpr_read_b32 v140, a92\n"
"v_accvgpr_read_b32 v141, a93\n"
"v_accvgpr_read_b32 v142, a94\n"
"v_accvgpr_read_b32 v143, a95\n"
"v_pk_mul_f32 v[136:137], v[136:137], v[202:203] op_sel_hi:[1,0]\n"
"v_pk_mul_f32 v[138:139], v[138:139], v[202:203] op_sel_hi:[1,0]\n"
"v_pk_mul_f32 v[140:141], v[140:141], v[202:203] op_sel_hi:[1,0]\n"
"v_pk_mul_f32 v[142:143], v[142:143], v[202:203] op_sel_hi:[1,0]\n"
"v_cvt_pk_bf16_f32 v136, v136, v137\n"
"v_cvt_pk_bf16_f32 v137, v138, v139\n"
"v_cvt_pk_bf16_f32 v138, v140, v141\n"
"v_cvt_pk_bf16_f32 v139, v142, v143\n"
"buffer_store_dwordx4 v[136:139], v252, s[52:55], s75 offen offset:3072 sc1\n"
"v_accvgpr_read_b32 v96, a96\n"
"v_accvgpr_read_b32 v97, a97\n"
"v_accvgpr_read_b32 v98, a98\n"
"v_accvgpr_read_b32 v99, a99\n"
"v_accvgpr_read_b32 v100, a120\n"
"v_accvgpr_read_b32 v101, a121\n"
"v_accvgpr_read_b32 v102, a122\n"
"v_accvgpr_read_b32 v103, a123\n"
"v_pk_mul_f32 v[96:97], v[96:97], v[192:193] op_sel_hi:[1,0]\n"
"v_pk_mul_f32 v[98:99], v[98:99], v[192:193] op_sel_hi:[1,0]\n"
"v_pk_mul_f32 v[100:101], v[100:101], v[192:193] op_sel_hi:[1,0]\n"
"v_pk_mul_f32 v[102:103], v[102:103], v[192:193] op_sel_hi:[1,0]\n"
"v_cvt_pk_bf16_f32 v96, v96, v97\n"
"v_cvt_pk_bf16_f32 v97, v98, v99\n"
"v_cvt_pk_bf16_f32 v98, v100, v101\n"
"v_cvt_pk_bf16_f32 v99, v102, v103\n"
"v_accvgpr_write_b32 a192, v96\n"
"v_accvgpr_write_b32 a193, v97\n"
"v_accvgpr_write_b32 a194, v98\n"
"v_accvgpr_write_b32 a195, v99\n"
"v_accvgpr_read_b32 v104, a100\n"
"v_accvgpr_read_b32 v105, a101\n"
"v_accvgpr_read_b32 v106, a102\n"
"v_accvgpr_read_b32 v107, a103\n"
"v_accvgpr_read_b32 v108, a124\n"
"v_accvgpr_read_b32 v109, a125\n"
"v_accvgpr_read_b32 v110, a126\n"
"v_accvgpr_read_b32 v111, a127\n"
"v_pk_mul_f32 v[104:105], v[104:105], v[194:195] op_sel_hi:[1,0]\n"
"v_pk_mul_f32 v[106:107], v[106:107], v[194:195] op_sel_hi:[1,0]\n"
"v_pk_mul_f32 v[108:109], v[108:109], v[194:195] op_sel_hi:[1,0]\n"
"v_pk_mul_f32 v[110:111], v[110:111], v[194:195] op_sel_hi:[1,0]\n"
"v_cvt_pk_bf16_f32 v104, v104, v105\n"
"v_cvt_pk_bf16_f32 v105, v106, v107\n"
"v_cvt_pk_bf16_f32 v106, v108, v109\n"
"v_cvt_pk_bf16_f32 v107, v110, v111\n"
"v_accvgpr_write_b32 a196, v104\n"
"v_accvgpr_write_b32 a197, v105\n"
"v_accvgpr_write_b32 a198, v106\n"
"v_accvgpr_write_b32 a199, v107\n"
"v_accvgpr_read_b32 v112, a104\n"
"v_accvgpr_read_b32 v113, a105\n"
"v_accvgpr_read_b32 v114, a106\n"
"v_accvgpr_read_b32 v115, a107\n"
"v_accvgpr_read_b32 v116, a128\n"
"v_accvgpr_read_b32 v117, a129\n"
"v_accvgpr_read_b32 v118, a130\n"
"v_accvgpr_read_b32 v119, a131\n"
"v_pk_mul_f32 v[112:113], v[112:113], v[196:197] op_sel_hi:[1,0]\n"
"v_pk_mul_f32 v[114:115], v[114:115], v[196:197] op_sel_hi:[1,0]\n"
"v_pk_mul_f32 v[116:117], v[116:117], v[196:197] op_sel_hi:[1,0]\n"
"v_pk_mul_f32 v[118:119], v[118:119], v[196:197] op_sel_hi:[1,0]\n"
"v_cvt_pk_bf16_f32 v112, v112, v113\n"
"v_cvt_pk_bf16_f32 v113, v114, v115\n"
"v_cvt_pk_bf16_f32 v114, v116, v117\n"
"v_cvt_pk_bf16_f32 v115, v118, v119\n"
"v_accvgpr_write_b32 a200, v112\n"
"v_accvgpr_write_b32 a201, v113\n"
"v_accvgpr_write_b32 a202, v114\n"
"v_accvgpr_write_b32 a203, v115\n"
"v_accvgpr_read_b32 v120, a108\n"
"v_accvgpr_read_b32 v121, a109\n"
"v_accvgpr_read_b32 v122, a110\n"
"v_accvgpr_read_b32 v123, a111\n"
"v_accvgpr_read_b32 v124, a132\n"
"v_accvgpr_read_b32 v125, a133\n"
"v_accvgpr_read_b32 v126, a134\n"
"v_accvgpr_read_b32 v127, a135\n"
"v_pk_mul_f32 v[120:121], v[120:121], v[198:199] op_sel_hi:[1,0]\n"
"v_pk_mul_f32 v[122:123], v[122:123], v[198:199] op_sel_hi:[1,0]\n"
"v_pk_mul_f32 v[124:125], v[124:125], v[198:199] op_sel_hi:[1,0]\n"
"v_pk_mul_f32 v[126:127], v[126:127], v[198:199] op_sel_hi:[1,0]\n"
"v_cvt_pk_bf16_f32 v120, v120, v121\n"
"v_cvt_pk_bf16_f32 v121, v122, v123\n"
"v_cvt_pk_bf16_f32 v122, v124, v125\n"
"v_cvt_pk_bf16_f32 v123, v126, v127\n"
"v_accvgpr_write_b32 a204, v120\n"
"v_accvgpr_write_b32 a205, v121\n"
"v_accvgpr_write_b32 a206, v122\n"
"v_accvgpr_write_b32 a207, v123\n"
"v_accvgpr_read_b32 v128, a112\n"
"v_accvgpr_read_b32 v129, a113\n"
"v_accvgpr_read_b32 v130, a114\n"
"v_accvgpr_read_b32 v131, a115\n"
"v_accvgpr_read_b32 v132, a136\n"
"v_accvgpr_read_b32 v133, a137\n"
"v_accvgpr_read_b32 v134, a138\n"
"v_accvgpr_read_b32 v135, a139\n"
"v_pk_mul_f32 v[128:129], v[128:129], v[200:201] op_sel_hi:[1,0]\n"
"v_pk_mul_f32 v[130:131], v[130:131], v[200:201] op_sel_hi:[1,0]\n"
"v_pk_mul_f32 v[132:133], v[132:133], v[200:201] op_sel_hi:[1,0]\n"
"v_pk_mul_f32 v[134:135], v[134:135], v[200:201] op_sel_hi:[1,0]\n"
"v_cvt_pk_bf16_f32 v128, v128, v129\n"
"v_cvt_pk_bf16_f32 v129, v130, v131\n"
"v_cvt_pk_bf16_f32 v130, v132, v133\n"
"v_cvt_pk_bf16_f32 v131, v134, v135\n"
"v_accvgpr_write_b32 a208, v128\n"
"v_accvgpr_write_b32 a209, v129\n"
"v_accvgpr_write_b32 a210, v130\n"
"v_accvgpr_write_b32 a211, v131\n"
"v_accvgpr_read_b32 v136, a116\n"
"v_accvgpr_read_b32 v137, a117\n"
"v_accvgpr_read_b32 v138, a118\n"
"v_accvgpr_read_b32 v139, a119\n"
"v_accvgpr_read_b32 v140, a140\n"
"v_accvgpr_read_b32 v141, a141\n"
"v_accvgpr_read_b32 v142, a142\n"
"v_accvgpr_read_b32 v143, a143\n"
"v_pk_mul_f32 v[136:137], v[136:137], v[202:203] op_sel_hi:[1,0]\n"
"v_pk_mul_f32 v[138:139], v[138:139], v[202:203] op_sel_hi:[1,0]\n"
"v_pk_mul_f32 v[140:141], v[140:141], v[202:203] op_sel_hi:[1,0]\n"
"v_pk_mul_f32 v[142:143], v[142:143], v[202:203] op_sel_hi:[1,0]\n"
"v_cvt_pk_bf16_f32 v136, v136, v137\n"
"v_cvt_pk_bf16_f32 v137, v138, v139\n"
"v_cvt_pk_bf16_f32 v138, v140, v141\n"
"v_cvt_pk_bf16_f32 v139, v142, v143\n"
"v_accvgpr_write_b32 a212, v136\n"
"v_accvgpr_write_b32 a213, v137\n"
"v_accvgpr_write_b32 a214, v138\n"
"v_accvgpr_write_b32 a215, v139\n"
"v_accvgpr_read_b32 v96, a144\n"
"v_accvgpr_read_b32 v97, a145\n"
"v_accvgpr_read_b32 v98, a146\n"
"v_accvgpr_read_b32 v99, a147\n"
"v_accvgpr_read_b32 v100, a168\n"
"v_accvgpr_read_b32 v101, a169\n"
"v_accvgpr_read_b32 v102, a170\n"
"v_accvgpr_read_b32 v103, a171\n"
"v_pk_mul_f32 v[96:97], v[96:97], v[192:193] op_sel_hi:[1,0]\n"
"v_pk_mul_f32 v[98:99], v[98:99], v[192:193] op_sel_hi:[1,0]\n"
"v_pk_mul_f32 v[100:101], v[100:101], v[192:193] op_sel_hi:[1,0]\n"
"v_pk_mul_f32 v[102:103], v[102:103], v[192:193] op_sel_hi:[1,0]\n"
"v_cvt_pk_bf16_f32 v96, v96, v97\n"
"v_cvt_pk_bf16_f32 v97, v98, v99\n"
"v_cvt_pk_bf16_f32 v98, v100, v101\n"
"v_cvt_pk_bf16_f32 v99, v102, v103\n"
"v_accvgpr_write_b32 a216, v96\n"
"v_accvgpr_write_b32 a217, v97\n"
"v_accvgpr_write_b32 a218, v98\n"
"v_accvgpr_write_b32 a219, v99\n"
"v_accvgpr_read_b32 v104, a148\n"
"v_accvgpr_read_b32 v105, a149\n"
"v_accvgpr_read_b32 v106, a150\n"
"v_accvgpr_read_b32 v107, a151\n"
"v_accvgpr_read_b32 v108, a172\n"
"v_accvgpr_read_b32 v109, a173\n"
"v_accvgpr_read_b32 v110, a174\n"
"v_accvgpr_read_b32 v111, a175\n"
"v_pk_mul_f32 v[104:105], v[104:105], v[194:195] op_sel_hi:[1,0]\n"
"v_pk_mul_f32 v[106:107], v[106:107], v[194:195] op_sel_hi:[1,0]\n"
"v_pk_mul_f32 v[108:109], v[108:109], v[194:195] op_sel_hi:[1,0]\n"
"v_pk_mul_f32 v[110:111], v[110:111], v[194:195] op_sel_hi:[1,0]\n"
"v_cvt_pk_bf16_f32 v104, v104, v105\n"
"v_cvt_pk_bf16_f32 v105, v106, v107\n"
"v_cvt_pk_bf16_f32 v106, v108, v109\n"
"v_cvt_pk_bf16_f32 v107, v110, v111\n"
"v_accvgpr_write_b32 a220, v104\n"
"v_accvgpr_write_b32 a221, v105\n"
"v_accvgpr_write_b32 a222, v106\n"
"v_accvgpr_write_b32 a223, v107\n"
"v_accvgpr_read_b32 v112, a152\n"
"v_accvgpr_read_b32 v113, a153\n"
"v_accvgpr_read_b32 v114, a154\n"
"v_accvgpr_read_b32 v115, a155\n"
"v_accvgpr_read_b32 v116, a176\n"
"v_accvgpr_read_b32 v117, a177\n"
"v_accvgpr_read_b32 v118, a178\n"
"v_accvgpr_read_b32 v119, a179\n"
"v_pk_mul_f32 v[112:113], v[112:113], v[196:197] op_sel_hi:[1,0]\n"
"v_pk_mul_f32 v[114:115], v[114:115], v[196:197] op_sel_hi:[1,0]\n"
"v_pk_mul_f32 v[116:117], v[116:117], v[196:197] op_sel_hi:[1,0]\n"
"v_pk_mul_f32 v[118:119], v[118:119], v[196:197] op_sel_hi:[1,0]\n"
"v_cvt_pk_bf16_f32 v112, v112, v113\n"
"v_cvt_pk_bf16_f32 v113, v114, v115\n"
"v_cvt_pk_bf16_f32 v114, v116, v117\n"
"v_cvt_pk_bf16_f32 v115, v118, v119\n"
"v_accvgpr_write_b32 a224, v112\n"
"v_accvgpr_write_b32 a225, v113\n"
"v_accvgpr_write_b32 a226, v114\n"
"v_accvgpr_write_b32 a227, v115\n"
"v_accvgpr_read_b32 v120, a156\n"
"v_accvgpr_read_b32 v121, a157\n"
"v_accvgpr_read_b32 v122, a158\n"
"v_accvgpr_read_b32 v123, a159\n"
"v_accvgpr_read_b32 v124, a180\n"
"v_accvgpr_read_b32 v125, a181\n"
"v_accvgpr_read_b32 v126, a182\n"
"v_accvgpr_read_b32 v127, a183\n"
"v_pk_mul_f32 v[120:121], v[120:121], v[198:199] op_sel_hi:[1,0]\n"
"v_pk_mul_f32 v[122:123], v[122:123], v[198:199] op_sel_hi:[1,0]\n"
"v_pk_mul_f32 v[124:125], v[124:125], v[198:199] op_sel_hi:[1,0]\n"
"v_pk_mul_f32 v[126:127], v[126:127], v[198:199] op_sel_hi:[1,0]\n"
"v_cvt_pk_bf16_f32 v120, v120, v121\n"
"v_cvt_pk_bf16_f32 v121, v122, v123\n"
"v_cvt_pk_bf16_f32 v122, v124, v125\n"
"v_cvt_pk_bf16_f32 v123, v126, v127\n"
"v_accvgpr_write_b32 a228, v120\n"
"v_accvgpr_write_b32 a229, v121\n"
"v_accvgpr_write_b32 a230, v122\n"
"v_accvgpr_write_b32 a231, v123\n"
"v_accvgpr_read_b32 v128, a160\n"
"v_accvgpr_read_b32 v129, a161\n"
"v_accvgpr_read_b32 v130, a162\n"
"v_accvgpr_read_b32 v131, a163\n"
"v_accvgpr_read_b32 v132, a184\n"
"v_accvgpr_read_b32 v133, a185\n"
"v_accvgpr_read_b32 v134, a186\n"
"v_accvgpr_read_b32 v135, a187\n"
"v_pk_mul_f32 v[128:129], v[128:129], v[200:201] op_sel_hi:[1,0]\n"
"v_pk_mul_f32 v[130:131], v[130:131], v[200:201] op_sel_hi:[1,0]\n"
"v_pk_mul_f32 v[132:133], v[132:133], v[200:201] op_sel_hi:[1,0]\n"
"v_pk_mul_f32 v[134:135], v[134:135], v[200:201] op_sel_hi:[1,0]\n"
"v_cvt_pk_bf16_f32 v128, v128, v129\n"
"v_cvt_pk_bf16_f32 v129, v130, v131\n"
"v_cvt_pk_bf16_f32 v130, v132, v133\n"
"v_cvt_pk_bf16_f32 v131, v134, v135\n"
"v_accvgpr_write_b32 a232, v128\n"
"v_accvgpr_write_b32 a233, v129\n"
"v_accvgpr_write_b32 a234, v130\n"
"v_accvgpr_write_b32 a235, v131\n"
"v_accvgpr_read_b32 v136, a164\n"
"v_accvgpr_read_b32 v137, a165\n"
"v_accvgpr_read_b32 v138, a166\n"
"v_accvgpr_read_b32 v139, a167\n"
"v_accvgpr_read_b32 v140, a188\n"
"v_accvgpr_read_b32 v141, a189\n"
"v_accvgpr_read_b32 v142, a190\n"
"v_accvgpr_read_b32 v143, a191\n"
"v_pk_mul_f32 v[136:137], v[136:137], v[202:203] op_sel_hi:[1,0]\n"
"v_pk_mul_f32 v[138:139], v[138:139], v[202:203] op_sel_hi:[1,0]\n"
"v_pk_mul_f32 v[140:141], v[140:141], v[202:203] op_sel_hi:[1,0]\n"
"v_pk_mul_f32 v[142:143], v[142:143], v[202:203] op_sel_hi:[1,0]\n"
"v_cvt_pk_bf16_f32 v136, v136, v137\n"
"v_cvt_pk_bf16_f32 v137, v138, v139\n"
"v_cvt_pk_bf16_f32 v138, v140, v141\n"
"v_cvt_pk_bf16_f32 v139, v142, v143\n"
"v_accvgpr_write_b32 a236, v136\n"
"v_accvgpr_write_b32 a237, v137\n"
"v_accvgpr_write_b32 a238, v138\n"
"v_accvgpr_write_b32 a239, v139\n"
"s_mov_b32 s66, 1\n"
"s_branch .Lf_round\n"
".Lf_last:\n"
"s_waitcnt vmcnt(0) lgkmcnt(0)\n"
"s_barrier\n"
"v_mfma_f32_16x16x32_bf16 v[168:171], v[96:99], v[16:19], v[196:199]\n"
"s_add_u32 s36, s36, 0x20000\n"
"s_addc_u32 s37, s37, 0\n"
"v_exp_f32 v165, v165\n"
"v_mfma_f32_16x16x32_bf16 v[168:171], v[100:103], v[20:23], v[168:171]\n"
"s_add_u32 s40, s40, 0x20000\n"
"s_addc_u32 s41, s41, 0\n"
"v_add_f32 v216, v216, v164\n"
"v_cvt_pk_bf16_f32 v185, v162, v163\n"
"v_mfma_f32_16x16x32_bf16 v[168:171], v[104:107], v[24:27], v[168:171]\n"
"s_mov_b64 s[48:49], %12\n"
"s_mov_b32 s50, %18\n"
"v_exp_f32 v166, v166\n"
"v_mfma_f32_16x16x32_bf16 v[168:171], v[108:111], v[28:31], v[168:171]\n"
"s_mov_b32 s51, 0x20000\n"
"s_mov_b64 s[44:45], %13\n"
"v_add_f32 v216, v216, v165\n"
"v_cvt_pk_bf16_f32 v186, v164, v165\n"
"v_mfma_f32_16x16x32_bf16 v[172:175], v[112:115], v[16:19], v[196:199]\n"
"s_mov_b32 s46, 196608\n"
"s_mov_b32 s47, 0x20000\n"
"v_exp_f32 v167, v167\n"
"v_mfma_f32_16x16x32_bf16 v[172:175], v[116:119], v[20:23], v[172:175]\n"
"s_and_b32 s73, %5, 1\n"
"s_mul_i32 s64, s73, 55296\n"
"v_cvt_pk_bf16_f32 v187, v166, v167\n"
"v_add_f32 v216, v216, v166\n"
"v_mfma_f32_16x16x32_bf16 v[172:175], v[120:123], v[24:27], v[172:175]\n"
"s_add_u32 s64, s64, %7\n"
"s_lshr_b32 s73, %5, 1\n"
"v_exp_f32 v168, v168\n"
"v_mfma_f32_16x16x32_bf16 v[172:175], v[124:127], v[28:31], v[172:175]\n"
"ds_read_b128 a[192:195], v222 offset:32768\n"
"s_mul_i32 s60, s73, 9216\n"
"s_add_u32 s60, s60, s64\n"
"v_add_f32 v216, v216, v167\n"
"v_mfma_f32_16x16x32_bf16 a[0:3], v[128:131], v[184:187], a[0:3]\n"
"v_mbcnt_lo_u32_b32 v252, -1, 0\n"
"v_mbcnt_hi_u32_b32 v252, -1, v252\n"
"v_exp_f32 v169, v169\n"
"v_mfma_f32_16x16x32_bf16 a[24:27], v[132:135], v[184:187], a[24:27]\n"
"ds_read_b128 a[196:199], v223 offset:32768\n"
"v_lshlrev_b32 v252, 7, v252\n"
"v_add_f32 v217, v217, v168\n"
"v_mfma_f32_16x16x32_bf16 a[48:51], v[136:139], v[184:187], a[48:51]\n"
"global_load_dword v255, v252, %17\n"
"v_exp_f32 v170, v170\n"
"v_mfma_f32_16x16x32_bf16 a[72:75], v[140:143], v[184:187], a[72:75]\n"
"v_add_f32 v217, v217, v169\n"
"v_cvt_pk_bf16_f32 v188, v168, v169\n"
"v_mfma_f32_16x16x32_bf16 a[96:99], v[144:147], v[184:187], a[96:99]\n"
"v_exp_f32 v171, v171\n"
"v_mfma_f32_16x16x32_bf16 a[120:123], v[148:151], v[184:187], a[120:123]\n"
"ds_read_b64_tr_b16 a[224:225], v226 offset:32768\n"
"v_add_u32 v252, 8192, v252\n"
"v_add_f32 v217, v217, v170\n"
"v_mfma_f32_16x16x32_bf16 a[144:147], v[152:155], v[184:187], a[144:147]\n"
"global_load_dword v255, v252, %17\n"
"v_exp_f32 v172, v172\n"
"v_mfma_f32_16x16x32_bf16 a[168:171], v[156:159], v[184:187], a[168:171]\n"
"ds_read_b64_tr_b16 a[226:227], v226 offset:33792\n"
"v_add_f32 v217, v217, v171\n"
"v_mfma_f32_16x16x32_bf16 v[176:179], v[96:99], v[32:35], v[200:203]\n"
"v_exp_f32 v173, v173\n"
"v_mfma_f32_16x16x32_bf16 v[176:179], v[100:103], v[36:39], v[176:179]\n"
"v_add_u32 v252, 2048, v252\n"
"v_add_f32 v217, v217, v172\n"
"v_cvt_pk_bf16_f32 v189, v170, v171\n"
"v_mfma_f32_16x16x32_bf16 v[176:179], v[104:107], v[40:43], v[176:179]\n"
"global_load_dword v255, v252, %17\n"
"v_exp_f32 v174, v174\n"
"v_mfma_f32_16x16x32_bf16 v[176:179], v[108:111], v[44:47], v[176:179]\n"
"v_mbcnt_lo_u32_b32 v252, -1, 0\n"
"v_mbcnt_hi_u32_b32 v252, -1, v252\n"
"v_add_f32 v217, v217, v173\n"
"v_cvt_pk_bf16_f32 v190, v172, v173\n"
"v_mfma_f32_16x16x32_bf16 v[180:183], v[112:115], v[32:35], v[200:203]\n"
"s_mov_b32 s75, 3641\n"
"v_add_u32 v253, 0, v252\n"
"v_exp_f32 v175, v175\n"
"v_mfma_f32_16x16x32_bf16 v[180:183], v[116:119], v[36:39], v[180:183]\n"
"v_mul_u32_u24 v238, s75, v253\n"
"v_lshrrev_b32 v238, 16, v238\n"
"v_cvt_pk_bf16_f32 v191, v174, v175\n"
"v_add_f32 v217, v217, v174\n"
"v_mfma_f32_16x16x32_bf16 v[180:183], v[120:123], v[40:43], v[180:183]\n"
"v_mul_u32_u24 v240, 18, v238\n"
"v_sub_u32 v253, v253, v240\n"
"v_exp_f32 v176, v176\n"
"v_mfma_f32_16x16x32_bf16 v[180:183], v[124:127], v[44:47], v[180:183]\n"
"ds_read_b128 a[200:203], v224 offset:32768\n"
"v_mul_u32_u24 v238, 1152, v238\n"
"v_lshl_add_u32 v238, v253, 4, v238\n"
"v_add_f32 v217, v217, v175\n"
"v_mfma_f32_16x16x32_bf16 a[4:7], v[128:131], v[188:191], a[4:7]\n"
"v_subrev_u32 v240, 0, v238\n"
"v_add_u32 v253, 64, v252\n"
"v_exp_f32 v177, v177\n"
"v_mfma_f32_16x16x32_bf16 a[28:31], v[132:135], v[188:191], a[28:31]\n"
"ds_read_b128 a[204:207], v225 offset:32768\n"
"v_mul_u32_u24 v238, s75, v253\n"
"v_lshrrev_b32 v238, 16, v238\n"
"v_add_f32 v218, v218, v176\n"
"v_mfma_f32_16x16x32_bf16 a[52:55], v[136:139], v[188:191], a[52:55]\n"
"v_mul_u32_u24 v241, 18, v238\n"
"v_sub_u32 v253, v253, v241\n"
"v_exp_f32 v178, v178\n"
"v_mfma_f32_16x16x32_bf16 a[76:79], v[140:143], v[188:191], a[76:79]\n"
"v_mul_u32_u24 v238, 1152, v238\n"
"v_lshl_add_u32 v238, v253, 4, v238\n"
"v_add_f32 v218, v218, v177\n"
"v_cvt_pk_bf16_f32 v184, v176, v177\n"
"v_mfma_f32_16x16x32_bf16 a[100:103], v[144:147], v[188:191], a[100:103]\n"
"v_subrev_u32 v241, 1024, v238\n"
"v_add_u32 v253, 128, v252\n"
"v_exp_f32 v179, v179\n"
"v_mfma_f32_16x16x32_bf16 a[124:127], v[148:151], v[188:191], a[124:127]\n"
"ds_read_b64_tr_b16 a[228:229], v227 offset:32768\n"
"v_mul_u32_u24 v238, s75, v253\n"
"v_lshrrev_b32 v238, 16, v238\n"
"v_add_f32 v218, v218, v178\n"
"v_mfma_f32_16x16x32_bf16 a[148:151], v[152:155], v[188:191], a[148:151]\n"
"v_mul_u32_u24 v242, 18, v238\n"
"v_sub_u32 v253, v253, v242\n"
"v_exp_f32 v180, v180\n"
"v_mfma_f32_16x16x32_bf16 a[172:175], v[156:159], v[188:191], a[172:175]\n"
"ds_read_b64_tr_b16 a[230:231], v227 offset:33792\n"
"v_mul_u32_u24 v238, 1152, v238\n"
"v_lshl_add_u32 v238, v253, 4, v238\n"
"v_add_f32 v218, v218, v179\n"
"v_mfma_f32_16x16x32_bf16 v[160:163], v[96:99], v[48:51], v[204:207]\n"
"v_subrev_u32 v242, 2048, v238\n"
"v_add_u32 v253, 192, v252\n"
"v_exp_f32 v181, v181\n"
"v_mfma_f32_16x16x32_bf16 v[160:163], v[100:103], v[52:55], v[160:163]\n"
"v_mul_u32_u24 v238, s75, v253\n"
"v_lshrrev_b32 v238, 16, v238\n"
"v_add_f32 v218, v218, v180\n"
"v_cvt_pk_bf16_f32 v185, v178, v179\n"
"v_mfma_f32_16x16x32_bf16 v[160:163], v[104:107], v[56:59], v[160:163]\n"
"v_mul_u32_u24 v243, 18, v238\n"
"v_sub_u32 v253, v253, v243\n"
"v_exp_f32 v182, v182\n"
"v_mfma_f32_16x16x32_bf16 v[160:163], v[108:111], v[60:63], v[160:163]\n"
"v_mul_u32_u24 v238, 1152, v238\n"
"v_lshl_add_u32 v238, v253, 4, v238\n"
"v_add_f32 v218, v218, v181\n"
"v_cvt_pk_bf16_f32 v186, v180, v181\n"
"v_mfma_f32_16x16x32_bf16 v[164:167], v[112:115], v[48:51], v[204:207]\n"
"v_subrev_u32 v243, 3072, v238\n"
"v_add_u32 v253, 256, v252\n"
"v_exp_f32 v183, v183\n"
"v_mfma_f32_16x16x32_bf16 v[164:167], v[116:119], v[52:55], v[164:167]\n"
"v_mul_u32_u24 v238, s75, v253\n"
"v_lshrrev_b32 v238, 16, v238\n"
"v_cvt_pk_bf16_f32 v187, v182, v183\n"
"v_add_f32 v218, v218, v182\n"
"v_mfma_f32_16x16x32_bf16 v[164:167], v[120:123], v[56:59], v[164:167]\n"
"v_mul_u32_u24 v244, 18, v238\n"
"v_sub_u32 v253, v253, v244\n"
"v_exp_f32 v160, v160\n"
"v_mfma_f32_16x16x32_bf16 v[164:167], v[124:127], v[60:63], v[164:167]\n"
"ds_read_b128 a[208:211], v222 offset:33792\n"
"v_mul_u32_u24 v238, 1152, v238\n"
"v_lshl_add_u32 v238, v253, 4, v238\n"
"v_add_f32 v218, v218, v183\n"
"v_mfma_f32_16x16x32_bf16 a[8:11], v[128:131], v[184:187], a[8:11]\n"
"v_subrev_u32 v244, 0, v238\n"
"v_add_u32 v253, 320, v252\n"
"v_exp_f32 v161, v161\n"
"v_mfma_f32_16x16x32_bf16 a[32:35], v[132:135], v[184:187], a[32:35]\n"
"ds_read_b128 a[212:215], v223 offset:33792\n"
"v_mul_u32_u24 v238, s75, v253\n"
"v_lshrrev_b32 v238, 16, v238\n"
"v_add_f32 v219, v219, v160\n"
"v_mfma_f32_16x16x32_bf16 a[56:59], v[136:139], v[184:187], a[56:59]\n"
"v_mul_u32_u24 v245, 18, v238\n"
"v_sub_u32 v253, v253, v245\n"
"v_exp_f32 v162, v162\n"
"v_mfma_f32_16x16x32_bf16 a[80:83], v[140:143], v[184:187], a[80:83]\n"
"v_mul_u32_u24 v238, 1152, v238\n"
"v_lshl_add_u32 v238, v253, 4, v238\n"
"v_add_f32 v219, v219, v161\n"
"v_cvt_pk_bf16_f32 v188, v160, v161\n"
"v_mfma_f32_16x16x32_bf16 a[104:107], v[144:147], v[184:187], a[104:107]\n"
"v_subrev_u32 v245, 1024, v238\n"
"v_add_u32 v253, 384, v252\n"
"v_exp_f32 v163, v163\n"
"v_mfma_f32_16x16x32_bf16 a[128:131], v[148:151], v[184:187], a[128:131]\n"
"ds_read_b64_tr_b16 a[232:233], v228 offset:32768\n"
"v_mul_u32_u24 v238, s75, v253\n"
"v_lshrrev_b32 v238, 16, v238\n"
"v_add_f32 v219, v219, v162\n"
"v_mfma_f32_16x16x32_bf16 a[152:155], v[152:155], v[184:187], a[152:155]\n"
"v_mul_u32_u24 v246, 18, v238\n"
"v_sub_u32 v253, v253, v246\n"
"v_exp_f32 v164, v164\n"
"v_mfma_f32_16x16x32_bf16 a[176:179], v[156:159], v[184:187], a[176:179]\n"
"ds_read_b64_tr_b16 a[234:235], v228 offset:33792\n"
"v_mul_u32_u24 v238, 1152, v238\n"
"v_lshl_add_u32 v238, v253, 4, v238\n"
"v_add_f32 v219, v219, v163\n"
"v_mfma_f32_16x16x32_bf16 v[168:171], v[96:99], v[64:67], v[208:211]\n"
"v_subrev_u32 v246, 2048, v238\n"
"v_add_u32 v253, 448, v252\n"
"v_exp_f32 v165, v165\n"
"v_mfma_f32_16x16x32_bf16 v[168:171], v[100:103], v[68:71], v[168:171]\n"
"v_mul_u32_u24 v238, s75, v253\n"
"v_lshrrev_b32 v238, 16, v238\n"
"v_add_f32 v219, v219, v164\n"
"v_cvt_pk_bf16_f32 v189, v162, v163\n"
"v_mfma_f32_16x16x32_bf16 v[168:171], v[104:107], v[72:75], v[168:171]\n"
"v_mul_u32_u24 v247, 18, v238\n"
"v_sub_u32 v253, v253, v247\n"
"v_exp_f32 v166, v166\n"
"v_mfma_f32_16x16x32_bf16 v[168:171], v[108:111], v[76:79], v[168:171]\n"
"v_mul_u32_u24 v238, 1152, v238\n"
"v_lshl_add_u32 v238, v253, 4, v238\n"
"v_add_f32 v219, v219, v165\n"
"v_cvt_pk_bf16_f32 v190, v164, v165\n"
"v_mfma_f32_16x16x32_bf16 v[172:175], v[112:115], v[64:67], v[208:211]\n"
"v_subrev_u32 v247, 3072, v238\n"
"v_add_u32 v253, 512, v252\n"
"v_exp_f32 v167, v167\n"
"v_mfma_f32_16x16x32_bf16 v[172:175], v[116:119], v[68:71], v[172:175]\n"
"v_mul_u32_u24 v238, s75, v253\n"
"v_lshrrev_b32 v238, 16, v238\n"
"v_cvt_pk_bf16_f32 v191, v166, v167\n"
"v_add_f32 v219, v219, v166\n"
"v_mfma_f32_16x16x32_bf16 v[172:175], v[120:123], v[72:75], v[172:175]\n"
"v_mul_u32_u24 v248, 18, v238\n"
"v_sub_u32 v253, v253, v248\n"
"v_exp_f32 v168, v168\n"
"v_mfma_f32_16x16x32_bf16 v[172:175], v[124:127], v[76:79], v[172:175]\n"
"ds_read_b128 a[216:219], v224 offset:33792\n"
"v_mul_u32_u24 v238, 1152, v238\n"
"v_lshl_add_u32 v238, v253, 4, v238\n"
"v_add_f32 v219, v219, v167\n"
"v_mfma_f32_16x16x32_bf16 a[12:15], v[128:131], v[188:191], a[12:15]\n"
"v_subrev_u32 v248, 0, v238\n"
"v_exp_f32 v169, v169\n"
"v_mfma_f32_16x16x32_bf16 a[36:39], v[132:135], v[188:191], a[36:39]\n"
"ds_read_b128 a[220:223], v225 offset:33792\n"
"v_add_f32 v220, v220, v168\n"
"v_mfma_f32_16x16x32_bf16 a[60:63], v[136:139], v[188:191], a[60:63]\n"
"v_exp_f32 v170, v170\n"
"v_mfma_f32_16x16x32_bf16 a[84:87], v[140:143], v[188:191], a[84:87]\n"
"v_add_f32 v220, v220, v169\n"
"v_cvt_pk_bf16_f32 v184, v168, v169\n"
"v_mfma_f32_16x16x32_bf16 a[108:111], v[144:147], v[188:191], a[108:111]\n"
"v_exp_f32 v171, v171\n"
"v_mfma_f32_16x16x32_bf16 a[132:135], v[148:151], v[188:191], a[132:135]\n"
"ds_read_b64_tr_b16 a[236:237], v229 offset:32768\n"
"v_add_f32 v220, v220, v170\n"
"v_mfma_f32_16x16x32_bf16 a[156:159], v[152:155], v[188:191], a[156:159]\n"
"v_exp_f32 v172, v172\n"
"v_mfma_f32_16x16x32_bf16 a[180:183], v[156:159], v[188:191], a[180:183]\n"
"ds_read_b64_tr_b16 a[238:239], v229 offset:33792\n"
"v_add_f32 v220, v220, v171\n"
"v_mfma_f32_16x16x32_bf16 v[176:179], v[96:99], v[80:83], v[212:215]\n"
"v_exp_f32 v173, v173\n"
"v_mfma_f32_16x16x32_bf16 v[176:179], v[100:103], v[84:87], v[176:179]\n"
"v_add_f32 v220, v220, v172\n"
"v_cvt_pk_bf16_f32 v185, v170, v171\n"
"v_mfma_f32_16x16x32_bf16 v[176:179], v[104:107], v[88:91], v[176:179]\n"
"v_exp_f32 v174, v174\n"
"v_mfma_f32_16x16x32_bf16 v[176:179], v[108:111], v[92:95], v[176:179]\n"
"v_add_f32 v220, v220, v173\n"
"v_cvt_pk_bf16_f32 v186, v172, v173\n"
"v_mfma_f32_16x16x32_bf16 v[180:183], v[112:115], v[80:83], v[212:215]\n"
"v_exp_f32 v175, v175\n"
"v_mfma_f32_16x16x32_bf16 v[180:183], v[116:119], v[84:87], v[180:183]\n"
"v_cvt_pk_bf16_f32 v187, v174, v175\n"
"v_add_f32 v220, v220, v174\n"
"v_mfma_f32_16x16x32_bf16 v[180:183], v[120:123], v[88:91], v[180:183]\n"
"v_exp_f32 v176, v176\n"
"v_mfma_f32_16x16x32_bf16 v[180:183], v[124:127], v[92:95], v[180:183]\n"
"ds_read_b64_tr_b16 a[240:241], v230 offset:32768\n"
"v_add_f32 v220, v220, v175\n"
"v_mfma_f32_16x16x32_bf16 a[16:19], v[128:131], v[184:187], a[16:19]\n"
"v_exp_f32 v177, v177\n"
"v_mfma_f32_16x16x32_bf16 a[40:43], v[132:135], v[184:187], a[40:43]\n"
"ds_read_b64_tr_b16 a[242:243], v230 offset:33792\n"
"v_add_f32 v221, v221, v176\n"
"v_mfma_f32_16x16x32_bf16 a[64:67], v[136:139], v[184:187], a[64:67]\n"
"v_exp_f32 v178, v178\n"
"v_mfma_f32_16x16x32_bf16 a[88:91], v[140:143], v[184:187], a[88:91]\n"
"v_add_f32 v221, v221, v177\n"
"v_cvt_pk_bf16_f32 v188, v176, v177\n"
"v_mfma_f32_16x16x32_bf16 a[112:115], v[144:147], v[184:187], a[112:115]\n"
"v_exp_f32 v179, v179\n"
"v_mfma_f32_16x16x32_bf16 a[136:139], v[148:151], v[184:187], a[136:139]\n"
"ds_read_b64_tr_b16 a[244:245], v231 offset:32768\n"
"v_add_f32 v221, v221, v178\n"
"v_mfma_f32_16x16x32_bf16 a[160:163], v[152:155], v[184:187], a[160:163]\n"
"v_exp_f32 v180, v180\n"
"v_mfma_f32_16x16x32_bf16 a[184:187], v[156:159], v[184:187], a[184:187]\n"
"ds_read_b64_tr_b16 a[246:247], v231 offset:33792\n"
"v_add_f32 v221, v221, v179\n"
"s_waitcnt lgkmcnt(8)\n"
"v_mfma_f32_16x16x32_bf16 v[160:163], a[192:195], v[0:3], v[192:195]\n"
"v_exp_f32 v181, v181\n"
"v_mfma_f32_16x16x32_bf16 v[160:163], a[196:199], v[4:7], v[160:163]\n"
"v_add_f32 v221, v221, v180\n"
"v_cvt_pk_bf16_f32 v189, v178, v179\n"
"v_mfma_f32_16x16x32_bf16 v[160:163], a[200:203], v[8:11], v[160:163]\n"
"v_exp_f32 v182, v182\n"
"v_mfma_f32_16x16x32_bf16 v[160:163], a[204:207], v[12:15], v[160:163]\n"
"ds_read_b64_tr_b16 a[248:249], v232 offset:32768\n"
"v_add_f32 v221, v221, v181\n"
"v_cvt_pk_bf16_f32 v190, v180, v181\n"
"v_mfma_f32_16x16x32_bf16 v[164:167], a[208:211], v[0:3], v[192:195]\n"
"v_exp_f32 v183, v183\n"
"v_mfma_f32_16x16x32_bf16 v[164:167], a[212:215], v[4:7], v[164:167]\n"
"ds_read_b64_tr_b16 a[250:251], v232 offset:33792\n"
"v_cvt_pk_bf16_f32 v191, v182, v183\n"
"v_add_f32 v221, v221, v182\n"
"s_waitcnt lgkmcnt(9)\n"
"v_mfma_f32_16x16x32_bf16 v[164:167], a[216:219], v[8:11], v[164:167]\n"
"v_exp_f32 v160, v160\n"
"s_waitcnt lgkmcnt(8)\n"
"v_mfma_f32_16x16x32_bf16 v[164:167], a[220:223], v[12:15], v[164:167]\n"
"ds_read_b64_tr_b16 a[252:253], v233 offset:32768\n"
"v_add_f32 v221, v221, v183\n"
"v_mfma_f32_16x16x32_bf16 a[20:23], v[128:131], v[188:191], a[20:23]\n"
"v_exp_f32 v161, v161\n"
"v_mfma_f32_16x16x32_bf16 a[44:47], v[132:135], v[188:191], a[44:47]\n"
"ds_read_b64_tr_b16 a[254:255], v233 offset:33792\n"
"v_add_f32 v216, v216, v160\n"
"v_mfma_f32_16x16x32_bf16 a[68:71], v[136:139], v[188:191], a[68:71]\n"
"v_exp_f32 v162, v162\n"
"v_mfma_f32_16x16x32_bf16 a[92:95], v[140:143], v[188:191], a[92:95]\n"
"v_add_f32 v216, v216, v161\n"
"v_cvt_pk_bf16_f32 v184, v160, v161\n"
"v_mfma_f32_16x16x32_bf16 a[116:119], v[144:147], v[188:191], a[116:119]\n"
"v_exp_f32 v163, v163\n"
"v_mfma_f32_16x16x32_bf16 a[140:143], v[148:151], v[188:191], a[140:143]\n"
"v_add_f32 v216, v216, v162\n"
"v_mfma_f32_16x16x32_bf16 a[164:167], v[152:155], v[188:191], a[164:167]\n"
"v_exp_f32 v164, v164\n"
"v_mfma_f32_16x16x32_bf16 a[188:191], v[156:159], v[188:191], a[188:191]\n"
"v_add_f32 v216, v216, v163\n"
"s_waitcnt lgkmcnt(0)\n"
"v_mfma_f32_16x16x32_bf16 v[168:171], a[192:195], v[16:19], v[196:199]\n"
"v_mov_b32 v249, 0\n"
"s_add_u32 s67, s67, 49152\n"
"v_exp_f32 v165, v165\n"
"v_mfma_f32_16x16x32_bf16 v[168:171], a[196:199], v[20:23], v[168:171]\n"
"v_add_f32 v216, v216, v164\n"
"v_cvt_pk_bf16_f32 v185, v162, v163\n"
"v_mfma_f32_16x16x32_bf16 v[168:171], a[200:203], v[24:27], v[168:171]\n"
"v_exp_f32 v166, v166\n"
"v_mfma_f32_16x16x32_bf16 v[168:171], a[204:207], v[28:31], v[168:171]\n"
"v_add_f32 v216, v216, v165\n"
"v_cvt_pk_bf16_f32 v186, v164, v165\n"
"v_mfma_f32_16x16x32_bf16 v[172:175], a[208:211], v[16:19], v[196:199]\n"
"v_exp_f32 v167, v167\n"
"v_mfma_f32_16x16x32_bf16 v[172:175], a[212:215], v[20:23], v[172:175]\n"
"v_cvt_pk_bf16_f32 v187, v166, v167\n"
"v_add_f32 v216, v216, v166\n"
"v_mfma_f32_16x16x32_bf16 v[172:175], a[216:219], v[24:27], v[172:175]\n"
"v_exp_f32 v168, v168\n"
"v_mfma_f32_16x16x32_bf16 v[172:175], a[220:223], v[28:31], v[172:175]\n"
"ds_read_b128 v[96:99], v222 offset:49152\n"
"v_add_f32 v216, v216, v167\n"
"v_mfma_f32_16x16x32_bf16 a[0:3], a[224:227], v[184:187], a[0:3]\n"
"v_exp_f32 v169, v169\n"
"v_mfma_f32_16x16x32_bf16 a[24:27], a[228:231], v[184:187], a[24:27]\n"
"ds_read_b128 v[100:103], v223 offset:49152\n"
"v_add_f32 v217, v217, v168\n"
"v_mfma_f32_16x16x32_bf16 a[48:51], a[232:235], v[184:187], a[48:51]\n"
"v_exp_f32 v170, v170\n"
"v_mfma_f32_16x16x32_bf16 a[72:75], a[236:239], v[184:187], a[72:75]\n"
"v_add_f32 v217, v217, v169\n"
"v_cvt_pk_bf16_f32 v188, v168, v169\n"
"v_mfma_f32_16x16x32_bf16 a[96:99], a[240:243], v[184:187], a[96:99]\n"
"v_exp_f32 v171, v171\n"
"v_mfma_f32_16x16x32_bf16 a[120:123], a[244:247], v[184:187], a[120:123]\n"
"ds_read_b64_tr_b16 v[128:129], v226 offset:49152\n"
"v_add_f32 v217, v217, v170\n"
"v_mfma_f32_16x16x32_bf16 a[144:147], a[248:251], v[184:187], a[144:147]\n"
"v_exp_f32 v172, v172\n"
"v_mfma_f32_16x16x32_bf16 a[168:171], a[252:255], v[184:187], a[168:171]\n"
"ds_read_b64_tr_b16 v[130:131], v226 offset:50176\n"
"v_add_f32 v217, v217, v171\n"
"v_mfma_f32_16x16x32_bf16 v[176:179], a[192:195], v[32:35], v[200:203]\n"
"v_exp_f32 v173, v173\n"
"v_mfma_f32_16x16x32_bf16 v[176:179], a[196:199], v[36:39], v[176:179]\n"
"v_add_f32 v217, v217, v172\n"
"v_cvt_pk_bf16_f32 v189, v170, v171\n"
"v_mfma_f32_16x16x32_bf16 v[176:179], a[200:203], v[40:43], v[176:179]\n"
"v_exp_f32 v174, v174\n"
"v_mfma_f32_16x16x32_bf16 v[176:179], a[204:207], v[44:47], v[176:179]\n"
"v_add_f32 v217, v217, v173\n"
"v_cvt_pk_bf16_f32 v190, v172, v173\n"
"v_mfma_f32_16x16x32_bf16 v[180:183], a[208:211], v[32:35], v[200:203]\n"
"v_exp_f32 v175, v175\n"
"v_mfma_f32_16x16x32_bf16 v[180:183], a[212:215], v[36:39], v[180:183]\n"
"v_cvt_pk_bf16_f32 v191, v174, v175\n"
"v_add_f32 v217, v217, v174\n"
"v_mfma_f32_16x16x32_bf16 v[180:183], a[216:219], v[40:43], v[180:183]\n"
"v_exp_f32 v176, v176\n"
"v_mfma_f32_16x16x32_bf16 v[180:183], a[220:223], v[44:47], v[180:183]\n"
"ds_read_b128 v[104:107], v224 offset:49152\n"
"v_add_f32 v217, v217, v175\n"
"v_mfma_f32_16x16x32_bf16 a[4:7], a[224:227], v[188:191], a[4:7]\n"
"v_exp_f32 v177, v177\n"
"v_mfma_f32_16x16x32_bf16 a[28:31], a[228:231], v[188:191], a[28:31]\n"
"ds_read_b128 v[108:111], v225 offset:49152\n"
"v_add_f32 v218, v218, v176\n"
"v_mfma_f32_16x16x32_bf16 a[52:55], a[232:235], v[188:191], a[52:55]\n"
"v_exp_f32 v178, v178\n"
"v_mfma_f32_16x16x32_bf16 a[76:79], a[236:239], v[188:191], a[76:79]\n"
"v_add_f32 v218, v218, v177\n"
"v_cvt_pk_bf16_f32 v184, v176, v177\n"
"v_mfma_f32_16x16x32_bf16 a[100:103], a[240:243], v[188:191], a[100:103]\n"
"v_exp_f32 v179, v179\n"
"v_mfma_f32_16x16x32_bf16 a[124:127], a[244:247], v[188:191], a[124:127]\n"
"ds_read_b64_tr_b16 v[132:133], v227 offset:49152\n"
"v_add_f32 v218, v218, v178\n"
"v_mfma_f32_16x16x32_bf16 a[148:151], a[248:251], v[188:191], a[148:151]\n"
"v_exp_f32 v180, v180\n"
"v_mfma_f32_16x16x32_bf16 a[172:175], a[252:255], v[188:191], a[172:175]\n"
"ds_read_b64_tr_b16 v[134:135], v227 offset:50176\n"
"v_add_f32 v218, v218, v179\n"
"v_mfma_f32_16x16x32_bf16 v[160:163], a[192:195], v[48:51], v[204:207]\n"
"v_exp_f32 v181, v181\n"
"v_mfma_f32_16x16x32_bf16 v[160:163], a[196:199], v[52:55], v[160:163]\n"
"v_add_f32 v218, v218, v180\n"
"v_cvt_pk_bf16_f32 v185, v178, v179\n"
"v_mfma_f32_16x16x32_bf16 v[160:163], a[200:203], v[56:59], v[160:163]\n"
"v_exp_f32 v182, v182\n"
"v_mfma_f32_16x16x32_bf16 v[160:163], a[204:207], v[60:63], v[160:163]\n"
"v_add_f32 v218, v218, v181\n"
"v_cvt_pk_bf16_f32 v186, v180, v181\n"
"v_mfma_f32_16x16x32_bf16 v[164:167], a[208:211], v[48:51], v[204:207]\n"
"v_exp_f32 v183, v183\n"
"v_mfma_f32_16x16x32_bf16 v[164:167], a[212:215], v[52:55], v[164:167]\n"
"v_cvt_pk_bf16_f32 v187, v182, v183\n"
"v_add_f32 v218, v218, v182\n"
"v_mfma_f32_16x16x32_bf16 v[164:167], a[216:219], v[56:59], v[164:167]\n"
"v_exp_f32 v160, v160\n"
"v_mfma_f32_16x16x32_bf16 v[164:167], a[220:223], v[60:63], v[164:167]\n"
"ds_read_b128 v[112:115], v222 offset:50176\n"
"v_add_f32 v218, v218, v183\n"
"v_mfma_f32_16x16x32_bf16 a[8:11], a[224:227], v[184:187], a[8:11]\n"
"v_exp_f32 v161, v161\n"
"v_mfma_f32_16x16x32_bf16 a[32:35], a[228:231], v[184:187], a[32:35]\n"
"ds_read_b128 v[116:119], v223 offset:50176\n"
"v_add_f32 v219, v219, v160\n"
"v_mfma_f32_16x16x32_bf16 a[56:59], a[232:235], v[184:187], a[56:59]\n"
"v_exp_f32 v162, v162\n"
"v_mfma_f32_16x16x32_bf16 a[80:83], a[236:239], v[184:187], a[80:83]\n"
"v_add_f32 v219, v219, v161\n"
"v_cvt_pk_bf16_f32 v188, v160, v161\n"
"v_mfma_f32_16x16x32_bf16 a[104:107], a[240:243], v[184:187], a[104:107]\n"
"v_exp_f32 v163, v163\n"
"v_mfma_f32_16x16x32_bf16 a[128:131], a[244:247], v[184:187], a[128:131]\n"
"ds_read_b64_tr_b16 v[136:137], v228 offset:49152\n"
"v_add_f32 v219, v219, v162\n"
"v_mfma_f32_16x16x32_bf16 a[152:155], a[248:251], v[184:187], a[152:155]\n"
"v_exp_f32 v164, v164\n"
"v_mfma_f32_16x16x32_bf16 a[176:179], a[252:255], v[184:187], a[176:179]\n"
"ds_read_b64_tr_b16 v[138:139], v228 offset:50176\n"
"v_add_f32 v219, v219, v163\n"
"v_mfma_f32_16x16x32_bf16 v[168:171], a[192:195], v[64:67], v[208:211]\n"
"v_exp_f32 v165, v165\n"
"v_mfma_f32_16x16x32_bf16 v[168:171], a[196:199], v[68:71], v[168:171]\n"
"v_add_f32 v219, v219, v164\n"
"v_cvt_pk_bf16_f32 v189, v162, v163\n"
"v_mfma_f32_16x16x32_bf16 v[168:171], a[200:203], v[72:75], v[168:171]\n"
"v_exp_f32 v166, v166\n"
"v_mfma_f32_16x16x32_bf16 v[168:171], a[204:207], v[76:79], v[168:171]\n"
"v_add_f32 v219, v219, v165\n"
"v_cvt_pk_bf16_f32 v190, v164, v165\n"
"v_mfma_f32_16x16x32_bf16 v[172:175], a[208:211], v[64:67], v[208:211]\n"
"v_exp_f32 v167, v167\n"
"v_mfma_f32_16x16x32_bf16 v[172:175], a[212:215], v[68:71], v[172:175]\n"
"v_cvt_pk_bf16_f32 v191, v166, v167\n"
"v_add_f32 v219, v219, v166\n"
"v_mfma_f32_16x16x32_bf16 v[172:175], a[216:219], v[72:75], v[172:175]\n"
"v_exp_f32 v168, v168\n"
"v_mfma_f32_16x16x32_bf16 v[172:175], a[220:223], v[76:79], v[172:175]\n"
"ds_read_b128 v[120:123], v224 offset:50176\n"
"v_add_f32 v219, v219, v167\n"
"v_mfma_f32_16x16x32_bf16 a[12:15], a[224:227], v[188:191], a[12:15]\n"
"v_exp_f32 v169, v169\n"
"v_mfma_f32_16x16x32_bf16 a[36:39], a[228:231], v[188:191], a[36:39]\n"
"ds_read_b128 v[124:127], v225 offset:50176\n"
"v_add_f32 v220, v220, v168\n"
"v_mfma_f32_16x16x32_bf16 a[60:63], a[232:235], v[188:191], a[60:63]\n"
"v_exp_f32 v170, v170\n"
"v_mfma_f32_16x16x32_bf16 a[84:87], a[236:239], v[188:191], a[84:87]\n"
"v_add_f32 v220, v220, v169\n"
"v_cvt_pk_bf16_f32 v184, v168, v169\n"
"v_mfma_f32_16x16x32_bf16 a[108:111], a[240:243], v[188:191], a[108:111]\n"
"v_exp_f32 v171, v171\n"
"v_mfma_f32_16x16x32_bf16 a[132:135], a[244:247], v[188:191], a[132:135]\n"
"ds_read_b64_tr_b16 v[140:141], v229 offset:49152\n"
"v_add_f32 v220, v220, v170\n"
"v_mfma_f32_16x16x32_bf16 a[156:159], a[248:251], v[188:191], a[156:159]\n"
"v_exp_f32 v172, v172\n"
"v_mfma_f32_16x16x32_bf16 a[180:183], a[252:255], v[188:191], a[180:183]\n"
"ds_read_b64_tr_b16 v[142:143], v229 offset:50176\n"
"v_add_f32 v220, v220, v171\n"
"v_mfma_f32_16x16x32_bf16 v[176:179], a[192:195], v[80:83], v[212:215]\n"
"v_exp_f32 v173, v173\n"
"v_mfma_f32_16x16x32_bf16 v[176:179], a[196:199], v[84:87], v[176:179]\n"
"v_add_f32 v220, v220, v172\n"
"v_cvt_pk_bf16_f32 v185, v170, v171\n"
"v_mfma_f32_16x16x32_bf16 v[176:179], a[200:203], v[88:91], v[176:179]\n"
"v_exp_f32 v174, v174\n"
"v_mfma_f32_16x16x32_bf16 v[176:179], a[204:207], v[92:95], v[176:179]\n"
"v_add_f32 v220, v220, v173\n"
"v_cvt_pk_bf16_f32 v186, v172, v173\n"
"v_mfma_f32_16x16x32_bf16 v[180:183], a[208:211], v[80:83], v[212:215]\n"
"v_exp_f32 v175, v175\n"
"v_mfma_f32_16x16x32_bf16 v[180:183], a[212:215], v[84:87], v[180:183]\n"
"v_cvt_pk_bf16_f32 v187, v174, v175\n"
"v_add_f32 v220, v220, v174\n"
"v_mfma_f32_16x16x32_bf16 v[180:183], a[216:219], v[88:91], v[180:183]\n"
"v_exp_f32 v176, v176\n"
"v_mfma_f32_16x16x32_bf16 v[180:183], a[220:223], v[92:95], v[180:183]\n"
"ds_read_b64_tr_b16 v[144:145], v230 offset:49152\n"
"v_add_f32 v220, v220, v175\n"
"v_mfma_f32_16x16x32_bf16 a[16:19], a[224:227], v[184:187], a[16:19]\n"
"v_exp_f32 v177, v177\n"
"v_mfma_f32_16x16x32_bf16 a[40:43], a[228:231], v[184:187], a[40:43]\n"
"ds_read_b64_tr_b16 v[146:147], v230 offset:50176\n"
"v_add_f32 v221, v221, v176\n"
"v_mfma_f32_16x16x32_bf16 a[64:67], a[232:235], v[184:187], a[64:67]\n"
"v_exp_f32 v178, v178\n"
"v_mfma_f32_16x16x32_bf16 a[88:91], a[236:239], v[184:187], a[88:91]\n"
"v_add_f32 v221, v221, v177\n"
"v_cvt_pk_bf16_f32 v188, v176, v177\n"
"v_mfma_f32_16x16x32_bf16 a[112:115], a[240:243], v[184:187], a[112:115]\n"
"v_exp_f32 v179, v179\n"
"v_mfma_f32_16x16x32_bf16 a[136:139], a[244:247], v[184:187], a[136:139]\n"
"ds_read_b64_tr_b16 v[148:149], v231 offset:49152\n"
"v_add_f32 v221, v221, v178\n"
"v_mfma_f32_16x16x32_bf16 a[160:163], a[248:251], v[184:187], a[160:163]\n"
"v_exp_f32 v180, v180\n"
"v_mfma_f32_16x16x32_bf16 a[184:187], a[252:255], v[184:187], a[184:187]\n"
"ds_read_b64_tr_b16 v[150:151], v231 offset:50176\n"
"v_add_f32 v221, v221, v179\n"
"s_waitcnt lgkmcnt(8)\n"
"v_mfma_f32_16x16x32_bf16 v[160:163], v[96:99], v[0:3], v[192:195]\n"
"v_exp_f32 v181, v181\n"
"v_mfma_f32_16x16x32_bf16 v[160:163], v[100:103], v[4:7], v[160:163]\n"
"v_add_f32 v221, v221, v180\n"
"v_cvt_pk_bf16_f32 v189, v178, v179\n"
"v_mfma_f32_16x16x32_bf16 v[160:163], v[104:107], v[8:11], v[160:163]\n"
"v_exp_f32 v182, v182\n"
"v_mfma_f32_16x16x32_bf16 v[160:163], v[108:111], v[12:15], v[160:163]\n"
"ds_read_b64_tr_b16 v[152:153], v232 offset:49152\n"
"v_add_f32 v221, v221, v181\n"
"v_cvt_pk_bf16_f32 v190, v180, v181\n"
"v_mfma_f32_16x16x32_bf16 v[164:167], v[112:115], v[0:3], v[192:195]\n"
"v_exp_f32 v183, v183\n"
"v_mfma_f32_16x16x32_bf16 v[164:167], v[116:119], v[4:7], v[164:167]\n"
"ds_read_b64_tr_b16 v[154:155], v232 offset:50176\n"
"v_cvt_pk_bf16_f32 v191, v182, v183\n"
"v_add_f32 v221, v221, v182\n"
"s_waitcnt lgkmcnt(9)\n"
"v_mfma_f32_16x16x32_bf16 v[164:167], v[120:123], v[8:11], v[164:167]\n"
"v_exp_f32 v160, v160\n"
"s_waitcnt lgkmcnt(8)\n"
"v_mfma_f32_16x16x32_bf16 v[164:167], v[124:127], v[12:15], v[164:167]\n"
"ds_read_b64_tr_b16 v[156:157], v233 offset:49152\n"
"v_add_f32 v221, v221, v183\n"
"v_mfma_f32_16x16x32_bf16 a[20:23], a[224:227], v[188:191], a[20:23]\n"
"v_exp_f32 v161, v161\n"
"v_mfma_f32_16x16x32_bf16 a[44:47], a[228:231], v[188:191], a[44:47]\n"
"ds_read_b64_tr_b16 v[158:159], v233 offset:50176\n"
"v_add_f32 v216, v216, v160\n"
"v_mfma_f32_16x16x32_bf16 a[68:71], a[232:235], v[188:191], a[68:71]\n"
"v_exp_f32 v162, v162\n"
"v_mfma_f32_16x16x32_bf16 a[92:95], a[236:239], v[188:191], a[92:95]\n"
"v_add_f32 v216, v216, v161\n"
"v_cvt_pk_bf16_f32 v184, v160, v161\n"
"v_mfma_f32_16x16x32_bf16 a[116:119], a[240:243], v[188:191], a[116:119]\n"
"v_exp_f32 v163, v163\n"
"v_mfma_f32_16x16x32_bf16 a[140:143], a[244:247], v[188:191], a[140:143]\n"
"v_add_f32 v216, v216, v162\n"
"v_mfma_f32_16x16x32_bf16 a[164:167], a[248:251], v[188:191], a[164:167]\n"
"v_exp_f32 v164, v164\n"
"v_mfma_f32_16x16x32_bf16 a[188:191], a[252:255], v[188:191], a[188:191]\n"
"v_add_f32 v216, v216, v163\n"
"s_waitcnt lgkmcnt(0)\n"
"s_barrier\n"
"v_mfma_f32_16x16x32_bf16 v[168:171], v[96:99], v[16:19], v[196:199]\n"
"v_exp_f32 v165, v165\n"
"v_mfma_f32_16x16x32_bf16 v[168:171], v[100:103], v[20:23], v[168:171]\n"
"v_add_f32 v216, v216, v164\n"
"v_cvt_pk_bf16_f32 v185, v162, v163\n"
"v_mfma_f32_16x16x32_bf16 v[168:171], v[104:107], v[24:27], v[168:171]\n"
"global_load_dwordx2 a[216:217], v249, %16 sc1\n"
"v_exp_f32 v166, v166\n"
"v_mfma_f32_16x16x32_bf16 v[168:171], v[108:111], v[28:31], v[168:171]\n"
"v_add_f32 v216, v216, v165\n"
"v_cvt_pk_bf16_f32 v186, v164, v165\n"
"v_mfma_f32_16x16x32_bf16 v[172:175], v[112:115], v[16:19], v[196:199]\n"
"s_mov_b32 s59, 0\n"
"s_add_u32 m0, s60, 0\n"
"v_exp_f32 v167, v167\n"
"v_mfma_f32_16x16x32_bf16 v[172:175], v[116:119], v[20:23], v[172:175]\n"
"buffer_load_dwordx4 v240, s[48:51], s59 offen offset:0 lds\n"
"v_cvt_pk_bf16_f32 v187, v166, v167\n"
"v_add_f32 v216, v216, v166\n"
"v_mfma_f32_16x16x32_bf16 v[172:175], v[120:123], v[24:27], v[172:175]\n"
"v_exp_f32 v168, v168\n"
"v_mfma_f32_16x16x32_bf16 v[172:175], v[124:127], v[28:31], v[172:175]\n"
"v_add_f32 v216, v216, v167\n"
"v_mfma_f32_16x16x32_bf16 a[0:3], v[128:131], v[184:187], a[0:3]\n"
"buffer_load_dwordx4 v241, s[48:51], s59 offen offset:1024 lds\n"
"v_exp_f32 v169, v169\n"
"v_mfma_f32_16x16x32_bf16 a[24:27], v[132:135], v[184:187], a[24:27]\n"
"v_add_f32 v217, v217, v168\n"
"v_mfma_f32_16x16x32_bf16 a[48:51], v[136:139], v[184:187], a[48:51]\n"
"v_exp_f32 v170, v170\n"
"v_mfma_f32_16x16x32_bf16 a[72:75], v[140:143], v[184:187], a[72:75]\n"
"buffer_load_dwordx4 v242, s[48:51], s59 offen offset:2048 lds\n"
"v_add_f32 v217, v217, v169\n"
"v_cvt_pk_bf16_f32 v188, v168, v169\n"
"v_mfma_f32_16x16x32_bf16 a[96:99], v[144:147], v[184:187], a[96:99]\n"
"v_exp_f32 v171, v171\n"
"v_mfma_f32_16x16x32_bf16 a[120:123], v[148:151], v[184:187], a[120:123]\n"
"v_add_f32 v217, v217, v170\n"
"v_mfma_f32_16x16x32_bf16 a[144:147], v[152:155], v[184:187], a[144:147]\n"
"buffer_load_dwordx4 v243, s[48:51], s59 offen offset:3072 lds\n"
"v_exp_f32 v172, v172\n"
"v_mfma_f32_16x16x32_bf16 a[168:171], v[156:159], v[184:187], a[168:171]\n"
"v_add_f32 v217, v217, v171\n"
"v_mfma_f32_16x16x32_bf16 v[176:179], v[96:99], v[32:35], v[200:203]\n"
"s_add_u32 m0, s60, 4096\n"
"v_exp_f32 v173, v173\n"
"v_mfma_f32_16x16x32_bf16 v[176:179], v[100:103], v[36:39], v[176:179]\n"
"buffer_load_dwordx4 v244, s[48:51], s59 offen offset:0 lds\n"
"v_add_f32 v217, v217, v172\n"
"v_cvt_pk_bf16_f32 v189, v170, v171\n"
"v_mfma_f32_16x16x32_bf16 v[176:179], v[104:107], v[40:43], v[176:179]\n"
"v_exp_f32 v174, v174\n"
"v_mfma_f32_16x16x32_bf16 v[176:179], v[108:111], v[44:47], v[176:179]\n"
"v_add_f32 v217, v217, v173\n"
"v_cvt_pk_bf16_f32 v190, v172, v173\n"
"v_mfma_f32_16x16x32_bf16 v[180:183], v[112:115], v[32:35], v[200:203]\n"
"buffer_load_dwordx4 v245, s[48:51], s59 offen offset:1024 lds\n"
"v_exp_f32 v175, v175\n"
"v_mfma_f32_16x16x32_bf16 v[180:183], v[116:119], v[36:39], v[180:183]\n"
"v_cvt_pk_bf16_f32 v191, v174, v175\n"
"v_add_f32 v217, v217, v174\n"
"v_mfma_f32_16x16x32_bf16 v[180:183], v[120:123], v[40:43], v[180:183]\n"
"v_exp_f32 v176, v176\n"
"v_mfma_f32_16x16x32_bf16 v[180:183], v[124:127], v[44:47], v[180:183]\n"
"buffer_load_dwordx4 v246, s[48:51], s59 offen offset:2048 lds\n"
"v_add_f32 v217, v217, v175\n"
"v_mfma_f32_16x16x32_bf16 a[4:7], v[128:131], v[188:191], a[4:7]\n"
"v_exp_f32 v177, v177\n"
"v_mfma_f32_16x16x32_bf16 a[28:31], v[132:135], v[188:191], a[28:31]\n"
"v_add_f32 v218, v218, v176\n"
"v_mfma_f32_16x16x32_bf16 a[52:55], v[136:139], v[188:191], a[52:55]\n"
"buffer_load_dwordx4 v247, s[48:51], s59 offen offset:3072 lds\n"
"v_exp_f32 v178, v178\n"
"v_mfma_f32_16x16x32_bf16 a[76:79], v[140:143], v[188:191], a[76:79]\n"
"v_add_f32 v218, v218, v177\n"
"v_cvt_pk_bf16_f32 v184, v176, v177\n"
"v_mfma_f32_16x16x32_bf16 a[100:103], v[144:147], v[188:191], a[100:103]\n"
"s_add_u32 m0, s60, 8192\n"
"v_exp_f32 v179, v179\n"
"v_mfma_f32_16x16x32_bf16 a[124:127], v[148:151], v[188:191], a[124:127]\n"
"buffer_load_dwordx4 v248, s[48:51], s59 offen offset:0 lds\n"
"v_add_f32 v218, v218, v178\n"
"v_mfma_f32_16x16x32_bf16 a[148:151], v[152:155], v[188:191], a[148:151]\n"
"v_exp_f32 v180, v180\n"
"v_mfma_f32_16x16x32_bf16 a[172:175], v[156:159], v[188:191], a[172:175]\n"
"s_mov_b32 s59, 73728\n"
"s_add_u32 m0, s60, 18432\n"
"v_add_f32 v218, v218, v179\n"
"v_mfma_f32_16x16x32_bf16 v[160:163], v[96:99], v[48:51], v[204:207]\n"
"buffer_load_dwordx4 v240, s[48:51], s59 offen offset:0 lds\n"
"v_exp_f32 v181, v181\n"
"v_mfma_f32_16x16x32_bf16 v[160:163], v[100:103], v[52:55], v[160:163]\n"
"v_add_f32 v218, v218, v180\n"
"v_cvt_pk_bf16_f32 v185, v178, v179\n"
"v_mfma_f32_16x16x32_bf16 v[160:163], v[104:107], v[56:59], v[160:163]\n"
"v_exp_f32 v182, v182\n"
"v_mfma_f32_16x16x32_bf16 v[160:163], v[108:111], v[60:63], v[160:163]\n"
"buffer_load_dwordx4 v241, s[48:51], s59 offen offset:1024 lds\n"
"v_add_f32 v218, v218, v181\n"
"v_cvt_pk_bf16_f32 v186, v180, v181\n"
"v_mfma_f32_16x16x32_bf16 v[164:167], v[112:115], v[48:51], v[204:207]\n"
"v_exp_f32 v183, v183\n"
"v_mfma_f32_16x16x32_bf16 v[164:167], v[116:119], v[52:55], v[164:167]\n"
"v_cvt_pk_bf16_f32 v187, v182, v183\n"
"v_add_f32 v218, v218, v182\n"
"v_mfma_f32_16x16x32_bf16 v[164:167], v[120:123], v[56:59], v[164:167]\n"
"buffer_load_dwordx4 v242, s[48:51], s59 offen offset:2048 lds\n"
"v_exp_f32 v160, v160\n"
"v_mfma_f32_16x16x32_bf16 v[164:167], v[124:127], v[60:63], v[164:167]\n"
"v_add_f32 v218, v218, v183\n"
"v_mfma_f32_16x16x32_bf16 a[8:11], v[128:131], v[184:187], a[8:11]\n"
"v_exp_f32 v161, v161\n"
"v_mfma_f32_16x16x32_bf16 a[32:35], v[132:135], v[184:187], a[32:35]\n"
"buffer_load_dwordx4 v243, s[48:51], s59 offen offset:3072 lds\n"
"v_add_f32 v219, v219, v160\n"
"v_mfma_f32_16x16x32_bf16 a[56:59], v[136:139], v[184:187], a[56:59]\n"
"v_exp_f32 v162, v162\n"
"v_mfma_f32_16x16x32_bf16 a[80:83], v[140:143], v[184:187], a[80:83]\n"
"s_add_u32 m0, s60, 22528\n"
"v_add_f32 v219, v219, v161\n"
"v_cvt_pk_bf16_f32 v188, v160, v161\n"
"v_mfma_f32_16x16x32_bf16 a[104:107], v[144:147], v[184:187], a[104:107]\n"
"buffer_load_dwordx4 v244, s[48:51], s59 offen offset:0 lds\n"
"v_exp_f32 v163, v163\n"
"v_mfma_f32_16x16x32_bf16 a[128:131], v[148:151], v[184:187], a[128:131]\n"
"v_add_f32 v219, v219, v162\n"
"v_mfma_f32_16x16x32_bf16 a[152:155], v[152:155], v[184:187], a[152:155]\n"
"v_exp_f32 v164, v164\n"
"v_mfma_f32_16x16x32_bf16 a[176:179], v[156:159], v[184:187], a[176:179]\n"
"buffer_load_dwordx4 v245, s[48:51], s59 offen offset:1024 lds\n"
"v_add_f32 v219, v219, v163\n"
"v_mfma_f32_16x16x32_bf16 v[168:171], v[96:99], v[64:67], v[208:211]\n"
"v_exp_f32 v165, v165\n"
"v_mfma_f32_16x16x32_bf16 v[168:171], v[100:103], v[68:71], v[168:171]\n"
"v_add_f32 v219, v219, v164\n"
"v_cvt_pk_bf16_f32 v189, v162, v163\n"
"v_mfma_f32_16x16x32_bf16 v[168:171], v[104:107], v[72:75], v[168:171]\n"
"buffer_load_dwordx4 v246, s[48:51], s59 offen offset:2048 lds\n"
"v_exp_f32 v166, v166\n"
"v_mfma_f32_16x16x32_bf16 v[168:171], v[108:111], v[76:79], v[168:171]\n"
"v_add_f32 v219, v219, v165\n"
"v_cvt_pk_bf16_f32 v190, v164, v165\n"
"v_mfma_f32_16x16x32_bf16 v[172:175], v[112:115], v[64:67], v[208:211]\n"
"v_exp_f32 v167, v167\n"
"v_mfma_f32_16x16x32_bf16 v[172:175], v[116:119], v[68:71], v[172:175]\n"
"buffer_load_dwordx4 v247, s[48:51], s59 offen offset:3072 lds\n"
"v_cvt_pk_bf16_f32 v191, v166, v167\n"
"v_add_f32 v219, v219, v166\n"
"v_mfma_f32_16x16x32_bf16 v[172:175], v[120:123], v[72:75], v[172:175]\n"
"v_exp_f32 v168, v168\n"
"v_mfma_f32_16x16x32_bf16 v[172:175], v[124:127], v[76:79], v[172:175]\n"
"s_add_u32 m0, s60, 26624\n"
"v_add_f32 v219, v219, v167\n"
"v_mfma_f32_16x16x32_bf16 a[12:15], v[128:131], v[188:191], a[12:15]\n"
"buffer_load_dwordx4 v248, s[48:51], s59 offen offset:0 lds\n"
"v_exp_f32 v169, v169\n"
"v_mfma_f32_16x16x32_bf16 a[36:39], v[132:135], v[188:191], a[36:39]\n"
"v_add_f32 v220, v220, v168\n"
"v_mfma_f32_16x16x32_bf16 a[60:63], v[136:139], v[188:191], a[60:63]\n"
"s_mov_b32 s59, 147456\n"
"s_add_u32 m0, s60, 36864\n"
"v_exp_f32 v170, v170\n"
"v_mfma_f32_16x16x32_bf16 a[84:87], v[140:143], v[188:191], a[84:87]\n"
"buffer_load_dwordx4 v240, s[48:51], s59 offen offset:0 lds\n"
"v_add_f32 v220, v220, v169\n"
"v_cvt_pk_bf16_f32 v184, v168, v169\n"
"v_mfma_f32_16x16x32_bf16 a[108:111], v[144:147], v[188:191], a[108:111]\n"
"v_exp_f32 v171, v171\n"
"v_mfma_f32_16x16x32_bf16 a[132:135], v[148:151], v[188:191], a[132:135]\n"
"v_add_f32 v220, v220, v170\n"
"v_mfma_f32_16x16x32_bf16 a[156:159], v[152:155], v[188:191], a[156:159]\n"
"buffer_load_dwordx4 v241, s[48:51], s59 offen offset:1024 lds\n"
"v_exp_f32 v172, v172\n"
"v_mfma_f32_16x16x32_bf16 a[180:183], v[156:159], v[188:191], a[180:183]\n"
"v_add_f32 v220, v220, v171\n"
"v_mfma_f32_16x16x32_bf16 v[176:179], v[96:99], v[80:83], v[212:215]\n"
"v_exp_f32 v173, v173\n"
"v_mfma_f32_16x16x32_bf16 v[176:179], v[100:103], v[84:87], v[176:179]\n"
"buffer_load_dwordx4 v242, s[48:51], s59 offen offset:2048 lds\n"
"v_add_f32 v220, v220, v172\n"
"v_cvt_pk_bf16_f32 v185, v170, v171\n"
"v_mfma_f32_16x16x32_bf16 v[176:179], v[104:107], v[88:91], v[176:179]\n"
"v_exp_f32 v174, v174\n"
"v_mfma_f32_16x16x32_bf16 v[176:179], v[108:111], v[92:95], v[176:179]\n"
"v_add_f32 v220, v220, v173\n"
"v_cvt_pk_bf16_f32 v186, v172, v173\n"
"v_mfma_f32_16x16x32_bf16 v[180:183], v[112:115], v[80:83], v[212:215]\n"
"buffer_load_dwordx4 v243, s[48:51], s59 offen offset:3072 lds\n"
"v_exp_f32 v175, v175\n"
"v_mfma_f32_16x16x32_bf16 v[180:183], v[116:119], v[84:87], v[180:183]\n"
"v_cvt_pk_bf16_f32 v187, v174, v175\n"
"v_add_f32 v220, v220, v174\n"
"v_mfma_f32_16x16x32_bf16 v[180:183], v[120:123], v[88:91], v[180:183]\n"
"s_add_u32 m0, s60, 40960\n"
"v_exp_f32 v176, v176\n"
"v_mfma_f32_16x16x32_bf16 v[180:183], v[124:127], v[92:95], v[180:183]\n"
"buffer_load_dwordx4 v244, s[48:51], s59 offen offset:0 lds\n"
"v_add_f32 v220, v220, v175\n"
"v_mfma_f32_16x16x32_bf16 a[16:19], v[128:131], v[184:187], a[16:19]\n"
"v_exp_f32 v177, v177\n"
"v_mfma_f32_16x16x32_bf16 a[40:43], v[132:135], v[184:187], a[40:43]\n"
"v_add_f32 v221, v221, v176\n"
"v_mfma_f32_16x16x32_bf16 a[64:67], v[136:139], v[184:187], a[64:67]\n"
"buffer_load_dwordx4 v245, s[48:51], s59 offen offset:1024 lds\n"
"v_exp_f32 v178, v178\n"
"v_mfma_f32_16x16x32_bf16 a[88:91], v[140:143], v[184:187], a[88:91]\n"
"v_add_f32 v221, v221, v177\n"
"v_cvt_pk_bf16_f32 v188, v176, v177\n"
"v_mfma_f32_16x16x32_bf16 a[112:115], v[144:147], v[184:187], a[112:115]\n"
"v_exp_f32 v179, v179\n"
"v_mfma_f32_16x16x32_bf16 a[136:139], v[148:151], v[184:187], a[136:139]\n"
"buffer_load_dwordx4 v246, s[48:51], s59 offen offset:2048 lds\n"
"v_add_f32 v221, v221, v178\n"
"v_mfma_f32_16x16x32_bf16 a[160:163], v[152:155], v[184:187], a[160:163]\n"
"v_exp_f32 v180, v180\n"
"v_mfma_f32_16x16x32_bf16 a[184:187], v[156:159], v[184:187], a[184:187]\n"
"v_add_f32 v221, v221, v179\n"
"buffer_load_dwordx4 v247, s[48:51], s59 offen offset:3072 lds\n"
"v_exp_f32 v181, v181\n"
"v_add_f32 v221, v221, v180\n"
"v_cvt_pk_bf16_f32 v189, v178, v179\n"
"s_add_u32 m0, s60, 45056\n"
"v_exp_f32 v182, v182\n"
"buffer_load_dwordx4 v248, s[48:51], s59 offen offset:0 lds\n"
"v_add_f32 v221, v221, v181\n"
"v_cvt_pk_bf16_f32 v190, v180, v181\n"
"v_exp_f32 v183, v183\n"
"s_nop 0\n"
"v_cvt_pk_bf16_f32 v191, v182, v183\n"
"v_add_f32 v221, v221, v182\n"
"v_add_f32 v221, v221, v183\n"
"v_mfma_f32_16x16x32_bf16 a[20:23], v[128:131], v[188:191], a[20:23]\n"
"v_mfma_f32_16x16x32_bf16 a[44:47], v[132:135], v[188:191], a[44:47]\n"
"v_mfma_f32_16x16x32_bf16 a[68:71], v[136:139], v[188:191], a[68:71]\n"
"v_mfma_f32_16x16x32_bf16 a[92:95], v[140:143], v[188:191], a[92:95]\n"
"v_mfma_f32_16x16x32_bf16 a[116:119], v[144:147], v[188:191], a[116:119]\n"
"v_mfma_f32_16x16x32_bf16 a[140:143], v[148:151], v[188:191], a[140:143]\n"
"v_mfma_f32_16x16x32_bf16 a[164:167], v[152:155], v[188:191], a[164:167]\n"
"v_mfma_f32_16x16x32_bf16 a[188:191], v[156:159], v[188:191], a[188:191]\n"
"v_mov_b32 v253, v216\n"
"s_nop 1\n"
"v_permlane32_swap_b32 v216, v253\n"
"v_add_f32 v216, v216, v253\n"
"v_mov_b32 v253, v216\n"
"s_nop 1\n"
"v_permlane16_swap_b32 v216, v253\n"
"v_add_f32 v216, v216, v253\n"
"v_mov_b32 v253, v217\n"
"s_nop 1\n"
"v_permlane32_swap_b32 v217, v253\n"
"v_add_f32 v217, v217, v253\n"
"v_mov_b32 v253, v217\n"
"s_nop 1\n"
"v_permlane16_swap_b32 v217, v253\n"
"v_add_f32 v217, v217, v253\n"
"v_mov_b32 v253, v218\n"
"s_nop 1\n"
"v_permlane32_swap_b32 v218, v253\n"
"v_add_f32 v218, v218, v253\n"
"v_mov_b32 v253, v218\n"
"s_nop 1\n"
"v_permlane16_swap_b32 v218, v253\n"
"v_add_f32 v218, v218, v253\n"
"v_mov_b32 v253, v219\n"
"s_nop 1\n"
"v_permlane32_swap_b32 v219, v253\n"
"v_add_f32 v219, v219, v253\n"
"v_mov_b32 v253, v219\n"
"s_nop 1\n"
"v_permlane16_swap_b32 v219, v253\n"
"v_add_f32 v219, v219, v253\n"
"v_mov_b32 v253, v220\n"
"s_nop 1\n"
"v_permlane32_swap_b32 v220, v253\n"
"v_add_f32 v220, v220, v253\n"
"v_mov_b32 v253, v220\n"
"s_nop 1\n"
"v_permlane16_swap_b32 v220, v253\n"
"v_add_f32 v220, v220, v253\n"
"v_mov_b32 v253, v221\n"
"s_nop 1\n"
"v_permlane32_swap_b32 v221, v253\n"
"v_add_f32 v221, v221, v253\n"
"v_mov_b32 v253, v221\n"
"s_nop 1\n"
"v_permlane16_swap_b32 v221, v253\n"
"v_add_f32 v221, v221, v253\n"
"v_add_f32 v253, v216, v217\n"
"v_add_f32 v253, v253, v218\n"
"v_add_f32 v253, v253, v219\n"
"v_add_f32 v253, v253, v220\n"
"v_add_f32 v253, v253, v221\n"
"v_cmp_nge_f32 vcc, s70, v253\n"
"s_nop 4\n"
"s_cbranch_vccz .Lf_ebok\n"
"s_getpc_b64 s[80:81]\n"
"s_add_u32 s80, s80, 12\n"
"s_addc_u32 s81, s81, 0\n"
"s_branch .Lf_slow\n"
".Lf_ebok:\n"
"s_mov_b64 s[36:37], %14\n"
"s_mov_b32 s38, 221184\n"
"s_mov_b64 s[40:41], %19\n"
"s_mov_b32 s42, 14336\n"
"s_waitcnt vmcnt(28)\n"
"v_mbcnt_lo_u32_b32 v251, -1, 0\n"
"v_mbcnt_hi_u32_b32 v251, -1, v251\n"
"v_lshlrev_b32 v250, 4, v251\n"
"v_mov_b32 v252, 0\n"
"v_lshrrev_b32 v1, 2, v251\n"
"v_mul_u32_u24 v1, 288, v1\n"
"v_and_b32 v2, 3, v251\n"
"v_lshl_add_u32 v1, v2, 3, v1\n"
"v_add_u32 v249, s64, v1\n"
"s_mul_i32 s73, %21, 3072\n"
"v_add_u32 v253, s73, v250\n"
"s_sub_u32 s73, 3072, s73\n"
"v_add_u32 v255, s73, v250\n"
"s_waitcnt vmcnt(27)\n"
"v_accvgpr_read_b32 v251, a216\n"
"s_nop 0\n"
"v_readfirstlane_b32 s73, v251\n"
"v_accvgpr_read_b32 v251, a217\n"
"s_nop 0\n"
"v_readfirstlane_b32 s74, v251\n"
"s_nop 0\n"
"s_sub_u32 s73, s73, s85\n"
"s_sub_u32 s74, s74, s86\n"
"s_sub_u32 s73, s73, 1\n"
"s_sub_u32 s74, s74, 1\n"
"s_max_u32 s73, s73, s74\n"
"s_cmp_le_u32 s73, 1\n"
"s_cbranch_scc1 .Lf_p0ok\n"
"s_mov_b32 s75, 0x4000\n"
".Lf_p0spin:\n"
"s_sleep 2\n"
"global_load_dwordx2 a[216:217], v252, %16 sc1\n"
"s_waitcnt vmcnt(0)\n"
"v_accvgpr_read_b32 v251, a216\n"
"s_nop 0\n"
"v_readfirstlane_b32 s73, v251\n"
"v_accvgpr_read_b32 v251, a217\n"
"s_nop 0\n"
"v_readfirstlane_b32 s74, v251\n"
"s_nop 0\n"
"s_sub_u32 s73, s73, s85\n"
"s_sub_u32 s74, s74, s86\n"
"s_sub_u32 s73, s73, 1\n"
"s_sub_u32 s74, s74, 1\n"
"s_max_u32 s73, s73, s74\n"
"s_sub_u32 s75, s75, 1\n"
"s_cmp_eq_u32 s75, 0\n"
"s_cbranch_scc1 .Lf_p0ok\n"
"s_cmp_gt_u32 s73, 1\n"
"s_cbranch_scc1 .Lf_p0spin\n"
".Lf_p0ok:\n"
"v_rcp_f32 v176, v216\n"
"v_rcp_f32 v178, v217\n"
"v_rcp_f32 v180, v218\n"
"v_rcp_f32 v182, v219\n"
"v_rcp_f32 v184, v220\n"
"v_rcp_f32 v186, v221\n"
"v_accvgpr_read_b32 v120, a0\n"
"v_accvgpr_read_b32 v121, a1\n"
"v_accvgpr_read_b32 v122, a2\n"
"v_accvgpr_read_b32 v123, a3\n"
"s_mov_b32 s61, 0\n"
"s_mov_b32 s62, 4096\n"
"s_mov_b32 s63, 8192\n"
"buffer_load_dwordx4 v[0:3], v255, s[44:47], s61 offen offset:0 sc1\n"
"v_accvgpr_read_b32 v124, a24\n"
"v_accvgpr_read_b32 v125, a25\n"
"v_accvgpr_read_b32 v126, a26\n"
"v_accvgpr_read_b32 v127, a27\n"
"buffer_load_dwordx4 v[4:7], v255, s[44:47], s62 offen offset:2048 sc1\n"
"v_pk_mul_f32 v[120:121], v[120:121], v[176:177] op_sel_hi:[1,0]\n"
"v_pk_mul_f32 v[122:123], v[122:123], v[176:177] op_sel_hi:[1,0]\n"
"v_pk_mul_f32 v[124:125], v[124:125], v[176:177] op_sel_hi:[1,0]\n"
"v_pk_mul_f32 v[126:127], v[126:127], v[176:177] op_sel_hi:[1,0]\n"
"buffer_load_dwordx4 v[8:11], v255, s[44:47], s61 offen offset:1024 sc1\n"
"v_cvt_pk_bf16_f32 v120, v120, v121\n"
"v_cvt_pk_bf16_f32 v121, v122, v123\n"
"v_cvt_pk_bf16_f32 v122, v124, v125\n"
"v_cvt_pk_bf16_f32 v123, v126, v127\n"
"buffer_load_dwordx4 v[12:15], v255, s[44:47], s62 offen offset:3072 sc1\n"
"v_accvgpr_read_b32 v124, a4\n"
"v_accvgpr_read_b32 v125, a5\n"
"v_accvgpr_read_b32 v126, a6\n"
"v_accvgpr_read_b32 v127, a7\n"
"buffer_load_dwordx4 v[16:19], v255, s[44:47], s61 offen offset:2048 sc1\n"
"v_accvgpr_read_b32 v128, a28\n"
"v_accvgpr_read_b32 v129, a29\n"
"v_accvgpr_read_b32 v130, a30\n"
"v_accvgpr_read_b32 v131, a31\n"
"buffer_load_dwordx4 v[20:23], v255, s[44:47], s63 offen offset:0 sc1\n"
"v_pk_mul_f32 v[124:125], v[124:125], v[178:179] op_sel_hi:[1,0]\n"
"v_pk_mul_f32 v[126:127], v[126:127], v[178:179] op_sel_hi:[1,0]\n"
"v_pk_mul_f32 v[128:129], v[128:129], v[178:179] op_sel_hi:[1,0]\n"
"v_pk_mul_f32 v[130:131], v[130:131], v[178:179] op_sel_hi:[1,0]\n"
"buffer_load_dwordx4 v[24:27], v253, s[44:47], s61 offen offset:0 sc1\n"
"v_cvt_pk_bf16_f32 v124, v124, v125\n"
"v_cvt_pk_bf16_f32 v125, v126, v127\n"
"v_cvt_pk_bf16_f32 v126, v128, v129\n"
"v_cvt_pk_bf16_f32 v127, v130, v131\n"
"buffer_load_dwordx4 v[28:31], v253, s[44:47], s62 offen offset:2048 sc1\n"
"v_accvgpr_read_b32 v128, a8\n"
"v_accvgpr_read_b32 v129, a9\n"
"v_accvgpr_read_b32 v130, a10\n"
"v_accvgpr_read_b32 v131, a11\n"
"buffer_load_dwordx4 v[32:35], v253, s[44:47], s61 offen offset:1024 sc1\n"
"v_accvgpr_read_b32 v132, a32\n"
"v_accvgpr_read_b32 v133, a33\n"
"v_accvgpr_read_b32 v134, a34\n"
"v_accvgpr_read_b32 v135, a35\n"
"buffer_load_dwordx4 v[36:39], v253, s[44:47], s62 offen offset:3072 sc1\n"
"v_pk_mul_f32 v[128:129], v[128:129], v[180:181] op_sel_hi:[1,0]\n"
"v_pk_mul_f32 v[130:131], v[130:131], v[180:181] op_sel_hi:[1,0]\n"
"v_pk_mul_f32 v[132:133], v[132:133], v[180:181] op_sel_hi:[1,0]\n"
"v_pk_mul_f32 v[134:135], v[134:135], v[180:181] op_sel_hi:[1,0]\n"
"buffer_load_dwordx4 v[40:43], v253, s[44:47], s61 offen offset:2048 sc1\n"
"v_cvt_pk_bf16_f32 v128, v128, v129\n"
"v_cvt_pk_bf16_f32 v129, v130, v131\n"
"v_cvt_pk_bf16_f32 v130, v132, v133\n"
"v_cvt_pk_bf16_f32 v131, v134, v135\n"
"buffer_load_dwordx4 v[44:47], v253, s[44:47], s63 offen offset:0 sc1\n"
"v_accvgpr_read_b32 v132, a12\n"
"v_accvgpr_read_b32 v133, a13\n"
"v_accvgpr_read_b32 v134, a14\n"
"v_accvgpr_read_b32 v135, a15\n"
"s_mov_b32 s61, 12288\n"
"s_mov_b32 s62, 16384\n"
"s_mov_b32 s63, 20480\n"
"buffer_load_dwordx4 v[192:195], v255, s[44:47], s61 offen offset:0 sc1\n"
"v_accvgpr_read_b32 v136, a36\n"
"v_accvgpr_read_b32 v137, a37\n"
"v_accvgpr_read_b32 v138, a38\n"
"v_accvgpr_read_b32 v139, a39\n"
"buffer_load_dwordx4 v[196:199], v255, s[44:47], s62 offen offset:2048 sc1\n"
"v_pk_mul_f32 v[132:133], v[132:133], v[182:183] op_sel_hi:[1,0]\n"
"v_pk_mul_f32 v[134:135], v[134:135], v[182:183] op_sel_hi:[1,0]\n"
"v_pk_mul_f32 v[136:137], v[136:137], v[182:183] op_sel_hi:[1,0]\n"
"v_pk_mul_f32 v[138:139], v[138:139], v[182:183] op_sel_hi:[1,0]\n"
"buffer_load_dwordx4 v[200:203], v255, s[44:47], s61 offen offset:1024 sc1\n"
"v_cvt_pk_bf16_f32 v132, v132, v133\n"
"v_cvt_pk_bf16_f32 v133, v134, v135\n"
"v_cvt_pk_bf16_f32 v134, v136, v137\n"
"v_cvt_pk_bf16_f32 v135, v138, v139\n"
"buffer_load_dwordx4 v[204:207], v255, s[44:47], s62 offen offset:3072 sc1\n"
"v_accvgpr_read_b32 v136, a16\n"
"v_accvgpr_read_b32 v137, a17\n"
"v_accvgpr_read_b32 v138, a18\n"
"v_accvgpr_read_b32 v139, a19\n"
"buffer_load_dwordx4 v[208:211], v255, s[44:47], s61 offen offset:2048 sc1\n"
"v_accvgpr_read_b32 v140, a40\n"
"v_accvgpr_read_b32 v141, a41\n"
"v_accvgpr_read_b32 v142, a42\n"
"v_accvgpr_read_b32 v143, a43\n"
"buffer_load_dwordx4 v[212:215], v255, s[44:47], s63 offen offset:0 sc1\n"
"v_pk_mul_f32 v[136:137], v[136:137], v[184:185] op_sel_hi:[1,0]\n"
"v_pk_mul_f32 v[138:139], v[138:139], v[184:185] op_sel_hi:[1,0]\n"
"v_pk_mul_f32 v[140:141], v[140:141], v[184:185] op_sel_hi:[1,0]\n"
"v_pk_mul_f32 v[142:143], v[142:143], v[184:185] op_sel_hi:[1,0]\n"
"buffer_load_dwordx4 v[216:219], v253, s[44:47], s61 offen offset:0 sc1\n"
"v_cvt_pk_bf16_f32 v136, v136, v137\n"
"v_cvt_pk_bf16_f32 v137, v138, v139\n"
"v_cvt_pk_bf16_f32 v138, v140, v141\n"
"v_cvt_pk_bf16_f32 v139, v142, v143\n"
"buffer_load_dwordx4 v[220:223], v253, s[44:47], s62 offen offset:2048 sc1\n"
"v_accvgpr_read_b32 v140, a20\n"
"v_accvgpr_read_b32 v141, a21\n"
"v_accvgpr_read_b32 v142, a22\n"
"v_accvgpr_read_b32 v143, a23\n"
"buffer_load_dwordx4 v[224:227], v253, s[44:47], s61 offen offset:1024 sc1\n"
"v_accvgpr_read_b32 v144, a44\n"
"v_accvgpr_read_b32 v145, a45\n"
"v_accvgpr_read_b32 v146, a46\n"
"v_accvgpr_read_b32 v147, a47\n"
"buffer_load_dwordx4 v[228:231], v253, s[44:47], s62 offen offset:3072 sc1\n"
"v_pk_mul_f32 v[140:141], v[140:141], v[186:187] op_sel_hi:[1,0]\n"
"v_pk_mul_f32 v[142:143], v[142:143], v[186:187] op_sel_hi:[1,0]\n"
"v_pk_mul_f32 v[144:145], v[144:145], v[186:187] op_sel_hi:[1,0]\n"
"v_pk_mul_f32 v[146:147], v[146:147], v[186:187] op_sel_hi:[1,0]\n"
"buffer_load_dwordx4 v[232:235], v253, s[44:47], s61 offen offset:2048 sc1\n"
"v_cvt_pk_bf16_f32 v140, v140, v141\n"
"v_cvt_pk_bf16_f32 v141, v142, v143\n"
"v_cvt_pk_bf16_f32 v142, v144, v145\n"
"v_cvt_pk_bf16_f32 v143, v146, v147\n"
"buffer_load_dwordx4 v[236:239], v253, s[44:47], s63 offen offset:0 sc1\n"
"v_accvgpr_read_b32 v144, a48\n"
"v_accvgpr_read_b32 v145, a49\n"
"v_accvgpr_read_b32 v146, a50\n"
"v_accvgpr_read_b32 v147, a51\n"
"v_accvgpr_read_b32 v148, a72\n"
"v_accvgpr_read_b32 v149, a73\n"
"v_accvgpr_read_b32 v150, a74\n"
"v_accvgpr_read_b32 v151, a75\n"
"v_pk_mul_f32 v[144:145], v[144:145], v[176:177] op_sel_hi:[1,0]\n"
"v_pk_mul_f32 v[146:147], v[146:147], v[176:177] op_sel_hi:[1,0]\n"
"v_pk_mul_f32 v[148:149], v[148:149], v[176:177] op_sel_hi:[1,0]\n"
"v_pk_mul_f32 v[150:151], v[150:151], v[176:177] op_sel_hi:[1,0]\n"
"v_cvt_pk_bf16_f32 v144, v144, v145\n"
"v_cvt_pk_bf16_f32 v145, v146, v147\n"
"v_cvt_pk_bf16_f32 v146, v148, v149\n"
"v_cvt_pk_bf16_f32 v147, v150, v151\n"
"s_waitcnt vmcnt(42)\n"
"s_barrier\n"
"s_add_u32 s75, s67, 0\n"
"buffer_store_dwordx4 v[120:123], v250, s[52:55], s75 offen offset:0 sc1\n"
"v_accvgpr_read_b32 v148, a52\n"
"v_accvgpr_read_b32 v149, a53\n"
"ds_read_b64_tr_b16 v[48:49], v249 offset:0\n"
"v_accvgpr_read_b32 v150, a54\n"
"v_accvgpr_read_b32 v151, a55\n"
"v_accvgpr_read_b32 v152, a76\n"
"v_accvgpr_read_b32 v153, a77\n"
"ds_read_b64_tr_b16 v[50:51], v249 offset:4608\n"
"v_accvgpr_read_b32 v154, a78\n"
"v_accvgpr_read_b32 v155, a79\n"
"v_pk_mul_f32 v[148:149], v[148:149], v[178:179] op_sel_hi:[1,0]\n"
"v_pk_mul_f32 v[150:151], v[150:151], v[178:179] op_sel_hi:[1,0]\n"
"ds_read_b64_tr_b16 v[52:53], v249 offset:32\n"
"v_pk_mul_f32 v[152:153], v[152:153], v[178:179] op_sel_hi:[1,0]\n"
"v_pk_mul_f32 v[154:155], v[154:155], v[178:179] op_sel_hi:[1,0]\n"
"v_cvt_pk_bf16_f32 v148, v148, v149\n"
"v_cvt_pk_bf16_f32 v149, v150, v151\n"
"ds_read_b64_tr_b16 v[54:55], v249 offset:4640\n"
"v_cvt_pk_bf16_f32 v150, v152, v153\n"
"v_cvt_pk_bf16_f32 v151, v154, v155\n"
"buffer_store_dwordx4 v[124:127], v250, s[52:55], s75 offen offset:1024 sc1\n"
"v_accvgpr_read_b32 v152, a56\n"
"v_accvgpr_read_b32 v153, a57\n"
"ds_read_b64_tr_b16 v[56:57], v249 offset:64\n"
"v_accvgpr_read_b32 v154, a58\n"
"v_accvgpr_read_b32 v155, a59\n"
"v_accvgpr_read_b32 v156, a80\n"
"v_accvgpr_read_b32 v157, a81\n"
"ds_read_b64_tr_b16 v[58:59], v249 offset:4672\n"
"v_accvgpr_read_b32 v158, a82\n"
"v_accvgpr_read_b32 v159, a83\n"
"v_pk_mul_f32 v[152:153], v[152:153], v[180:181] op_sel_hi:[1,0]\n"
"v_pk_mul_f32 v[154:155], v[154:155], v[180:181] op_sel_hi:[1,0]\n"
"ds_read_b64_tr_b16 v[60:61], v249 offset:96\n"
"v_pk_mul_f32 v[156:157], v[156:157], v[180:181] op_sel_hi:[1,0]\n"
"v_pk_mul_f32 v[158:159], v[158:159], v[180:181] op_sel_hi:[1,0]\n"
"v_cvt_pk_bf16_f32 v152, v152, v153\n"
"v_cvt_pk_bf16_f32 v153, v154, v155\n"
"ds_read_b64_tr_b16 v[62:63], v249 offset:4704\n"
"v_cvt_pk_bf16_f32 v154, v156, v157\n"
"v_cvt_pk_bf16_f32 v155, v158, v159\n"
"buffer_store_dwordx4 v[128:131], v250, s[52:55], s75 offen offset:2048 sc1\n"
"v_accvgpr_read_b32 v156, a60\n"
"v_accvgpr_read_b32 v157, a61\n"
"ds_read_b64_tr_b16 v[64:65], v249 offset:128\n"
"v_accvgpr_read_b32 v158, a62\n"
"v_accvgpr_read_b32 v159, a63\n"
"v_accvgpr_read_b32 v160, a84\n"
"v_accvgpr_read_b32 v161, a85\n"
"ds_read_b64_tr_b16 v[66:67], v249 offset:4736\n"
"v_accvgpr_read_b32 v162, a86\n"
"v_accvgpr_read_b32 v163, a87\n"
"v_pk_mul_f32 v[156:157], v[156:157], v[182:183] op_sel_hi:[1,0]\n"
"v_pk_mul_f32 v[158:159], v[158:159], v[182:183] op_sel_hi:[1,0]\n"
"ds_read_b64_tr_b16 v[68:69], v249 offset:160\n"
"v_pk_mul_f32 v[160:161], v[160:161], v[182:183] op_sel_hi:[1,0]\n"
"v_pk_mul_f32 v[162:163], v[162:163], v[182:183] op_sel_hi:[1,0]\n"
"v_cvt_pk_bf16_f32 v156, v156, v157\n"
"v_cvt_pk_bf16_f32 v157, v158, v159\n"
"ds_read_b64_tr_b16 v[70:71], v249 offset:4768\n"
"v_cvt_pk_bf16_f32 v158, v160, v161\n"
"v_cvt_pk_bf16_f32 v159, v162, v163\n"
"buffer_store_dwordx4 v[132:135], v250, s[52:55], s75 offen offset:3072 sc1\n"
"v_accvgpr_read_b32 v160, a64\n"
"v_accvgpr_read_b32 v161, a65\n"
"ds_read_b64_tr_b16 v[72:73], v249 offset:192\n"
"v_accvgpr_read_b32 v162, a66\n"
"v_accvgpr_read_b32 v163, a67\n"
"v_accvgpr_read_b32 v164, a88\n"
"v_accvgpr_read_b32 v165, a89\n"
"ds_read_b64_tr_b16 v[74:75], v249 offset:4800\n"
"v_accvgpr_read_b32 v166, a90\n"
"v_accvgpr_read_b32 v167, a91\n"
"v_pk_mul_f32 v[160:161], v[160:161], v[184:185] op_sel_hi:[1,0]\n"
"v_pk_mul_f32 v[162:163], v[162:163], v[184:185] op_sel_hi:[1,0]\n"
"ds_read_b64_tr_b16 v[76:77], v249 offset:224\n"
"v_pk_mul_f32 v[164:165], v[164:165], v[184:185] op_sel_hi:[1,0]\n"
"v_pk_mul_f32 v[166:167], v[166:167], v[184:185] op_sel_hi:[1,0]\n"
"v_cvt_pk_bf16_f32 v160, v160, v161\n"
"v_cvt_pk_bf16_f32 v161, v162, v163\n"
"ds_read_b64_tr_b16 v[78:79], v249 offset:4832\n"
"v_cvt_pk_bf16_f32 v162, v164, v165\n"
"v_cvt_pk_bf16_f32 v163, v166, v167\n"
"s_add_u32 s75, s67, 4096\n"
"buffer_store_dwordx4 v[136:139], v250, s[52:55], s75 offen offset:0 sc1\n"
"v_accvgpr_read_b32 v164, a68\n"
"v_accvgpr_read_b32 v165, a69\n"
"ds_read_b64_tr_b16 v[80:81], v249 offset:256\n"
"v_accvgpr_read_b32 v166, a70\n"
"v_accvgpr_read_b32 v167, a71\n"
"v_accvgpr_read_b32 v168, a92\n"
"v_accvgpr_read_b32 v169, a93\n"
"ds_read_b64_tr_b16 v[82:83], v249 offset:4864\n"
"v_accvgpr_read_b32 v170, a94\n"
"v_accvgpr_read_b32 v171, a95\n"
"v_pk_mul_f32 v[164:165], v[164:165], v[186:187] op_sel_hi:[1,0]\n"
"v_pk_mul_f32 v[166:167], v[166:167], v[186:187] op_sel_hi:[1,0]\n"
"ds_read_b64_tr_b16 v[84:85], v249 offset:9216\n"
"v_pk_mul_f32 v[168:169], v[168:169], v[186:187] op_sel_hi:[1,0]\n"
"v_pk_mul_f32 v[170:171], v[170:171], v[186:187] op_sel_hi:[1,0]\n"
"v_cvt_pk_bf16_f32 v164, v164, v165\n"
"v_cvt_pk_bf16_f32 v165, v166, v167\n"
"ds_read_b64_tr_b16 v[86:87], v249 offset:13824\n"
"v_cvt_pk_bf16_f32 v166, v168, v169\n"
"v_cvt_pk_bf16_f32 v167, v170, v171\n"
"buffer_store_dwordx4 v[140:143], v250, s[52:55], s75 offen offset:1024 sc1\n"
"v_accvgpr_read_b32 v120, a96\n"
"v_accvgpr_read_b32 v121, a97\n"
"ds_read_b64_tr_b16 v[88:89], v249 offset:9248\n"
"v_accvgpr_read_b32 v122, a98\n"
"v_accvgpr_read_b32 v123, a99\n"
"v_accvgpr_read_b32 v124, a120\n"
"v_accvgpr_read_b32 v125, a121\n"
"ds_read_b64_tr_b16 v[90:91], v249 offset:13856\n"
"v_accvgpr_read_b32 v126, a122\n"
"v_accvgpr_read_b32 v127, a123\n"
"v_pk_mul_f32 v[120:121], v[120:121], v[176:177] op_sel_hi:[1,0]\n"
"v_pk_mul_f32 v[122:123], v[122:123], v[176:177] op_sel_hi:[1,0]\n"
"ds_read_b64_tr_b16 v[92:93], v249 offset:9280\n"
"v_pk_mul_f32 v[124:125], v[124:125], v[176:177] op_sel_hi:[1,0]\n"
"v_pk_mul_f32 v[126:127], v[126:127], v[176:177] op_sel_hi:[1,0]\n"
"v_cvt_pk_bf16_f32 v120, v120, v121\n"
"v_cvt_pk_bf16_f32 v121, v122, v123\n"
"ds_read_b64_tr_b16 v[94:95], v249 offset:13888\n"
"v_cvt_pk_bf16_f32 v122, v124, v125\n"
"v_cvt_pk_bf16_f32 v123, v126, v127\n"
"buffer_store_dwordx4 v[144:147], v250, s[52:55], s75 offen offset:2048 sc1\n"
"v_accvgpr_read_b32 v124, a100\n"
"v_accvgpr_read_b32 v125, a101\n"
"ds_read_b64_tr_b16 v[96:97], v249 offset:9312\n"
"v_accvgpr_read_b32 v126, a102\n"
"v_accvgpr_read_b32 v127, a103\n"
"v_accvgpr_read_b32 v128, a124\n"
"v_accvgpr_read_b32 v129, a125\n"
"ds_read_b64_tr_b16 v[98:99], v249 offset:13920\n"
"v_accvgpr_read_b32 v130, a126\n"
"v_accvgpr_read_b32 v131, a127\n"
"v_pk_mul_f32 v[124:125], v[124:125], v[178:179] op_sel_hi:[1,0]\n"
"v_pk_mul_f32 v[126:127], v[126:127], v[178:179] op_sel_hi:[1,0]\n"
"ds_read_b64_tr_b16 v[100:101], v249 offset:9344\n"
"v_pk_mul_f32 v[128:129], v[128:129], v[178:179] op_sel_hi:[1,0]\n"
"v_pk_mul_f32 v[130:131], v[130:131], v[178:179] op_sel_hi:[1,0]\n"
"v_cvt_pk_bf16_f32 v124, v124, v125\n"
"v_cvt_pk_bf16_f32 v125, v126, v127\n"
"ds_read_b64_tr_b16 v[102:103], v249 offset:13952\n"
"v_cvt_pk_bf16_f32 v126, v128, v129\n"
"v_cvt_pk_bf16_f32 v127, v130, v131\n"
"buffer_store_dwordx4 v[148:151], v250, s[52:55], s75 offen offset:3072 sc1\n"
"v_accvgpr_read_b32 v128, a104\n"
"v_accvgpr_read_b32 v129, a105\n"
"ds_read_b64_tr_b16 v[104:105], v249 offset:9376\n"
"v_accvgpr_read_b32 v130, a106\n"
"v_accvgpr_read_b32 v131, a107\n"
"v_accvgpr_read_b32 v132, a128\n"
"v_accvgpr_read_b32 v133, a129\n"
"ds_read_b64_tr_b16 v[106:107], v249 offset:13984\n"
"v_accvgpr_read_b32 v134, a130\n"
"v_accvgpr_read_b32 v135, a131\n"
"v_pk_mul_f32 v[128:129], v[128:129], v[180:181] op_sel_hi:[1,0]\n"
"v_pk_mul_f32 v[130:131], v[130:131], v[180:181] op_sel_hi:[1,0]\n"
"ds_read_b64_tr_b16 v[108:109], v249 offset:9408\n"
"v_pk_mul_f32 v[132:133], v[132:133], v[180:181] op_sel_hi:[1,0]\n"
"v_pk_mul_f32 v[134:135], v[134:135], v[180:181] op_sel_hi:[1,0]\n"
"v_cvt_pk_bf16_f32 v128, v128, v129\n"
"v_cvt_pk_bf16_f32 v129, v130, v131\n"
"ds_read_b64_tr_b16 v[110:111], v249 offset:14016\n"
"v_cvt_pk_bf16_f32 v130, v132, v133\n"
"v_cvt_pk_bf16_f32 v131, v134, v135\n"
"s_add_u32 s75, s67, 8192\n"
"buffer_store_dwordx4 v[152:155], v250, s[52:55], s75 offen offset:0 sc1\n"
"v_accvgpr_write_b32 a216, v128\n"
"v_accvgpr_write_b32 a217, v129\n"
"v_accvgpr_write_b32 a218, v130\n"
"v_accvgpr_write_b32 a219, v131\n"
"v_accvgpr_read_b32 v132, a108\n"
"v_accvgpr_read_b32 v133, a109\n"
"ds_read_b64_tr_b16 v[112:113], v249 offset:9440\n"
"v_accvgpr_read_b32 v134, a110\n"
"v_accvgpr_read_b32 v135, a111\n"
"v_accvgpr_read_b32 v136, a132\n"
"v_accvgpr_read_b32 v137, a133\n"
"ds_read_b64_tr_b16 v[114:115], v249 offset:14048\n"
"v_accvgpr_read_b32 v138, a134\n"
"v_accvgpr_read_b32 v139, a135\n"
"v_pk_mul_f32 v[132:133], v[132:133], v[182:183] op_sel_hi:[1,0]\n"
"v_pk_mul_f32 v[134:135], v[134:135], v[182:183] op_sel_hi:[1,0]\n"
"ds_read_b64_tr_b16 v[116:117], v249 offset:9472\n"
"v_pk_mul_f32 v[136:137], v[136:137], v[182:183] op_sel_hi:[1,0]\n"
"v_pk_mul_f32 v[138:139], v[138:139], v[182:183] op_sel_hi:[1,0]\n"
"v_cvt_pk_bf16_f32 v132, v132, v133\n"
"v_cvt_pk_bf16_f32 v133, v134, v135\n"
"ds_read_b64_tr_b16 v[118:119], v249 offset:14080\n"
"v_cvt_pk_bf16_f32 v134, v136, v137\n"
"v_cvt_pk_bf16_f32 v135, v138, v139\n"
"buffer_store_dwordx4 v[156:159], v250, s[52:55], s75 offen offset:1024 sc1\n"
"v_accvgpr_write_b32 a220, v132\n"
"v_accvgpr_write_b32 a221, v133\n"
"v_accvgpr_write_b32 a222, v134\n"
"v_accvgpr_write_b32 a223, v135\n"
"v_accvgpr_read_b32 v136, a112\n"
"v_accvgpr_read_b32 v137, a113\n"
"v_accvgpr_read_b32 v138, a114\n"
"v_accvgpr_read_b32 v139, a115\n"
"v_accvgpr_read_b32 v140, a136\n"
"v_accvgpr_read_b32 v141, a137\n"
"v_accvgpr_read_b32 v142, a138\n"
"v_accvgpr_read_b32 v143, a139\n"
"v_pk_mul_f32 v[136:137], v[136:137], v[184:185] op_sel_hi:[1,0]\n"
"v_pk_mul_f32 v[138:139], v[138:139], v[184:185] op_sel_hi:[1,0]\n"
"v_pk_mul_f32 v[140:141], v[140:141], v[184:185] op_sel_hi:[1,0]\n"
"v_pk_mul_f32 v[142:143], v[142:143], v[184:185] op_sel_hi:[1,0]\n"
"v_cvt_pk_bf16_f32 v136, v136, v137\n"
"v_cvt_pk_bf16_f32 v137, v138, v139\n"
"v_cvt_pk_bf16_f32 v138, v140, v141\n"
"v_cvt_pk_bf16_f32 v139, v142, v143\n"
"buffer_store_dwordx4 v[160:163], v250, s[52:55], s75 offen offset:2048 sc1\n"
"v_accvgpr_write_b32 a224, v136\n"
"v_accvgpr_write_b32 a225, v137\n"
"v_accvgpr_write_b32 a226, v138\n"
"v_accvgpr_write_b32 a227, v139\n"
"v_accvgpr_read_b32 v140, a116\n"
"v_accvgpr_read_b32 v141, a117\n"
"v_accvgpr_read_b32 v142, a118\n"
"v_accvgpr_read_b32 v143, a119\n"
"v_accvgpr_read_b32 v144, a140\n"
"v_accvgpr_read_b32 v145, a141\n"
"v_accvgpr_read_b32 v146, a142\n"
"v_accvgpr_read_b32 v147, a143\n"
"v_pk_mul_f32 v[140:141], v[140:141], v[186:187] op_sel_hi:[1,0]\n"
"v_pk_mul_f32 v[142:143], v[142:143], v[186:187] op_sel_hi:[1,0]\n"
"v_pk_mul_f32 v[144:145], v[144:145], v[186:187] op_sel_hi:[1,0]\n"
"v_pk_mul_f32 v[146:147], v[146:147], v[186:187] op_sel_hi:[1,0]\n"
"v_cvt_pk_bf16_f32 v140, v140, v141\n"
"v_cvt_pk_bf16_f32 v141, v142, v143\n"
"v_cvt_pk_bf16_f32 v142, v144, v145\n"
"v_cvt_pk_bf16_f32 v143, v146, v147\n"
"buffer_store_dwordx4 v[164:167], v250, s[52:55], s75 offen offset:3072 sc1\n"
"v_accvgpr_write_b32 a228, v140\n"
"v_accvgpr_write_b32 a229, v141\n"
"v_accvgpr_write_b32 a230, v142\n"
"v_accvgpr_write_b32 a231, v143\n"
"v_accvgpr_read_b32 v144, a144\n"
"v_accvgpr_read_b32 v145, a145\n"
"v_accvgpr_read_b32 v146, a146\n"
"v_accvgpr_read_b32 v147, a147\n"
"v_accvgpr_read_b32 v148, a168\n"
"v_accvgpr_read_b32 v149, a169\n"
"v_accvgpr_read_b32 v150, a170\n"
"v_accvgpr_read_b32 v151, a171\n"
"v_pk_mul_f32 v[144:145], v[144:145], v[176:177] op_sel_hi:[1,0]\n"
"v_pk_mul_f32 v[146:147], v[146:147], v[176:177] op_sel_hi:[1,0]\n"
"v_pk_mul_f32 v[148:149], v[148:149], v[176:177] op_sel_hi:[1,0]\n"
"v_pk_mul_f32 v[150:151], v[150:151], v[176:177] op_sel_hi:[1,0]\n"
"v_cvt_pk_bf16_f32 v144, v144, v145\n"
"v_cvt_pk_bf16_f32 v145, v146, v147\n"
"v_cvt_pk_bf16_f32 v146, v148, v149\n"
"v_cvt_pk_bf16_f32 v147, v150, v151\n"
"s_add_u32 s75, s67, 12288\n"
"buffer_store_dwordx4 v[120:123], v250, s[52:55], s75 offen offset:0 sc1\n"
"v_accvgpr_write_b32 a232, v144\n"
"v_accvgpr_write_b32 a233, v145\n"
"v_accvgpr_write_b32 a234, v146\n"
"v_accvgpr_write_b32 a235, v147\n"
"v_accvgpr_read_b32 v148, a148\n"
"v_accvgpr_read_b32 v149, a149\n"
"v_accvgpr_read_b32 v150, a150\n"
"v_accvgpr_read_b32 v151, a151\n"
"v_accvgpr_read_b32 v152, a172\n"
"v_accvgpr_read_b32 v153, a173\n"
"v_accvgpr_read_b32 v154, a174\n"
"v_accvgpr_read_b32 v155, a175\n"
"v_pk_mul_f32 v[148:149], v[148:149], v[178:179] op_sel_hi:[1,0]\n"
"v_pk_mul_f32 v[150:151], v[150:151], v[178:179] op_sel_hi:[1,0]\n"
"v_pk_mul_f32 v[152:153], v[152:153], v[178:179] op_sel_hi:[1,0]\n"
"v_pk_mul_f32 v[154:155], v[154:155], v[178:179] op_sel_hi:[1,0]\n"
"v_cvt_pk_bf16_f32 v148, v148, v149\n"
"v_cvt_pk_bf16_f32 v149, v150, v151\n"
"v_cvt_pk_bf16_f32 v150, v152, v153\n"
"v_cvt_pk_bf16_f32 v151, v154, v155\n"
"buffer_store_dwordx4 v[124:127], v250, s[52:55], s75 offen offset:1024 sc1\n"
"v_accvgpr_write_b32 a236, v148\n"
"v_accvgpr_write_b32 a237, v149\n"
"v_accvgpr_write_b32 a238, v150\n"
"v_accvgpr_write_b32 a239, v151\n"
"v_accvgpr_read_b32 v152, a152\n"
"v_accvgpr_read_b32 v153, a153\n"
"v_accvgpr_read_b32 v154, a154\n"
"v_accvgpr_read_b32 v155, a155\n"
"v_accvgpr_read_b32 v156, a176\n"
"v_accvgpr_read_b32 v157, a177\n"
"v_accvgpr_read_b32 v158, a178\n"
"v_accvgpr_read_b32 v159, a179\n"
"v_pk_mul_f32 v[152:153], v[152:153], v[180:181] op_sel_hi:[1,0]\n"
"v_pk_mul_f32 v[154:155], v[154:155], v[180:181] op_sel_hi:[1,0]\n"
"v_pk_mul_f32 v[156:157], v[156:157], v[180:181] op_sel_hi:[1,0]\n"
"v_pk_mul_f32 v[158:159], v[158:159], v[180:181] op_sel_hi:[1,0]\n"
"v_cvt_pk_bf16_f32 v152, v152, v153\n"
"v_cvt_pk_bf16_f32 v153, v154, v155\n"
"v_cvt_pk_bf16_f32 v154, v156, v157\n"
"v_cvt_pk_bf16_f32 v155, v158, v159\n"
"v_accvgpr_write_b32 a240, v152\n"
"v_accvgpr_write_b32 a241, v153\n"
"v_accvgpr_write_b32 a242, v154\n"
"v_accvgpr_write_b32 a243, v155\n"
"v_accvgpr_read_b32 v156, a156\n"
"v_accvgpr_read_b32 v157, a157\n"
"v_accvgpr_read_b32 v158, a158\n"
"v_accvgpr_read_b32 v159, a159\n"
"v_accvgpr_read_b32 v160, a180\n"
"v_accvgpr_read_b32 v161, a181\n"
"v_accvgpr_read_b32 v162, a182\n"
"v_accvgpr_read_b32 v163, a183\n"
"v_pk_mul_f32 v[156:157], v[156:157], v[182:183] op_sel_hi:[1,0]\n"
"v_pk_mul_f32 v[158:159], v[158:159], v[182:183] op_sel_hi:[1,0]\n"
"v_pk_mul_f32 v[160:161], v[160:161], v[182:183] op_sel_hi:[1,0]\n"
"v_pk_mul_f32 v[162:163], v[162:163], v[182:183] op_sel_hi:[1,0]\n"
"v_cvt_pk_bf16_f32 v156, v156, v157\n"
"v_cvt_pk_bf16_f32 v157, v158, v159\n"
"v_cvt_pk_bf16_f32 v158, v160, v161\n"
"v_cvt_pk_bf16_f32 v159, v162, v163\n"
"v_accvgpr_write_b32 a244, v156\n"
"v_accvgpr_write_b32 a245, v157\n"
"v_accvgpr_write_b32 a246, v158\n"
"v_accvgpr_write_b32 a247, v159\n"
"v_accvgpr_read_b32 v160, a160\n"
"v_accvgpr_read_b32 v161, a161\n"
"v_accvgpr_read_b32 v162, a162\n"
"v_accvgpr_read_b32 v163, a163\n"
"v_accvgpr_read_b32 v164, a184\n"
"v_accvgpr_read_b32 v165, a185\n"
"v_accvgpr_read_b32 v166, a186\n"
"v_accvgpr_read_b32 v167, a187\n"
"v_pk_mul_f32 v[160:161], v[160:161], v[184:185] op_sel_hi:[1,0]\n"
"v_pk_mul_f32 v[162:163], v[162:163], v[184:185] op_sel_hi:[1,0]\n"
"v_pk_mul_f32 v[164:165], v[164:165], v[184:185] op_sel_hi:[1,0]\n"
"v_pk_mul_f32 v[166:167], v[166:167], v[184:185] op_sel_hi:[1,0]\n"
"v_cvt_pk_bf16_f32 v160, v160, v161\n"
"v_cvt_pk_bf16_f32 v161, v162, v163\n"
"v_cvt_pk_bf16_f32 v162, v164, v165\n"
"v_cvt_pk_bf16_f32 v163, v166, v167\n"
"v_accvgpr_write_b32 a248, v160\n"
"v_accvgpr_write_b32 a249, v161\n"
"v_accvgpr_write_b32 a250, v162\n"
"v_accvgpr_write_b32 a251, v163\n"
"v_accvgpr_read_b32 v164, a164\n"
"v_accvgpr_read_b32 v165, a165\n"
"v_accvgpr_read_b32 v166, a166\n"
"v_accvgpr_read_b32 v167, a167\n"
"v_accvgpr_read_b32 v168, a188\n"
"v_accvgpr_read_b32 v169, a189\n"
"v_accvgpr_read_b32 v170, a190\n"
"v_accvgpr_read_b32 v171, a191\n"
"v_pk_mul_f32 v[164:165], v[164:165], v[186:187] op_sel_hi:[1,0]\n"
"v_pk_mul_f32 v[166:167], v[166:167], v[186:187] op_sel_hi:[1,0]\n"
"v_pk_mul_f32 v[168:169], v[168:169], v[186:187] op_sel_hi:[1,0]\n"
"v_pk_mul_f32 v[170:171], v[170:171], v[186:187] op_sel_hi:[1,0]\n"
"v_cvt_pk_bf16_f32 v164, v164, v165\n"
"v_cvt_pk_bf16_f32 v165, v166, v167\n"
"v_cvt_pk_bf16_f32 v166, v168, v169\n"
"v_cvt_pk_bf16_f32 v167, v170, v171\n"
"v_accvgpr_write_b32 a252, v164\n"
"v_accvgpr_write_b32 a253, v165\n"
"v_accvgpr_write_b32 a254, v166\n"
"v_accvgpr_write_b32 a255, v167\n"
"s_waitcnt vmcnt(26) lgkmcnt(0)\n"
"s_barrier\n"
"v_mfma_f32_16x16x32_bf16 a[0:3], v[48:51], v[0:3], 0\n"
"s_mov_b32 s61, 24576\n"
"v_mfma_f32_16x16x32_bf16 a[0:3], v[84:87], v[4:7], a[0:3]\n"
"s_mov_b32 s62, 28672\n"
"v_mfma_f32_16x16x32_bf16 a[24:27], v[52:55], v[0:3], 0\n"
"s_mov_b32 s63, 32768\n"
"v_mfma_f32_16x16x32_bf16 a[24:27], v[88:91], v[4:7], a[24:27]\n"
"s_mov_b32 s59, 221184\n"
"ds_read_b64_tr_b16 v[120:121], v249 offset:18432\n"
"v_mfma_f32_16x16x32_bf16 a[48:51], v[56:59], v[0:3], 0\n"
"s_add_u32 m0, s60, 0\n"
"v_mfma_f32_16x16x32_bf16 a[48:51], v[92:95], v[4:7], a[48:51]\n"
"buffer_load_dwordx4 v240, s[48:51], s59 offen offset:0 lds\n"
"v_mfma_f32_16x16x32_bf16 a[72:75], v[60:63], v[0:3], 0\n"
"ds_read_b64_tr_b16 v[122:123], v249 offset:23040\n"
"v_mfma_f32_16x16x32_bf16 a[72:75], v[96:99], v[4:7], a[72:75]\n"
"v_mfma_f32_16x16x32_bf16 a[96:99], v[64:67], v[0:3], 0\n"
"ds_read_b64_tr_b16 v[124:125], v249 offset:18464\n"
"v_mfma_f32_16x16x32_bf16 a[96:99], v[100:103], v[4:7], a[96:99]\n"
"v_mfma_f32_16x16x32_bf16 a[120:123], v[68:71], v[0:3], 0\n"
"buffer_load_dwordx4 v241, s[48:51], s59 offen offset:1024 lds\n"
"v_mfma_f32_16x16x32_bf16 a[120:123], v[104:107], v[4:7], a[120:123]\n"
"ds_read_b64_tr_b16 v[126:127], v249 offset:23072\n"
"v_mfma_f32_16x16x32_bf16 a[144:147], v[72:75], v[0:3], 0\n"
"v_mfma_f32_16x16x32_bf16 a[144:147], v[108:111], v[4:7], a[144:147]\n"
"ds_read_b64_tr_b16 v[128:129], v249 offset:18496\n"
"v_mfma_f32_16x16x32_bf16 a[168:171], v[76:79], v[0:3], 0\n"
"buffer_load_dwordx4 v242, s[48:51], s59 offen offset:2048 lds\n"
"v_mfma_f32_16x16x32_bf16 a[168:171], v[112:115], v[4:7], a[168:171]\n"
"ds_read_b64_tr_b16 v[130:131], v249 offset:23104\n"
"v_mfma_f32_16x16x32_bf16 a[192:195], v[80:83], v[0:3], 0\n"
"v_mfma_f32_16x16x32_bf16 a[192:195], v[116:119], v[4:7], a[192:195]\n"
"ds_read_b64_tr_b16 v[132:133], v249 offset:18528\n"
"v_mfma_f32_16x16x32_bf16 a[4:7], v[48:51], v[8:11], 0\n"
"buffer_load_dwordx4 v[0:3], v255, s[44:47], s61 offen offset:0 sc1\n"
"v_mfma_f32_16x16x32_bf16 a[4:7], v[84:87], v[12:15], a[4:7]\n"
"ds_read_b64_tr_b16 v[134:135], v249 offset:23136\n"
"v_mfma_f32_16x16x32_bf16 a[28:31], v[52:55], v[8:11], 0\n"
"v_mfma_f32_16x16x32_bf16 a[28:31], v[88:91], v[12:15], a[28:31]\n"
"ds_read_b64_tr_b16 v[136:137], v249 offset:18560\n"
"v_mfma_f32_16x16x32_bf16 a[52:55], v[56:59], v[8:11], 0\n"
"v_mfma_f32_16x16x32_bf16 a[52:55], v[92:95], v[12:15], a[52:55]\n"
"buffer_load_dwordx4 v243, s[48:51], s59 offen offset:3072 lds\n"
"v_mfma_f32_16x16x32_bf16 a[76:79], v[60:63], v[8:11], 0\n"
"ds_read_b64_tr_b16 v[138:139], v249 offset:23168\n"
"v_mfma_f32_16x16x32_bf16 a[76:79], v[96:99], v[12:15], a[76:79]\n"
"v_mfma_f32_16x16x32_bf16 a[100:103], v[64:67], v[8:11], 0\n"
"ds_read_b64_tr_b16 v[140:141], v249 offset:18592\n"
"v_mfma_f32_16x16x32_bf16 a[100:103], v[100:103], v[12:15], a[100:103]\n"
"buffer_load_dwordx4 v[4:7], v255, s[44:47], s62 offen offset:2048 sc1\n"
"v_mfma_f32_16x16x32_bf16 a[124:127], v[68:71], v[8:11], 0\n"
"ds_read_b64_tr_b16 v[142:143], v249 offset:23200\n"
"v_mfma_f32_16x16x32_bf16 a[124:127], v[104:107], v[12:15], a[124:127]\n"
"v_mfma_f32_16x16x32_bf16 a[148:151], v[72:75], v[8:11], 0\n"
"ds_read_b64_tr_b16 v[144:145], v249 offset:18624\n"
"v_mfma_f32_16x16x32_bf16 a[148:151], v[108:111], v[12:15], a[148:151]\n"
"s_add_u32 m0, s60, 4096\n"
"v_mfma_f32_16x16x32_bf16 a[172:175], v[76:79], v[8:11], 0\n"
"buffer_load_dwordx4 v244, s[48:51], s59 offen offset:0 lds\n"
"v_mfma_f32_16x16x32_bf16 a[172:175], v[112:115], v[12:15], a[172:175]\n"
"ds_read_b64_tr_b16 v[146:147], v249 offset:23232\n"
"v_mfma_f32_16x16x32_bf16 a[196:199], v[80:83], v[8:11], 0\n"
"v_mfma_f32_16x16x32_bf16 a[196:199], v[116:119], v[12:15], a[196:199]\n"
"ds_read_b64_tr_b16 v[148:149], v249 offset:18656\n"
"v_mfma_f32_16x16x32_bf16 a[8:11], v[48:51], v[16:19], 0\n"
"buffer_load_dwordx4 v[8:11], v255, s[44:47], s61 offen offset:1024 sc1\n"
"v_mfma_f32_16x16x32_bf16 a[8:11], v[84:87], v[20:23], a[8:11]\n"
"ds_read_b64_tr_b16 v[150:151], v249 offset:23264\n"
"v_mfma_f32_16x16x32_bf16 a[32:35], v[52:55], v[16:19], 0\n"
"v_mfma_f32_16x16x32_bf16 a[32:35], v[88:91], v[20:23], a[32:35]\n"
"ds_read_b64_tr_b16 v[152:153], v249 offset:18688\n"
"v_mfma_f32_16x16x32_bf16 a[56:59], v[56:59], v[16:19], 0\n"
"v_mfma_f32_16x16x32_bf16 a[56:59], v[92:95], v[20:23], a[56:59]\n"
"buffer_load_dwordx4 v245, s[48:51], s59 offen offset:1024 lds\n"
"v_mfma_f32_16x16x32_bf16 a[80:83], v[60:63], v[16:19], 0\n"
"ds_read_b64_tr_b16 v[154:155], v249 offset:23296\n"
"v_mfma_f32_16x16x32_bf16 a[80:83], v[96:99], v[20:23], a[80:83]\n"
"v_mfma_f32_16x16x32_bf16 a[104:107], v[64:67], v[16:19], 0\n"
"ds_read_b64_tr_b16 v[156:157], v249 offset:27648\n"
"v_mfma_f32_16x16x32_bf16 a[104:107], v[100:103], v[20:23], a[104:107]\n"
"buffer_load_dwordx4 v[12:15], v255, s[44:47], s62 offen offset:3072 sc1\n"
"v_mfma_f32_16x16x32_bf16 a[128:131], v[68:71], v[16:19], 0\n"
"ds_read_b64_tr_b16 v[158:159], v249 offset:32256\n"
"v_mfma_f32_16x16x32_bf16 a[128:131], v[104:107], v[20:23], a[128:131]\n"
"v_mfma_f32_16x16x32_bf16 a[152:155], v[72:75], v[16:19], 0\n"
"ds_read_b64_tr_b16 v[160:161], v249 offset:27680\n"
"v_mfma_f32_16x16x32_bf16 a[152:155], v[108:111], v[20:23], a[152:155]\n"
"v_mfma_f32_16x16x32_bf16 a[176:179], v[76:79], v[16:19], 0\n"
"buffer_load_dwordx4 v246, s[48:51], s59 offen offset:2048 lds\n"
"v_mfma_f32_16x16x32_bf16 a[176:179], v[112:115], v[20:23], a[176:179]\n"
"ds_read_b64_tr_b16 v[162:163], v249 offset:32288\n"
"v_mfma_f32_16x16x32_bf16 a[200:203], v[80:83], v[16:19], 0\n"
"v_mfma_f32_16x16x32_bf16 a[200:203], v[116:119], v[20:23], a[200:203]\n"
"ds_read_b64_tr_b16 v[164:165], v249 offset:27712\n"
"v_mfma_f32_16x16x32_bf16 a[12:15], v[48:51], v[24:27], 0\n"
"buffer_load_dwordx4 v[16:19], v255, s[44:47], s61 offen offset:2048 sc1\n"
"v_mfma_f32_16x16x32_bf16 a[12:15], v[84:87], v[28:31], a[12:15]\n"
"ds_read_b64_tr_b16 v[166:167], v249 offset:32320\n"
"v_mfma_f32_16x16x32_bf16 a[36:39], v[52:55], v[24:27], 0\n"
"v_mfma_f32_16x16x32_bf16 a[36:39], v[88:91], v[28:31], a[36:39]\n"
"ds_read_b64_tr_b16 v[168:169], v249 offset:27744\n"
"v_mfma_f32_16x16x32_bf16 a[60:63], v[56:59], v[24:27], 0\n"
"v_mfma_f32_16x16x32_bf16 a[60:63], v[92:95], v[28:31], a[60:63]\n"
"buffer_load_dwordx4 v247, s[48:51], s59 offen offset:3072 lds\n"
"v_mfma_f32_16x16x32_bf16 a[84:87], v[60:63], v[24:27], 0\n"
"ds_read_b64_tr_b16 v[170:171], v249 offset:32352\n"
"v_mfma_f32_16x16x32_bf16 a[84:87], v[96:99], v[28:31], a[84:87]\n"
"v_mfma_f32_16x16x32_bf16 a[108:111], v[64:67], v[24:27], 0\n"
"ds_read_b64_tr_b16 v[172:173], v249 offset:27776\n"
"v_mfma_f32_16x16x32_bf16 a[108:111], v[100:103], v[28:31], a[108:111]\n"
"buffer_load_dwordx4 v[20:23], v255, s[44:47], s63 offen offset:0 sc1\n"
"v_mfma_f32_16x16x32_bf16 a[132:135], v[68:71], v[24:27], 0\n"
"ds_read_b64_tr_b16 v[174:175], v249 offset:32384\n"
"v_mfma_f32_16x16x32_bf16 a[132:135], v[104:107], v[28:31], a[132:135]\n"
"v_mfma_f32_16x16x32_bf16 a[156:159], v[72:75], v[24:27], 0\n"
"ds_read_b64_tr_b16 v[176:177], v249 offset:27808\n"
"v_mfma_f32_16x16x32_bf16 a[156:159], v[108:111], v[28:31], a[156:159]\n"
"s_add_u32 m0, s60, 8192\n"
"v_mfma_f32_16x16x32_bf16 a[180:183], v[76:79], v[24:27], 0\n"
"buffer_load_dwordx4 v248, s[48:51], s59 offen offset:0 lds\n"
"v_mfma_f32_16x16x32_bf16 a[180:183], v[112:115], v[28:31], a[180:183]\n"
"ds_read_b64_tr_b16 v[178:179], v249 offset:32416\n"
"v_mfma_f32_16x16x32_bf16 a[204:207], v[80:83], v[24:27], 0\n"
"v_mfma_f32_16x16x32_bf16 a[204:207], v[116:119], v[28:31], a[204:207]\n"
"ds_read_b64_tr_b16 v[180:181], v249 offset:27840\n"
"v_mfma_f32_16x16x32_bf16 a[16:19], v[48:51], v[32:35], 0\n"
"buffer_load_dwordx4 v[24:27], v253, s[44:47], s61 offen offset:0 sc1\n"
"v_mfma_f32_16x16x32_bf16 a[16:19], v[84:87], v[36:39], a[16:19]\n"
"ds_read_b64_tr_b16 v[182:183], v249 offset:32448\n"
"v_mfma_f32_16x16x32_bf16 a[40:43], v[52:55], v[32:35], 0\n"
"v_mfma_f32_16x16x32_bf16 a[40:43], v[88:91], v[36:39], a[40:43]\n"
"ds_read_b64_tr_b16 v[184:185], v249 offset:27872\n"
"v_mfma_f32_16x16x32_bf16 a[64:67], v[56:59], v[32:35], 0\n"
"s_add_u32 s75, s67, 12288\n"
"v_mfma_f32_16x16x32_bf16 a[64:67], v[92:95], v[36:39], a[64:67]\n"
"ds_read_b64_tr_b16 v[186:187], v249 offset:32480\n"
"buffer_store_dwordx4 a[216:219], v250, s[52:55], s75 offen offset:2048 sc1\n"
"v_mfma_f32_16x16x32_bf16 a[88:91], v[60:63], v[32:35], 0\n"
"v_mfma_f32_16x16x32_bf16 a[88:91], v[96:99], v[36:39], a[88:91]\n"
"ds_read_b64_tr_b16 v[188:189], v249 offset:27904\n"
"v_mfma_f32_16x16x32_bf16 a[112:115], v[64:67], v[32:35], 0\n"
"v_mfma_f32_16x16x32_bf16 a[112:115], v[100:103], v[36:39], a[112:115]\n"
"buffer_load_dwordx4 v[28:31], v253, s[44:47], s62 offen offset:2048 sc1\n"
"s_add_u32 s75, s67, 12288\n"
"v_mfma_f32_16x16x32_bf16 a[136:139], v[68:71], v[32:35], 0\n"
"ds_read_b64_tr_b16 v[190:191], v249 offset:32512\n"
"buffer_store_dwordx4 a[220:223], v250, s[52:55], s75 offen offset:3072 sc1\n"
"v_mfma_f32_16x16x32_bf16 a[136:139], v[104:107], v[36:39], a[136:139]\n"
"v_mfma_f32_16x16x32_bf16 a[160:163], v[72:75], v[32:35], 0\n"
"v_mfma_f32_16x16x32_bf16 a[160:163], v[108:111], v[36:39], a[160:163]\n"
"v_mfma_f32_16x16x32_bf16 a[184:187], v[76:79], v[32:35], 0\n"
"s_add_u32 s75, s67, 16384\n"
"v_mfma_f32_16x16x32_bf16 a[184:187], v[112:115], v[36:39], a[184:187]\n"
"buffer_store_dwordx4 a[224:227], v250, s[52:55], s75 offen offset:0 sc1\n"
"v_mfma_f32_16x16x32_bf16 a[208:211], v[80:83], v[32:35], 0\n"
"v_mfma_f32_16x16x32_bf16 a[208:211], v[116:119], v[36:39], a[208:211]\n"
"v_mfma_f32_16x16x32_bf16 a[20:23], v[48:51], v[40:43], 0\n"
"buffer_load_dwordx4 v[32:35], v253, s[44:47], s61 offen offset:1024 sc1\n"
"v_mfma_f32_16x16x32_bf16 a[20:23], v[84:87], v[44:47], a[20:23]\n"
"s_add_u32 s75, s67, 16384\n"
"v_mfma_f32_16x16x32_bf16 a[44:47], v[52:55], v[40:43], 0\n"
"buffer_store_dwordx4 a[228:231], v250, s[52:55], s75 offen offset:1024 sc1\n"
"v_mfma_f32_16x16x32_bf16 a[44:47], v[88:91], v[44:47], a[44:47]\n"
"v_mfma_f32_16x16x32_bf16 a[68:71], v[56:59], v[40:43], 0\n"
"v_mfma_f32_16x16x32_bf16 a[68:71], v[92:95], v[44:47], a[68:71]\n"
"v_mfma_f32_16x16x32_bf16 a[92:95], v[60:63], v[40:43], 0\n"
"s_add_u32 s75, s67, 16384\n"
"v_mfma_f32_16x16x32_bf16 a[92:95], v[96:99], v[44:47], a[92:95]\n"
"buffer_store_dwordx4 a[232:235], v250, s[52:55], s75 offen offset:2048 sc1\n"
"v_mfma_f32_16x16x32_bf16 a[116:119], v[64:67], v[40:43], 0\n"
"v_mfma_f32_16x16x32_bf16 a[116:119], v[100:103], v[44:47], a[116:119]\n"
"buffer_load_dwordx4 v[36:39], v253, s[44:47], s62 offen offset:3072 sc1\n"
"v_mfma_f32_16x16x32_bf16 a[140:143], v[68:71], v[40:43], 0\n"
"v_mfma_f32_16x16x32_bf16 a[140:143], v[104:107], v[44:47], a[140:143]\n"
"v_mfma_f32_16x16x32_bf16 a[164:167], v[72:75], v[40:43], 0\n"
"v_mfma_f32_16x16x32_bf16 a[164:167], v[108:111], v[44:47], a[164:167]\n"
"v_mfma_f32_16x16x32_bf16 a[188:191], v[76:79], v[40:43], 0\n"
"v_mfma_f32_16x16x32_bf16 a[188:191], v[112:115], v[44:47], a[188:191]\n"
"v_mfma_f32_16x16x32_bf16 a[212:215], v[80:83], v[40:43], 0\n"
"buffer_load_dwordx4 v[40:43], v253, s[44:47], s61 offen offset:2048 sc1\n"
"v_mfma_f32_16x16x32_bf16 a[212:215], v[116:119], v[44:47], a[212:215]\n"
"buffer_load_dwordx4 v[44:47], v253, s[44:47], s63 offen offset:0 sc1\n"
"s_waitcnt vmcnt(40) lgkmcnt(0)\n"
"s_barrier\n"
"v_mfma_f32_16x16x32_bf16 a[0:3], v[120:123], v[192:195], a[0:3]\n"
"s_mov_b32 s61, 36864\n"
"v_mfma_f32_16x16x32_bf16 a[0:3], v[156:159], v[196:199], a[0:3]\n"
"s_mov_b32 s62, 40960\n"
"v_mfma_f32_16x16x32_bf16 a[24:27], v[124:127], v[192:195], a[24:27]\n"
"s_mov_b32 s63, 45056\n"
"v_mfma_f32_16x16x32_bf16 a[24:27], v[160:163], v[196:199], a[24:27]\n"
"s_mov_b32 s59, 589824\n"
"ds_read_b64_tr_b16 v[48:49], v249 offset:36864\n"
"v_mfma_f32_16x16x32_bf16 a[48:51], v[128:131], v[192:195], a[48:51]\n"
"s_add_u32 m0, s60, 18432\n"
"v_mfma_f32_16x16x32_bf16 a[48:51], v[164:167], v[196:199], a[48:51]\n"
"buffer_load_dwordx4 v240, s[48:51], s59 offen offset:0 lds\n"
"v_mfma_f32_16x16x32_bf16 a[72:75], v[132:135], v[192:195], a[72:75]\n"
"ds_read_b64_tr_b16 v[50:51], v249 offset:41472\n"
"v_mfma_f32_16x16x32_bf16 a[72:75], v[168:171], v[196:199], a[72:75]\n"
"v_mfma_f32_16x16x32_bf16 a[96:99], v[136:139], v[192:195], a[96:99]\n"
"ds_read_b64_tr_b16 v[52:53], v249 offset:36896\n"
"v_mfma_f32_16x16x32_bf16 a[96:99], v[172:175], v[196:199], a[96:99]\n"
"v_mfma_f32_16x16x32_bf16 a[120:123], v[140:143], v[192:195], a[120:123]\n"
"buffer_load_dwordx4 v241, s[48:51], s59 offen offset:1024 lds\n"
"v_mfma_f32_16x16x32_bf16 a[120:123], v[176:179], v[196:199], a[120:123]\n"
"ds_read_b64_tr_b16 v[54:55], v249 offset:41504\n"
"v_mfma_f32_16x16x32_bf16 a[144:147], v[144:147], v[192:195], a[144:147]\n"
"v_mfma_f32_16x16x32_bf16 a[144:147], v[180:183], v[196:199], a[144:147]\n"
"ds_read_b64_tr_b16 v[56:57], v249 offset:36928\n"
"v_mfma_f32_16x16x32_bf16 a[168:171], v[148:151], v[192:195], a[168:171]\n"
"buffer_load_dwordx4 v242, s[48:51], s59 offen offset:2048 lds\n"
"v_mfma_f32_16x16x32_bf16 a[168:171], v[184:187], v[196:199], a[168:171]\n"
"ds_read_b64_tr_b16 v[58:59], v249 offset:41536\n"
"v_mfma_f32_16x16x32_bf16 a[192:195], v[152:155], v[192:195], a[192:195]\n"
"v_mfma_f32_16x16x32_bf16 a[192:195], v[188:191], v[196:199], a[192:195]\n"
"ds_read_b64_tr_b16 v[60:61], v249 offset:36960\n"
"v_mfma_f32_16x16x32_bf16 a[4:7], v[120:123], v[200:203], a[4:7]\n"
"buffer_load_dwordx4 v[192:195], v255, s[44:47], s61 offen offset:0 sc1\n"
"v_mfma_f32_16x16x32_bf16 a[4:7], v[156:159], v[204:207], a[4:7]\n"
"ds_read_b64_tr_b16 v[62:63], v249 offset:41568\n"
"v_mfma_f32_16x16x32_bf16 a[28:31], v[124:127], v[200:203], a[28:31]\n"
"v_mfma_f32_16x16x32_bf16 a[28:31], v[160:163], v[204:207], a[28:31]\n"
"ds_read_b64_tr_b16 v[64:65], v249 offset:36992\n"
"v_mfma_f32_16x16x32_bf16 a[52:55], v[128:131], v[200:203], a[52:55]\n"
"v_mfma_f32_16x16x32_bf16 a[52:55], v[164:167], v[204:207], a[52:55]\n"
"buffer_load_dwordx4 v243, s[48:51], s59 offen offset:3072 lds\n"
"v_mfma_f32_16x16x32_bf16 a[76:79], v[132:135], v[200:203], a[76:79]\n"
"ds_read_b64_tr_b16 v[66:67], v249 offset:41600\n"
"v_mfma_f32_16x16x32_bf16 a[76:79], v[168:171], v[204:207], a[76:79]\n"
"v_mfma_f32_16x16x32_bf16 a[100:103], v[136:139], v[200:203], a[100:103]\n"
"ds_read_b64_tr_b16 v[68:69], v249 offset:37024\n"
"v_mfma_f32_16x16x32_bf16 a[100:103], v[172:175], v[204:207], a[100:103]\n"
"buffer_load_dwordx4 v[196:199], v255, s[44:47], s62 offen offset:2048 sc1\n"
"v_mfma_f32_16x16x32_bf16 a[124:127], v[140:143], v[200:203], a[124:127]\n"
"ds_read_b64_tr_b16 v[70:71], v249 offset:41632\n"
"v_mfma_f32_16x16x32_bf16 a[124:127], v[176:179], v[204:207], a[124:127]\n"
"v_mfma_f32_16x16x32_bf16 a[148:151], v[144:147], v[200:203], a[148:151]\n"
"ds_read_b64_tr_b16 v[72:73], v249 offset:37056\n"
"v_mfma_f32_16x16x32_bf16 a[148:151], v[180:183], v[204:207], a[148:151]\n"
"s_add_u32 m0, s60, 22528\n"
"v_mfma_f32_16x16x32_bf16 a[172:175], v[148:151], v[200:203], a[172:175]\n"
"buffer_load_dwordx4 v244, s[48:51], s59 offen offset:0 lds\n"
"v_mfma_f32_16x16x32_bf16 a[172:175], v[184:187], v[204:207], a[172:175]\n"
"ds_read_b64_tr_b16 v[74:75], v249 offset:41664\n"
"v_mfma_f32_16x16x32_bf16 a[196:199], v[152:155], v[200:203], a[196:199]\n"
"v_mfma_f32_16x16x32_bf16 a[196:199], v[188:191], v[204:207], a[196:199]\n"
"ds_read_b64_tr_b16 v[76:77], v249 offset:37088\n"
"v_mfma_f32_16x16x32_bf16 a[8:11], v[120:123], v[208:211], a[8:11]\n"
"buffer_load_dwordx4 v[200:203], v255, s[44:47], s61 offen offset:1024 sc1\n"
"v_mfma_f32_16x16x32_bf16 a[8:11], v[156:159], v[212:215], a[8:11]\n"
"ds_read_b64_tr_b16 v[78:79], v249 offset:41696\n"
"v_mfma_f32_16x16x32_bf16 a[32:35], v[124:127], v[208:211], a[32:35]\n"
"v_mfma_f32_16x16x32_bf16 a[32:35], v[160:163], v[212:215], a[32:35]\n"
"ds_read_b64_tr_b16 v[80:81], v249 offset:37120\n"
"v_mfma_f32_16x16x32_bf16 a[56:59], v[128:131], v[208:211], a[56:59]\n"
"v_mfma_f32_16x16x32_bf16 a[56:59], v[164:167], v[212:215], a[56:59]\n"
"buffer_load_dwordx4 v245, s[48:51], s59 offen offset:1024 lds\n"
"v_mfma_f32_16x16x32_bf16 a[80:83], v[132:135], v[208:211], a[80:83]\n"
"ds_read_b64_tr_b16 v[82:83], v249 offset:41728\n"
"v_mfma_f32_16x16x32_bf16 a[80:83], v[168:171], v[212:215], a[80:83]\n"
"v_mfma_f32_16x16x32_bf16 a[104:107], v[136:139], v[208:211], a[104:107]\n"
"ds_read_b64_tr_b16 v[84:85], v249 offset:46080\n"
"v_mfma_f32_16x16x32_bf16 a[104:107], v[172:175], v[212:215], a[104:107]\n"
"buffer_load_dwordx4 v[204:207], v255, s[44:47], s62 offen offset:3072 sc1\n"
"v_mfma_f32_16x16x32_bf16 a[128:131], v[140:143], v[208:211], a[128:131]\n"
"ds_read_b64_tr_b16 v[86:87], v249 offset:50688\n"
"v_mfma_f32_16x16x32_bf16 a[128:131], v[176:179], v[212:215], a[128:131]\n"
"v_mfma_f32_16x16x32_bf16 a[152:155], v[144:147], v[208:211], a[152:155]\n"
"ds_read_b64_tr_b16 v[88:89], v249 offset:46112\n"
"v_mfma_f32_16x16x32_bf16 a[152:155], v[180:183], v[212:215], a[152:155]\n"
"v_mfma_f32_16x16x32_bf16 a[176:179], v[148:151], v[208:211], a[176:179]\n"
"buffer_load_dwordx4 v246, s[48:51], s59 offen offset:2048 lds\n"
"v_mfma_f32_16x16x32_bf16 a[176:179], v[184:187], v[212:215], a[176:179]\n"
"ds_read_b64_tr_b16 v[90:91], v249 offset:50720\n"
"v_mfma_f32_16x16x32_bf16 a[200:203], v[152:155], v[208:211], a[200:203]\n"
"v_mfma_f32_16x16x32_bf16 a[200:203], v[188:191], v[212:215], a[200:203]\n"
"ds_read_b64_tr_b16 v[92:93], v249 offset:46144\n"
"v_mfma_f32_16x16x32_bf16 a[12:15], v[120:123], v[216:219], a[12:15]\n"
"buffer_load_dwordx4 v[208:211], v255, s[44:47], s61 offen offset:2048 sc1\n"
"v_mfma_f32_16x16x32_bf16 a[12:15], v[156:159], v[220:223], a[12:15]\n"
"ds_read_b64_tr_b16 v[94:95], v249 offset:50752\n"
"v_mfma_f32_16x16x32_bf16 a[36:39], v[124:127], v[216:219], a[36:39]\n"
"v_mfma_f32_16x16x32_bf16 a[36:39], v[160:163], v[220:223], a[36:39]\n"
"ds_read_b64_tr_b16 v[96:97], v249 offset:46176\n"
"v_mfma_f32_16x16x32_bf16 a[60:63], v[128:131], v[216:219], a[60:63]\n"
"v_mfma_f32_16x16x32_bf16 a[60:63], v[164:167], v[220:223], a[60:63]\n"
"buffer_load_dwordx4 v247, s[48:51], s59 offen offset:3072 lds\n"
"v_mfma_f32_16x16x32_bf16 a[84:87], v[132:135], v[216:219], a[84:87]\n"
"ds_read_b64_tr_b16 v[98:99], v249 offset:50784\n"
"v_mfma_f32_16x16x32_bf16 a[84:87], v[168:171], v[220:223], a[84:87]\n"
"v_mfma_f32_16x16x32_bf16 a[108:111], v[136:139], v[216:219], a[108:111]\n"
"ds_read_b64_tr_b16 v[100:101], v249 offset:46208\n"
"v_mfma_f32_16x16x32_bf16 a[108:111], v[172:175], v[220:223], a[108:111]\n"
"buffer_load_dwordx4 v[212:215], v255, s[44:47], s63 offen offset:0 sc1\n"
"v_mfma_f32_16x16x32_bf16 a[132:135], v[140:143], v[216:219], a[132:135]\n"
"ds_read_b64_tr_b16 v[102:103], v249 offset:50816\n"
"v_mfma_f32_16x16x32_bf16 a[132:135], v[176:179], v[220:223], a[132:135]\n"
"v_mfma_f32_16x16x32_bf16 a[156:159], v[144:147], v[216:219], a[156:159]\n"
"ds_read_b64_tr_b16 v[104:105], v249 offset:46240\n"
"v_mfma_f32_16x16x32_bf16 a[156:159], v[180:183], v[220:223], a[156:159]\n"
"s_add_u32 m0, s60, 26624\n"
"v_mfma_f32_16x16x32_bf16 a[180:183], v[148:151], v[216:219], a[180:183]\n"
"buffer_load_dwordx4 v248, s[48:51], s59 offen offset:0 lds\n"
"v_mfma_f32_16x16x32_bf16 a[180:183], v[184:187], v[220:223], a[180:183]\n"
"ds_read_b64_tr_b16 v[106:107], v249 offset:50848\n"
"v_mfma_f32_16x16x32_bf16 a[204:207], v[152:155], v[216:219], a[204:207]\n"
"v_mfma_f32_16x16x32_bf16 a[204:207], v[188:191], v[220:223], a[204:207]\n"
"ds_read_b64_tr_b16 v[108:109], v249 offset:46272\n"
"v_mfma_f32_16x16x32_bf16 a[16:19], v[120:123], v[224:227], a[16:19]\n"
"buffer_load_dwordx4 v[216:219], v253, s[44:47], s61 offen offset:0 sc1\n"
"v_mfma_f32_16x16x32_bf16 a[16:19], v[156:159], v[228:231], a[16:19]\n"
"ds_read_b64_tr_b16 v[110:111], v249 offset:50880\n"
"v_mfma_f32_16x16x32_bf16 a[40:43], v[124:127], v[224:227], a[40:43]\n"
"v_mfma_f32_16x16x32_bf16 a[40:43], v[160:163], v[228:231], a[40:43]\n"
"ds_read_b64_tr_b16 v[112:113], v249 offset:46304\n"
"v_mfma_f32_16x16x32_bf16 a[64:67], v[128:131], v[224:227], a[64:67]\n"
"s_add_u32 s75, s67, 16384\n"
"v_mfma_f32_16x16x32_bf16 a[64:67], v[164:167], v[228:231], a[64:67]\n"
"ds_read_b64_tr_b16 v[114:115], v249 offset:50912\n"
"buffer_store_dwordx4 a[236:239], v250, s[52:55], s75 offen offset:3072 sc1\n"
"v_mfma_f32_16x16x32_bf16 a[88:91], v[132:135], v[224:227], a[88:91]\n"
"v_mfma_f32_16x16x32_bf16 a[88:91], v[168:171], v[228:231], a[88:91]\n"
"ds_read_b64_tr_b16 v[116:117], v249 offset:46336\n"
"v_mfma_f32_16x16x32_bf16 a[112:115], v[136:139], v[224:227], a[112:115]\n"
"v_mfma_f32_16x16x32_bf16 a[112:115], v[172:175], v[228:231], a[112:115]\n"
"buffer_load_dwordx4 v[220:223], v253, s[44:47], s62 offen offset:2048 sc1\n"
"s_add_u32 s75, s67, 20480\n"
"v_mfma_f32_16x16x32_bf16 a[136:139], v[140:143], v[224:227], a[136:139]\n"
"ds_read_b64_tr_b16 v[118:119], v249 offset:50944\n"
"buffer_store_dwordx4 a[240:243], v250, s[52:55], s75 offen offset:0 sc1\n"
"v_mfma_f32_16x16x32_bf16 a[136:139], v[176:179], v[228:231], a[136:139]\n"
"v_mfma_f32_16x16x32_bf16 a[160:163], v[144:147], v[224:227], a[160:163]\n"
"v_mfma_f32_16x16x32_bf16 a[160:163], v[180:183], v[228:231], a[160:163]\n"
"v_mfma_f32_16x16x32_bf16 a[184:187], v[148:151], v[224:227], a[184:187]\n"
"s_add_u32 s75, s67, 20480\n"
"v_mfma_f32_16x16x32_bf16 a[184:187], v[184:187], v[228:231], a[184:187]\n"
"buffer_store_dwordx4 a[244:247], v250, s[52:55], s75 offen offset:1024 sc1\n"
"v_mfma_f32_16x16x32_bf16 a[208:211], v[152:155], v[224:227], a[208:211]\n"
"v_mfma_f32_16x16x32_bf16 a[208:211], v[188:191], v[228:231], a[208:211]\n"
"v_mfma_f32_16x16x32_bf16 a[20:23], v[120:123], v[232:235], a[20:23]\n"
"buffer_load_dwordx4 v[224:227], v253, s[44:47], s61 offen offset:1024 sc1\n"
"v_mfma_f32_16x16x32_bf16 a[20:23], v[156:159], v[236:239], a[20:23]\n"
"s_add_u32 s75, s67, 20480\n"
"v_mfma_f32_16x16x32_bf16 a[44:47], v[124:127], v[232:235], a[44:47]\n"
"buffer_store_dwordx4 a[248:251], v250, s[52:55], s75 offen offset:2048 sc1\n"
"v_mfma_f32_16x16x32_bf16 a[44:47], v[160:163], v[236:239], a[44:47]\n"
"v_mfma_f32_16x16x32_bf16 a[68:71], v[128:131], v[232:235], a[68:71]\n"
"v_mfma_f32_16x16x32_bf16 a[68:71], v[164:167], v[236:239], a[68:71]\n"
"v_mfma_f32_16x16x32_bf16 a[92:95], v[132:135], v[232:235], a[92:95]\n"
"s_add_u32 s75, s67, 20480\n"
"v_mfma_f32_16x16x32_bf16 a[92:95], v[168:171], v[236:239], a[92:95]\n"
"buffer_store_dwordx4 a[252:255], v250, s[52:55], s75 offen offset:3072 sc1\n"
"v_mfma_f32_16x16x32_bf16 a[116:119], v[136:139], v[232:235], a[116:119]\n"
"v_mfma_f32_16x16x32_bf16 a[116:119], v[172:175], v[236:239], a[116:119]\n"
"s_waitcnt vmcnt(62)\n"
"buffer_load_dwordx4 v[228:231], v253, s[44:47], s62 offen offset:3072 sc1\n"
"v_mfma_f32_16x16x32_bf16 a[140:143], v[140:143], v[232:235], a[140:143]\n"
"v_mfma_f32_16x16x32_bf16 a[140:143], v[176:179], v[236:239], a[140:143]\n"
"v_mfma_f32_16x16x32_bf16 a[164:167], v[144:147], v[232:235], a[164:167]\n"
"v_mfma_f32_16x16x32_bf16 a[164:167], v[180:183], v[236:239], a[164:167]\n"
"v_mfma_f32_16x16x32_bf16 a[188:191], v[148:151], v[232:235], a[188:191]\n"
"v_mfma_f32_16x16x32_bf16 a[188:191], v[184:187], v[236:239], a[188:191]\n"
"v_mfma_f32_16x16x32_bf16 a[212:215], v[152:155], v[232:235], a[212:215]\n"
"s_waitcnt vmcnt(62)\n"
"buffer_load_dwordx4 v[232:235], v253, s[44:47], s61 offen offset:2048 sc1\n"
"v_mfma_f32_16x16x32_bf16 a[212:215], v[188:191], v[236:239], a[212:215]\n"
"s_waitcnt vmcnt(62)\n"
"buffer_load_dwordx4 v[236:239], v253, s[44:47], s63 offen offset:0 sc1\n"
"s_mov_b32 s56, 0xc7654ba9\n"
"s_mov_b32 s57, 0xfed\n"
"s_mov_b32 s70, 0x7654ba98\n"
"s_mov_b32 s71, 0xfedc\n"
"s_mov_b32 s65, 2\n"
".Lf_op:\n"
"s_waitcnt vmcnt(21) lgkmcnt(0)\n"
"s_barrier\n"
"v_mfma_f32_16x16x32_bf16 a[0:3], v[48:51], v[0:3], a[0:3]\n"
"v_mfma_f32_16x16x32_bf16 a[0:3], v[84:87], v[4:7], a[0:3]\n"
"s_and_b32 s59, s56, 15\n"
"v_mfma_f32_16x16x32_bf16 a[24:27], v[52:55], v[0:3], a[24:27]\n"
"s_lshr_b64 s[56:57], s[56:57], 4\n"
"v_mfma_f32_16x16x32_bf16 a[24:27], v[88:91], v[4:7], a[24:27]\n"
"s_mul_i32 s59, s59, 73728\n"
"ds_read_b64_tr_b16 v[120:121], v249 offset:0\n"
"v_mfma_f32_16x16x32_bf16 a[48:51], v[56:59], v[0:3], a[48:51]\n"
"s_add_u32 m0, s60, 36864\n"
"v_mfma_f32_16x16x32_bf16 a[48:51], v[92:95], v[4:7], a[48:51]\n"
"buffer_load_dwordx4 v240, s[48:51], s59 offen offset:0 lds\n"
"v_mfma_f32_16x16x32_bf16 a[72:75], v[60:63], v[0:3], a[72:75]\n"
"s_and_b32 s61, s70, 15\n"
"s_lshr_b64 s[70:71], s[70:71], 4\n"
"ds_read_b64_tr_b16 v[122:123], v249 offset:4608\n"
"v_mfma_f32_16x16x32_bf16 a[72:75], v[96:99], v[4:7], a[72:75]\n"
"s_mul_i32 s61, s61, 12288\n"
"s_add_u32 s62, s61, 4096\n"
"v_mfma_f32_16x16x32_bf16 a[96:99], v[64:67], v[0:3], a[96:99]\n"
"s_add_u32 s63, s61, 8192\n"
"ds_read_b64_tr_b16 v[124:125], v249 offset:32\n"
"v_mfma_f32_16x16x32_bf16 a[96:99], v[100:103], v[4:7], a[96:99]\n"
"v_mfma_f32_16x16x32_bf16 a[120:123], v[68:71], v[0:3], a[120:123]\n"
"buffer_load_dwordx4 v241, s[48:51], s59 offen offset:1024 lds\n"
"v_mfma_f32_16x16x32_bf16 a[120:123], v[104:107], v[4:7], a[120:123]\n"
"ds_read_b64_tr_b16 v[126:127], v249 offset:4640\n"
"v_mfma_f32_16x16x32_bf16 a[144:147], v[72:75], v[0:3], a[144:147]\n"
"v_mfma_f32_16x16x32_bf16 a[144:147], v[108:111], v[4:7], a[144:147]\n"
"ds_read_b64_tr_b16 v[128:129], v249 offset:64\n"
"v_mfma_f32_16x16x32_bf16 a[168:171], v[76:79], v[0:3], a[168:171]\n"
"buffer_load_dwordx4 v242, s[48:51], s59 offen offset:2048 lds\n"
"v_mfma_f32_16x16x32_bf16 a[168:171], v[112:115], v[4:7], a[168:171]\n"
"ds_read_b64_tr_b16 v[130:131], v249 offset:4672\n"
"v_mfma_f32_16x16x32_bf16 a[192:195], v[80:83], v[0:3], a[192:195]\n"
"v_mfma_f32_16x16x32_bf16 a[192:195], v[116:119], v[4:7], a[192:195]\n"
"ds_read_b64_tr_b16 v[132:133], v249 offset:96\n"
"v_mfma_f32_16x16x32_bf16 a[4:7], v[48:51], v[8:11], a[4:7]\n"
"buffer_load_dwordx4 v[0:3], v255, s[44:47], s61 offen offset:0 sc1\n"
"v_mfma_f32_16x16x32_bf16 a[4:7], v[84:87], v[12:15], a[4:7]\n"
"ds_read_b64_tr_b16 v[134:135], v249 offset:4704\n"
"v_mfma_f32_16x16x32_bf16 a[28:31], v[52:55], v[8:11], a[28:31]\n"
"v_mfma_f32_16x16x32_bf16 a[28:31], v[88:91], v[12:15], a[28:31]\n"
"ds_read_b64_tr_b16 v[136:137], v249 offset:128\n"
"v_mfma_f32_16x16x32_bf16 a[52:55], v[56:59], v[8:11], a[52:55]\n"
"v_mfma_f32_16x16x32_bf16 a[52:55], v[92:95], v[12:15], a[52:55]\n"
"buffer_load_dwordx4 v243, s[48:51], s59 offen offset:3072 lds\n"
"v_mfma_f32_16x16x32_bf16 a[76:79], v[60:63], v[8:11], a[76:79]\n"
"ds_read_b64_tr_b16 v[138:139], v249 offset:4736\n"
"v_mfma_f32_16x16x32_bf16 a[76:79], v[96:99], v[12:15], a[76:79]\n"
"v_mfma_f32_16x16x32_bf16 a[100:103], v[64:67], v[8:11], a[100:103]\n"
"ds_read_b64_tr_b16 v[140:141], v249 offset:160\n"
"v_mfma_f32_16x16x32_bf16 a[100:103], v[100:103], v[12:15], a[100:103]\n"
"buffer_load_dwordx4 v[4:7], v255, s[44:47], s62 offen offset:2048 sc1\n"
"v_mfma_f32_16x16x32_bf16 a[124:127], v[68:71], v[8:11], a[124:127]\n"
"ds_read_b64_tr_b16 v[142:143], v249 offset:4768\n"
"v_mfma_f32_16x16x32_bf16 a[124:127], v[104:107], v[12:15], a[124:127]\n"
"v_mfma_f32_16x16x32_bf16 a[148:151], v[72:75], v[8:11], a[148:151]\n"
"ds_read_b64_tr_b16 v[144:145], v249 offset:192\n"
"v_mfma_f32_16x16x32_bf16 a[148:151], v[108:111], v[12:15], a[148:151]\n"
"s_add_u32 m0, s60, 40960\n"
"v_mfma_f32_16x16x32_bf16 a[172:175], v[76:79], v[8:11], a[172:175]\n"
"buffer_load_dwordx4 v244, s[48:51], s59 offen offset:0 lds\n"
"v_mfma_f32_16x16x32_bf16 a[172:175], v[112:115], v[12:15], a[172:175]\n"
"ds_read_b64_tr_b16 v[146:147], v249 offset:4800\n"
"v_mfma_f32_16x16x32_bf16 a[196:199], v[80:83], v[8:11], a[196:199]\n"
"v_mfma_f32_16x16x32_bf16 a[196:199], v[116:119], v[12:15], a[196:199]\n"
"ds_read_b64_tr_b16 v[148:149], v249 offset:224\n"
"v_mfma_f32_16x16x32_bf16 a[8:11], v[48:51], v[16:19], a[8:11]\n"
"buffer_load_dwordx4 v[8:11], v255, s[44:47], s61 offen offset:1024 sc1\n"
"v_mfma_f32_16x16x32_bf16 a[8:11], v[84:87], v[20:23], a[8:11]\n"
"ds_read_b64_tr_b16 v[150:151], v249 offset:4832\n"
"v_mfma_f32_16x16x32_bf16 a[32:35], v[52:55], v[16:19], a[32:35]\n"
"v_mfma_f32_16x16x32_bf16 a[32:35], v[88:91], v[20:23], a[32:35]\n"
"ds_read_b64_tr_b16 v[152:153], v249 offset:256\n"
"v_mfma_f32_16x16x32_bf16 a[56:59], v[56:59], v[16:19], a[56:59]\n"
"v_mfma_f32_16x16x32_bf16 a[56:59], v[92:95], v[20:23], a[56:59]\n"
"buffer_load_dwordx4 v245, s[48:51], s59 offen offset:1024 lds\n"
"v_mfma_f32_16x16x32_bf16 a[80:83], v[60:63], v[16:19], a[80:83]\n"
"ds_read_b64_tr_b16 v[154:155], v249 offset:4864\n"
"v_mfma_f32_16x16x32_bf16 a[80:83], v[96:99], v[20:23], a[80:83]\n"
"v_mfma_f32_16x16x32_bf16 a[104:107], v[64:67], v[16:19], a[104:107]\n"
"ds_read_b64_tr_b16 v[156:157], v249 offset:9216\n"
"v_mfma_f32_16x16x32_bf16 a[104:107], v[100:103], v[20:23], a[104:107]\n"
"buffer_load_dwordx4 v[12:15], v255, s[44:47], s62 offen offset:3072 sc1\n"
"v_mfma_f32_16x16x32_bf16 a[128:131], v[68:71], v[16:19], a[128:131]\n"
"ds_read_b64_tr_b16 v[158:159], v249 offset:13824\n"
"v_mfma_f32_16x16x32_bf16 a[128:131], v[104:107], v[20:23], a[128:131]\n"
"v_mfma_f32_16x16x32_bf16 a[152:155], v[72:75], v[16:19], a[152:155]\n"
"ds_read_b64_tr_b16 v[160:161], v249 offset:9248\n"
"v_mfma_f32_16x16x32_bf16 a[152:155], v[108:111], v[20:23], a[152:155]\n"
"v_mfma_f32_16x16x32_bf16 a[176:179], v[76:79], v[16:19], a[176:179]\n"
"buffer_load_dwordx4 v246, s[48:51], s59 offen offset:2048 lds\n"
"v_mfma_f32_16x16x32_bf16 a[176:179], v[112:115], v[20:23], a[176:179]\n"
"ds_read_b64_tr_b16 v[162:163], v249 offset:13856\n"
"v_mfma_f32_16x16x32_bf16 a[200:203], v[80:83], v[16:19], a[200:203]\n"
"v_mfma_f32_16x16x32_bf16 a[200:203], v[116:119], v[20:23], a[200:203]\n"
"ds_read_b64_tr_b16 v[164:165], v249 offset:9280\n"
"v_mfma_f32_16x16x32_bf16 a[12:15], v[48:51], v[24:27], a[12:15]\n"
"buffer_load_dwordx4 v[16:19], v255, s[44:47], s61 offen offset:2048 sc1\n"
"v_mfma_f32_16x16x32_bf16 a[12:15], v[84:87], v[28:31], a[12:15]\n"
"ds_read_b64_tr_b16 v[166:167], v249 offset:13888\n"
"v_mfma_f32_16x16x32_bf16 a[36:39], v[52:55], v[24:27], a[36:39]\n"
"v_mfma_f32_16x16x32_bf16 a[36:39], v[88:91], v[28:31], a[36:39]\n"
"ds_read_b64_tr_b16 v[168:169], v249 offset:9312\n"
"v_mfma_f32_16x16x32_bf16 a[60:63], v[56:59], v[24:27], a[60:63]\n"
"v_mfma_f32_16x16x32_bf16 a[60:63], v[92:95], v[28:31], a[60:63]\n"
"buffer_load_dwordx4 v247, s[48:51], s59 offen offset:3072 lds\n"
"v_mfma_f32_16x16x32_bf16 a[84:87], v[60:63], v[24:27], a[84:87]\n"
"ds_read_b64_tr_b16 v[170:171], v249 offset:13920\n"
"v_mfma_f32_16x16x32_bf16 a[84:87], v[96:99], v[28:31], a[84:87]\n"
"v_mfma_f32_16x16x32_bf16 a[108:111], v[64:67], v[24:27], a[108:111]\n"
"ds_read_b64_tr_b16 v[172:173], v249 offset:9344\n"
"v_mfma_f32_16x16x32_bf16 a[108:111], v[100:103], v[28:31], a[108:111]\n"
"buffer_load_dwordx4 v[20:23], v255, s[44:47], s63 offen offset:0 sc1\n"
"v_mfma_f32_16x16x32_bf16 a[132:135], v[68:71], v[24:27], a[132:135]\n"
"ds_read_b64_tr_b16 v[174:175], v249 offset:13952\n"
"v_mfma_f32_16x16x32_bf16 a[132:135], v[104:107], v[28:31], a[132:135]\n"
"v_mfma_f32_16x16x32_bf16 a[156:159], v[72:75], v[24:27], a[156:159]\n"
"ds_read_b64_tr_b16 v[176:177], v249 offset:9376\n"
"v_mfma_f32_16x16x32_bf16 a[156:159], v[108:111], v[28:31], a[156:159]\n"
"s_add_u32 m0, s60, 45056\n"
"v_mfma_f32_16x16x32_bf16 a[180:183], v[76:79], v[24:27], a[180:183]\n"
"buffer_load_dwordx4 v248, s[48:51], s59 offen offset:0 lds\n"
"v_mfma_f32_16x16x32_bf16 a[180:183], v[112:115], v[28:31], a[180:183]\n"
"ds_read_b64_tr_b16 v[178:179], v249 offset:13984\n"
"v_mfma_f32_16x16x32_bf16 a[204:207], v[80:83], v[24:27], a[204:207]\n"
"v_mfma_f32_16x16x32_bf16 a[204:207], v[116:119], v[28:31], a[204:207]\n"
"ds_read_b64_tr_b16 v[180:181], v249 offset:9408\n"
"v_mfma_f32_16x16x32_bf16 a[16:19], v[48:51], v[32:35], a[16:19]\n"
"buffer_load_dwordx4 v[24:27], v253, s[44:47], s61 offen offset:0 sc1\n"
"v_mfma_f32_16x16x32_bf16 a[16:19], v[84:87], v[36:39], a[16:19]\n"
"ds_read_b64_tr_b16 v[182:183], v249 offset:14016\n"
"v_mfma_f32_16x16x32_bf16 a[40:43], v[52:55], v[32:35], a[40:43]\n"
"v_mfma_f32_16x16x32_bf16 a[40:43], v[88:91], v[36:39], a[40:43]\n"
"ds_read_b64_tr_b16 v[184:185], v249 offset:9440\n"
"v_mfma_f32_16x16x32_bf16 a[64:67], v[56:59], v[32:35], a[64:67]\n"
"v_mfma_f32_16x16x32_bf16 a[64:67], v[92:95], v[36:39], a[64:67]\n"
"ds_read_b64_tr_b16 v[186:187], v249 offset:14048\n"
"v_mfma_f32_16x16x32_bf16 a[88:91], v[60:63], v[32:35], a[88:91]\n"
"v_mfma_f32_16x16x32_bf16 a[88:91], v[96:99], v[36:39], a[88:91]\n"
"ds_read_b64_tr_b16 v[188:189], v249 offset:9472\n"
"v_mfma_f32_16x16x32_bf16 a[112:115], v[64:67], v[32:35], a[112:115]\n"
"v_mfma_f32_16x16x32_bf16 a[112:115], v[100:103], v[36:39], a[112:115]\n"
"buffer_load_dwordx4 v[28:31], v253, s[44:47], s62 offen offset:2048 sc1\n"
"v_mfma_f32_16x16x32_bf16 a[136:139], v[68:71], v[32:35], a[136:139]\n"
"ds_read_b64_tr_b16 v[190:191], v249 offset:14080\n"
"v_mfma_f32_16x16x32_bf16 a[136:139], v[104:107], v[36:39], a[136:139]\n"
"v_mfma_f32_16x16x32_bf16 a[160:163], v[72:75], v[32:35], a[160:163]\n"
"v_mfma_f32_16x16x32_bf16 a[160:163], v[108:111], v[36:39], a[160:163]\n"
"v_mfma_f32_16x16x32_bf16 a[184:187], v[76:79], v[32:35], a[184:187]\n"
"v_mfma_f32_16x16x32_bf16 a[184:187], v[112:115], v[36:39], a[184:187]\n"
"v_mfma_f32_16x16x32_bf16 a[208:211], v[80:83], v[32:35], a[208:211]\n"
"v_mfma_f32_16x16x32_bf16 a[208:211], v[116:119], v[36:39], a[208:211]\n"
"v_mfma_f32_16x16x32_bf16 a[20:23], v[48:51], v[40:43], a[20:23]\n"
"buffer_load_dwordx4 v[32:35], v253, s[44:47], s61 offen offset:1024 sc1\n"
"v_mfma_f32_16x16x32_bf16 a[20:23], v[84:87], v[44:47], a[20:23]\n"
"v_mfma_f32_16x16x32_bf16 a[44:47], v[52:55], v[40:43], a[44:47]\n"
"v_mfma_f32_16x16x32_bf16 a[44:47], v[88:91], v[44:47], a[44:47]\n"
"v_mfma_f32_16x16x32_bf16 a[68:71], v[56:59], v[40:43], a[68:71]\n"
"v_mfma_f32_16x16x32_bf16 a[68:71], v[92:95], v[44:47], a[68:71]\n"
"v_mfma_f32_16x16x32_bf16 a[92:95], v[60:63], v[40:43], a[92:95]\n"
"v_mfma_f32_16x16x32_bf16 a[92:95], v[96:99], v[44:47], a[92:95]\n"
"v_mfma_f32_16x16x32_bf16 a[116:119], v[64:67], v[40:43], a[116:119]\n"
"v_mfma_f32_16x16x32_bf16 a[116:119], v[100:103], v[44:47], a[116:119]\n"
"buffer_load_dwordx4 v[36:39], v253, s[44:47], s62 offen offset:3072 sc1\n"
"v_mfma_f32_16x16x32_bf16 a[140:143], v[68:71], v[40:43], a[140:143]\n"
"v_mfma_f32_16x16x32_bf16 a[140:143], v[104:107], v[44:47], a[140:143]\n"
"v_mfma_f32_16x16x32_bf16 a[164:167], v[72:75], v[40:43], a[164:167]\n"
"v_mfma_f32_16x16x32_bf16 a[164:167], v[108:111], v[44:47], a[164:167]\n"
"v_mfma_f32_16x16x32_bf16 a[188:191], v[76:79], v[40:43], a[188:191]\n"
"v_mfma_f32_16x16x32_bf16 a[188:191], v[112:115], v[44:47], a[188:191]\n"
"v_mfma_f32_16x16x32_bf16 a[212:215], v[80:83], v[40:43], a[212:215]\n"
"buffer_load_dwordx4 v[40:43], v253, s[44:47], s61 offen offset:2048 sc1\n"
"v_mfma_f32_16x16x32_bf16 a[212:215], v[116:119], v[44:47], a[212:215]\n"
"buffer_load_dwordx4 v[44:47], v253, s[44:47], s63 offen offset:0 sc1\n"
"s_waitcnt vmcnt(24)\n"
"s_add_u32 s73, s84, 2\n"
"v_mov_b32 v251, s73\n"
"s_waitcnt vmcnt(21) lgkmcnt(0)\n"
"s_barrier\n"
"v_mfma_f32_16x16x32_bf16 a[0:3], v[120:123], v[192:195], a[0:3]\n"
"v_mfma_f32_16x16x32_bf16 a[0:3], v[156:159], v[196:199], a[0:3]\n"
"s_and_b32 s59, s56, 15\n"
"global_store_dword v252, v251, %15 sc1\n"
"v_mfma_f32_16x16x32_bf16 a[24:27], v[124:127], v[192:195], a[24:27]\n"
"s_lshr_b64 s[56:57], s[56:57], 4\n"
"v_mfma_f32_16x16x32_bf16 a[24:27], v[160:163], v[196:199], a[24:27]\n"
"s_mul_i32 s59, s59, 73728\n"
"ds_read_b64_tr_b16 v[48:49], v249 offset:18432\n"
"v_mfma_f32_16x16x32_bf16 a[48:51], v[128:131], v[192:195], a[48:51]\n"
"s_add_u32 m0, s60, 0\n"
"v_mfma_f32_16x16x32_bf16 a[48:51], v[164:167], v[196:199], a[48:51]\n"
"buffer_load_dwordx4 v240, s[48:51], s59 offen offset:0 lds\n"
"v_mfma_f32_16x16x32_bf16 a[72:75], v[132:135], v[192:195], a[72:75]\n"
"s_and_b32 s61, s70, 15\n"
"s_lshr_b64 s[70:71], s[70:71], 4\n"
"ds_read_b64_tr_b16 v[50:51], v249 offset:23040\n"
"v_mfma_f32_16x16x32_bf16 a[72:75], v[168:171], v[196:199], a[72:75]\n"
"s_mul_i32 s61, s61, 12288\n"
"s_add_u32 s62, s61, 4096\n"
"v_mfma_f32_16x16x32_bf16 a[96:99], v[136:139], v[192:195], a[96:99]\n"
"s_add_u32 s63, s61, 8192\n"
"ds_read_b64_tr_b16 v[52:53], v249 offset:18464\n"
"v_mfma_f32_16x16x32_bf16 a[96:99], v[172:175], v[196:199], a[96:99]\n"
"v_mfma_f32_16x16x32_bf16 a[120:123], v[140:143], v[192:195], a[120:123]\n"
"buffer_load_dwordx4 v241, s[48:51], s59 offen offset:1024 lds\n"
"v_mfma_f32_16x16x32_bf16 a[120:123], v[176:179], v[196:199], a[120:123]\n"
"ds_read_b64_tr_b16 v[54:55], v249 offset:23072\n"
"v_mfma_f32_16x16x32_bf16 a[144:147], v[144:147], v[192:195], a[144:147]\n"
"v_mfma_f32_16x16x32_bf16 a[144:147], v[180:183], v[196:199], a[144:147]\n"
"ds_read_b64_tr_b16 v[56:57], v249 offset:18496\n"
"v_mfma_f32_16x16x32_bf16 a[168:171], v[148:151], v[192:195], a[168:171]\n"
"buffer_load_dwordx4 v242, s[48:51], s59 offen offset:2048 lds\n"
"v_mfma_f32_16x16x32_bf16 a[168:171], v[184:187], v[196:199], a[168:171]\n"
"ds_read_b64_tr_b16 v[58:59], v249 offset:23104\n"
"v_mfma_f32_16x16x32_bf16 a[192:195], v[152:155], v[192:195], a[192:195]\n"
"v_mfma_f32_16x16x32_bf16 a[192:195], v[188:191], v[196:199], a[192:195]\n"
"ds_read_b64_tr_b16 v[60:61], v249 offset:18528\n"
"v_mfma_f32_16x16x32_bf16 a[4:7], v[120:123], v[200:203], a[4:7]\n"
"buffer_load_dwordx4 v[192:195], v255, s[44:47], s61 offen offset:0 sc1\n"
"v_mfma_f32_16x16x32_bf16 a[4:7], v[156:159], v[204:207], a[4:7]\n"
"ds_read_b64_tr_b16 v[62:63], v249 offset:23136\n"
"v_mfma_f32_16x16x32_bf16 a[28:31], v[124:127], v[200:203], a[28:31]\n"
"v_mfma_f32_16x16x32_bf16 a[28:31], v[160:163], v[204:207], a[28:31]\n"
"ds_read_b64_tr_b16 v[64:65], v249 offset:18560\n"
"v_mfma_f32_16x16x32_bf16 a[52:55], v[128:131], v[200:203], a[52:55]\n"
"v_mfma_f32_16x16x32_bf16 a[52:55], v[164:167], v[204:207], a[52:55]\n"
"buffer_load_dwordx4 v243, s[48:51], s59 offen offset:3072 lds\n"
"v_mfma_f32_16x16x32_bf16 a[76:79], v[132:135], v[200:203], a[76:79]\n"
"ds_read_b64_tr_b16 v[66:67], v249 offset:23168\n"
"v_mfma_f32_16x16x32_bf16 a[76:79], v[168:171], v[204:207], a[76:79]\n"
"v_mfma_f32_16x16x32_bf16 a[100:103], v[136:139], v[200:203], a[100:103]\n"
"ds_read_b64_tr_b16 v[68:69], v249 offset:18592\n"
"v_mfma_f32_16x16x32_bf16 a[100:103], v[172:175], v[204:207], a[100:103]\n"
"buffer_load_dwordx4 v[196:199], v255, s[44:47], s62 offen offset:2048 sc1\n"
"v_mfma_f32_16x16x32_bf16 a[124:127], v[140:143], v[200:203], a[124:127]\n"
"ds_read_b64_tr_b16 v[70:71], v249 offset:23200\n"
"v_mfma_f32_16x16x32_bf16 a[124:127], v[176:179], v[204:207], a[124:127]\n"
"v_mfma_f32_16x16x32_bf16 a[148:151], v[144:147], v[200:203], a[148:151]\n"
"ds_read_b64_tr_b16 v[72:73], v249 offset:18624\n"
"v_mfma_f32_16x16x32_bf16 a[148:151], v[180:183], v[204:207], a[148:151]\n"
"s_add_u32 m0, s60, 4096\n"
"v_mfma_f32_16x16x32_bf16 a[172:175], v[148:151], v[200:203], a[172:175]\n"
"buffer_load_dwordx4 v244, s[48:51], s59 offen offset:0 lds\n"
"v_mfma_f32_16x16x32_bf16 a[172:175], v[184:187], v[204:207], a[172:175]\n"
"ds_read_b64_tr_b16 v[74:75], v249 offset:23232\n"
"v_mfma_f32_16x16x32_bf16 a[196:199], v[152:155], v[200:203], a[196:199]\n"
"v_mfma_f32_16x16x32_bf16 a[196:199], v[188:191], v[204:207], a[196:199]\n"
"ds_read_b64_tr_b16 v[76:77], v249 offset:18656\n"
"v_mfma_f32_16x16x32_bf16 a[8:11], v[120:123], v[208:211], a[8:11]\n"
"buffer_load_dwordx4 v[200:203], v255, s[44:47], s61 offen offset:1024 sc1\n"
"v_mfma_f32_16x16x32_bf16 a[8:11], v[156:159], v[212:215], a[8:11]\n"
"ds_read_b64_tr_b16 v[78:79], v249 offset:23264\n"
"v_mfma_f32_16x16x32_bf16 a[32:35], v[124:127], v[208:211], a[32:35]\n"
"v_mfma_f32_16x16x32_bf16 a[32:35], v[160:163], v[212:215], a[32:35]\n"
"ds_read_b64_tr_b16 v[80:81], v249 offset:18688\n"
"v_mfma_f32_16x16x32_bf16 a[56:59], v[128:131], v[208:211], a[56:59]\n"
"v_mfma_f32_16x16x32_bf16 a[56:59], v[164:167], v[212:215], a[56:59]\n"
"buffer_load_dwordx4 v245, s[48:51], s59 offen offset:1024 lds\n"
"v_mfma_f32_16x16x32_bf16 a[80:83], v[132:135], v[208:211], a[80:83]\n"
"ds_read_b64_tr_b16 v[82:83], v249 offset:23296\n"
"v_mfma_f32_16x16x32_bf16 a[80:83], v[168:171], v[212:215], a[80:83]\n"
"v_mfma_f32_16x16x32_bf16 a[104:107], v[136:139], v[208:211], a[104:107]\n"
"ds_read_b64_tr_b16 v[84:85], v249 offset:27648\n"
"v_mfma_f32_16x16x32_bf16 a[104:107], v[172:175], v[212:215], a[104:107]\n"
"buffer_load_dwordx4 v[204:207], v255, s[44:47], s62 offen offset:3072 sc1\n"
"v_mfma_f32_16x16x32_bf16 a[128:131], v[140:143], v[208:211], a[128:131]\n"
"ds_read_b64_tr_b16 v[86:87], v249 offset:32256\n"
"v_mfma_f32_16x16x32_bf16 a[128:131], v[176:179], v[212:215], a[128:131]\n"
"v_mfma_f32_16x16x32_bf16 a[152:155], v[144:147], v[208:211], a[152:155]\n"
"ds_read_b64_tr_b16 v[88:89], v249 offset:27680\n"
"v_mfma_f32_16x16x32_bf16 a[152:155], v[180:183], v[212:215], a[152:155]\n"
"v_mfma_f32_16x16x32_bf16 a[176:179], v[148:151], v[208:211], a[176:179]\n"
"buffer_load_dwordx4 v246, s[48:51], s59 offen offset:2048 lds\n"
"v_mfma_f32_16x16x32_bf16 a[176:179], v[184:187], v[212:215], a[176:179]\n"
"ds_read_b64_tr_b16 v[90:91], v249 offset:32288\n"
"v_mfma_f32_16x16x32_bf16 a[200:203], v[152:155], v[208:211], a[200:203]\n"
"v_mfma_f32_16x16x32_bf16 a[200:203], v[188:191], v[212:215], a[200:203]\n"
"ds_read_b64_tr_b16 v[92:93], v249 offset:27712\n"
"v_mfma_f32_16x16x32_bf16 a[12:15], v[120:123], v[216:219], a[12:15]\n"
"buffer_load_dwordx4 v[208:211], v255, s[44:47], s61 offen offset:2048 sc1\n"
"v_mfma_f32_16x16x32_bf16 a[12:15], v[156:159], v[220:223], a[12:15]\n"
"ds_read_b64_tr_b16 v[94:95], v249 offset:32320\n"
"v_mfma_f32_16x16x32_bf16 a[36:39], v[124:127], v[216:219], a[36:39]\n"
"v_mfma_f32_16x16x32_bf16 a[36:39], v[160:163], v[220:223], a[36:39]\n"
"ds_read_b64_tr_b16 v[96:97], v249 offset:27744\n"
"v_mfma_f32_16x16x32_bf16 a[60:63], v[128:131], v[216:219], a[60:63]\n"
"v_mfma_f32_16x16x32_bf16 a[60:63], v[164:167], v[220:223], a[60:63]\n"
"buffer_load_dwordx4 v247, s[48:51], s59 offen offset:3072 lds\n"
"v_mfma_f32_16x16x32_bf16 a[84:87], v[132:135], v[216:219], a[84:87]\n"
"ds_read_b64_tr_b16 v[98:99], v249 offset:32352\n"
"v_mfma_f32_16x16x32_bf16 a[84:87], v[168:171], v[220:223], a[84:87]\n"
"v_mfma_f32_16x16x32_bf16 a[108:111], v[136:139], v[216:219], a[108:111]\n"
"ds_read_b64_tr_b16 v[100:101], v249 offset:27776\n"
"v_mfma_f32_16x16x32_bf16 a[108:111], v[172:175], v[220:223], a[108:111]\n"
"buffer_load_dwordx4 v[212:215], v255, s[44:47], s63 offen offset:0 sc1\n"
"v_mfma_f32_16x16x32_bf16 a[132:135], v[140:143], v[216:219], a[132:135]\n"
"ds_read_b64_tr_b16 v[102:103], v249 offset:32384\n"
"v_mfma_f32_16x16x32_bf16 a[132:135], v[176:179], v[220:223], a[132:135]\n"
"v_mfma_f32_16x16x32_bf16 a[156:159], v[144:147], v[216:219], a[156:159]\n"
"ds_read_b64_tr_b16 v[104:105], v249 offset:27808\n"
"v_mfma_f32_16x16x32_bf16 a[156:159], v[180:183], v[220:223], a[156:159]\n"
"s_add_u32 m0, s60, 8192\n"
"v_mfma_f32_16x16x32_bf16 a[180:183], v[148:151], v[216:219], a[180:183]\n"
"buffer_load_dwordx4 v248, s[48:51], s59 offen offset:0 lds\n"
"v_mfma_f32_16x16x32_bf16 a[180:183], v[184:187], v[220:223], a[180:183]\n"
"ds_read_b64_tr_b16 v[106:107], v249 offset:32416\n"
"v_mfma_f32_16x16x32_bf16 a[204:207], v[152:155], v[216:219], a[204:207]\n"
"v_mfma_f32_16x16x32_bf16 a[204:207], v[188:191], v[220:223], a[204:207]\n"
"ds_read_b64_tr_b16 v[108:109], v249 offset:27840\n"
"v_mfma_f32_16x16x32_bf16 a[16:19], v[120:123], v[224:227], a[16:19]\n"
"buffer_load_dwordx4 v[216:219], v253, s[44:47], s61 offen offset:0 sc1\n"
"v_mfma_f32_16x16x32_bf16 a[16:19], v[156:159], v[228:231], a[16:19]\n"
"ds_read_b64_tr_b16 v[110:111], v249 offset:32448\n"
"v_mfma_f32_16x16x32_bf16 a[40:43], v[124:127], v[224:227], a[40:43]\n"
"v_mfma_f32_16x16x32_bf16 a[40:43], v[160:163], v[228:231], a[40:43]\n"
"ds_read_b64_tr_b16 v[112:113], v249 offset:27872\n"
"v_mfma_f32_16x16x32_bf16 a[64:67], v[128:131], v[224:227], a[64:67]\n"
"v_mfma_f32_16x16x32_bf16 a[64:67], v[164:167], v[228:231], a[64:67]\n"
"ds_read_b64_tr_b16 v[114:115], v249 offset:32480\n"
"v_mfma_f32_16x16x32_bf16 a[88:91], v[132:135], v[224:227], a[88:91]\n"
"v_mfma_f32_16x16x32_bf16 a[88:91], v[168:171], v[228:231], a[88:91]\n"
"ds_read_b64_tr_b16 v[116:117], v249 offset:27904\n"
"v_mfma_f32_16x16x32_bf16 a[112:115], v[136:139], v[224:227], a[112:115]\n"
"v_mfma_f32_16x16x32_bf16 a[112:115], v[172:175], v[228:231], a[112:115]\n"
"buffer_load_dwordx4 v[220:223], v253, s[44:47], s62 offen offset:2048 sc1\n"
"v_mfma_f32_16x16x32_bf16 a[136:139], v[140:143], v[224:227], a[136:139]\n"
"ds_read_b64_tr_b16 v[118:119], v249 offset:32512\n"
"v_mfma_f32_16x16x32_bf16 a[136:139], v[176:179], v[228:231], a[136:139]\n"
"v_mfma_f32_16x16x32_bf16 a[160:163], v[144:147], v[224:227], a[160:163]\n"
"v_mfma_f32_16x16x32_bf16 a[160:163], v[180:183], v[228:231], a[160:163]\n"
"v_mfma_f32_16x16x32_bf16 a[184:187], v[148:151], v[224:227], a[184:187]\n"
"v_mfma_f32_16x16x32_bf16 a[184:187], v[184:187], v[228:231], a[184:187]\n"
"v_mfma_f32_16x16x32_bf16 a[208:211], v[152:155], v[224:227], a[208:211]\n"
"v_mfma_f32_16x16x32_bf16 a[208:211], v[188:191], v[228:231], a[208:211]\n"
"v_mfma_f32_16x16x32_bf16 a[20:23], v[120:123], v[232:235], a[20:23]\n"
"buffer_load_dwordx4 v[224:227], v253, s[44:47], s61 offen offset:1024 sc1\n"
"v_mfma_f32_16x16x32_bf16 a[20:23], v[156:159], v[236:239], a[20:23]\n"
"v_mfma_f32_16x16x32_bf16 a[44:47], v[124:127], v[232:235], a[44:47]\n"
"v_mfma_f32_16x16x32_bf16 a[44:47], v[160:163], v[236:239], a[44:47]\n"
"v_mfma_f32_16x16x32_bf16 a[68:71], v[128:131], v[232:235], a[68:71]\n"
"v_mfma_f32_16x16x32_bf16 a[68:71], v[164:167], v[236:239], a[68:71]\n"
"v_mfma_f32_16x16x32_bf16 a[92:95], v[132:135], v[232:235], a[92:95]\n"
"v_mfma_f32_16x16x32_bf16 a[92:95], v[168:171], v[236:239], a[92:95]\n"
"v_mfma_f32_16x16x32_bf16 a[116:119], v[136:139], v[232:235], a[116:119]\n"
"v_mfma_f32_16x16x32_bf16 a[116:119], v[172:175], v[236:239], a[116:119]\n"
"buffer_load_dwordx4 v[228:231], v253, s[44:47], s62 offen offset:3072 sc1\n"
"v_mfma_f32_16x16x32_bf16 a[140:143], v[140:143], v[232:235], a[140:143]\n"
"v_mfma_f32_16x16x32_bf16 a[140:143], v[176:179], v[236:239], a[140:143]\n"
"v_mfma_f32_16x16x32_bf16 a[164:167], v[144:147], v[232:235], a[164:167]\n"
"v_mfma_f32_16x16x32_bf16 a[164:167], v[180:183], v[236:239], a[164:167]\n"
"v_mfma_f32_16x16x32_bf16 a[188:191], v[148:151], v[232:235], a[188:191]\n"
"v_mfma_f32_16x16x32_bf16 a[188:191], v[184:187], v[236:239], a[188:191]\n"
"v_mfma_f32_16x16x32_bf16 a[212:215], v[152:155], v[232:235], a[212:215]\n"
"buffer_load_dwordx4 v[232:235], v253, s[44:47], s61 offen offset:2048 sc1\n"
"v_mfma_f32_16x16x32_bf16 a[212:215], v[188:191], v[236:239], a[212:215]\n"
"buffer_load_dwordx4 v[236:239], v253, s[44:47], s63 offen offset:0 sc1\n"
"s_waitcnt vmcnt(22) lgkmcnt(0)\n"
"s_barrier\n"
"v_mfma_f32_16x16x32_bf16 a[0:3], v[48:51], v[0:3], a[0:3]\n"
"v_mfma_f32_16x16x32_bf16 a[0:3], v[84:87], v[4:7], a[0:3]\n"
"s_and_b32 s59, s56, 15\n"
"v_mfma_f32_16x16x32_bf16 a[24:27], v[52:55], v[0:3], a[24:27]\n"
"s_lshr_b64 s[56:57], s[56:57], 4\n"
"v_mfma_f32_16x16x32_bf16 a[24:27], v[88:91], v[4:7], a[24:27]\n"
"s_mul_i32 s59, s59, 73728\n"
"ds_read_b64_tr_b16 v[120:121], v249 offset:36864\n"
"v_mfma_f32_16x16x32_bf16 a[48:51], v[56:59], v[0:3], a[48:51]\n"
"s_add_u32 m0, s60, 18432\n"
"v_mfma_f32_16x16x32_bf16 a[48:51], v[92:95], v[4:7], a[48:51]\n"
"buffer_load_dwordx4 v240, s[48:51], s59 offen offset:0 lds\n"
"v_mfma_f32_16x16x32_bf16 a[72:75], v[60:63], v[0:3], a[72:75]\n"
"s_and_b32 s61, s70, 15\n"
"s_lshr_b64 s[70:71], s[70:71], 4\n"
"ds_read_b64_tr_b16 v[122:123], v249 offset:41472\n"
"v_mfma_f32_16x16x32_bf16 a[72:75], v[96:99], v[4:7], a[72:75]\n"
"s_mul_i32 s61, s61, 12288\n"
"s_add_u32 s62, s61, 4096\n"
"v_mfma_f32_16x16x32_bf16 a[96:99], v[64:67], v[0:3], a[96:99]\n"
"s_add_u32 s63, s61, 8192\n"
"ds_read_b64_tr_b16 v[124:125], v249 offset:36896\n"
"v_mfma_f32_16x16x32_bf16 a[96:99], v[100:103], v[4:7], a[96:99]\n"
"v_mfma_f32_16x16x32_bf16 a[120:123], v[68:71], v[0:3], a[120:123]\n"
"buffer_load_dwordx4 v241, s[48:51], s59 offen offset:1024 lds\n"
"v_mfma_f32_16x16x32_bf16 a[120:123], v[104:107], v[4:7], a[120:123]\n"
"ds_read_b64_tr_b16 v[126:127], v249 offset:41504\n"
"v_mfma_f32_16x16x32_bf16 a[144:147], v[72:75], v[0:3], a[144:147]\n"
"v_mfma_f32_16x16x32_bf16 a[144:147], v[108:111], v[4:7], a[144:147]\n"
"ds_read_b64_tr_b16 v[128:129], v249 offset:36928\n"
"v_mfma_f32_16x16x32_bf16 a[168:171], v[76:79], v[0:3], a[168:171]\n"
"buffer_load_dwordx4 v242, s[48:51], s59 offen offset:2048 lds\n"
"v_mfma_f32_16x16x32_bf16 a[168:171], v[112:115], v[4:7], a[168:171]\n"
"ds_read_b64_tr_b16 v[130:131], v249 offset:41536\n"
"v_mfma_f32_16x16x32_bf16 a[192:195], v[80:83], v[0:3], a[192:195]\n"
"v_mfma_f32_16x16x32_bf16 a[192:195], v[116:119], v[4:7], a[192:195]\n"
"ds_read_b64_tr_b16 v[132:133], v249 offset:36960\n"
"v_mfma_f32_16x16x32_bf16 a[4:7], v[48:51], v[8:11], a[4:7]\n"
"buffer_load_dwordx4 v[0:3], v255, s[44:47], s61 offen offset:0 sc1\n"
"v_mfma_f32_16x16x32_bf16 a[4:7], v[84:87], v[12:15], a[4:7]\n"
"ds_read_b64_tr_b16 v[134:135], v249 offset:41568\n"
"v_mfma_f32_16x16x32_bf16 a[28:31], v[52:55], v[8:11], a[28:31]\n"
"v_mfma_f32_16x16x32_bf16 a[28:31], v[88:91], v[12:15], a[28:31]\n"
"ds_read_b64_tr_b16 v[136:137], v249 offset:36992\n"
"v_mfma_f32_16x16x32_bf16 a[52:55], v[56:59], v[8:11], a[52:55]\n"
"v_mfma_f32_16x16x32_bf16 a[52:55], v[92:95], v[12:15], a[52:55]\n"
"buffer_load_dwordx4 v243, s[48:51], s59 offen offset:3072 lds\n"
"v_mfma_f32_16x16x32_bf16 a[76:79], v[60:63], v[8:11], a[76:79]\n"
"ds_read_b64_tr_b16 v[138:139], v249 offset:41600\n"
"v_mfma_f32_16x16x32_bf16 a[76:79], v[96:99], v[12:15], a[76:79]\n"
"v_mfma_f32_16x16x32_bf16 a[100:103], v[64:67], v[8:11], a[100:103]\n"
"ds_read_b64_tr_b16 v[140:141], v249 offset:37024\n"
"v_mfma_f32_16x16x32_bf16 a[100:103], v[100:103], v[12:15], a[100:103]\n"
"buffer_load_dwordx4 v[4:7], v255, s[44:47], s62 offen offset:2048 sc1\n"
"v_mfma_f32_16x16x32_bf16 a[124:127], v[68:71], v[8:11], a[124:127]\n"
"ds_read_b64_tr_b16 v[142:143], v249 offset:41632\n"
"v_mfma_f32_16x16x32_bf16 a[124:127], v[104:107], v[12:15], a[124:127]\n"
"v_mfma_f32_16x16x32_bf16 a[148:151], v[72:75], v[8:11], a[148:151]\n"
"ds_read_b64_tr_b16 v[144:145], v249 offset:37056\n"
"v_mfma_f32_16x16x32_bf16 a[148:151], v[108:111], v[12:15], a[148:151]\n"
"s_add_u32 m0, s60, 22528\n"
"v_mfma_f32_16x16x32_bf16 a[172:175], v[76:79], v[8:11], a[172:175]\n"
"buffer_load_dwordx4 v244, s[48:51], s59 offen offset:0 lds\n"
"v_mfma_f32_16x16x32_bf16 a[172:175], v[112:115], v[12:15], a[172:175]\n"
"ds_read_b64_tr_b16 v[146:147], v249 offset:41664\n"
"v_mfma_f32_16x16x32_bf16 a[196:199], v[80:83], v[8:11], a[196:199]\n"
"v_mfma_f32_16x16x32_bf16 a[196:199], v[116:119], v[12:15], a[196:199]\n"
"ds_read_b64_tr_b16 v[148:149], v249 offset:37088\n"
"v_mfma_f32_16x16x32_bf16 a[8:11], v[48:51], v[16:19], a[8:11]\n"
"buffer_load_dwordx4 v[8:11], v255, s[44:47], s61 offen offset:1024 sc1\n"
"v_mfma_f32_16x16x32_bf16 a[8:11], v[84:87], v[20:23], a[8:11]\n"
"ds_read_b64_tr_b16 v[150:151], v249 offset:41696\n"
"v_mfma_f32_16x16x32_bf16 a[32:35], v[52:55], v[16:19], a[32:35]\n"
"v_mfma_f32_16x16x32_bf16 a[32:35], v[88:91], v[20:23], a[32:35]\n"
"ds_read_b64_tr_b16 v[152:153], v249 offset:37120\n"
"v_mfma_f32_16x16x32_bf16 a[56:59], v[56:59], v[16:19], a[56:59]\n"
"v_mfma_f32_16x16x32_bf16 a[56:59], v[92:95], v[20:23], a[56:59]\n"
"buffer_load_dwordx4 v245, s[48:51], s59 offen offset:1024 lds\n"
"v_mfma_f32_16x16x32_bf16 a[80:83], v[60:63], v[16:19], a[80:83]\n"
"ds_read_b64_tr_b16 v[154:155], v249 offset:41728\n"
"v_mfma_f32_16x16x32_bf16 a[80:83], v[96:99], v[20:23], a[80:83]\n"
"v_mfma_f32_16x16x32_bf16 a[104:107], v[64:67], v[16:19], a[104:107]\n"
"ds_read_b64_tr_b16 v[156:157], v249 offset:46080\n"
"v_mfma_f32_16x16x32_bf16 a[104:107], v[100:103], v[20:23], a[104:107]\n"
"buffer_load_dwordx4 v[12:15], v255, s[44:47], s62 offen offset:3072 sc1\n"
"v_mfma_f32_16x16x32_bf16 a[128:131], v[68:71], v[16:19], a[128:131]\n"
"ds_read_b64_tr_b16 v[158:159], v249 offset:50688\n"
"v_mfma_f32_16x16x32_bf16 a[128:131], v[104:107], v[20:23], a[128:131]\n"
"v_mfma_f32_16x16x32_bf16 a[152:155], v[72:75], v[16:19], a[152:155]\n"
"ds_read_b64_tr_b16 v[160:161], v249 offset:46112\n"
"v_mfma_f32_16x16x32_bf16 a[152:155], v[108:111], v[20:23], a[152:155]\n"
"v_mfma_f32_16x16x32_bf16 a[176:179], v[76:79], v[16:19], a[176:179]\n"
"buffer_load_dwordx4 v246, s[48:51], s59 offen offset:2048 lds\n"
"v_mfma_f32_16x16x32_bf16 a[176:179], v[112:115], v[20:23], a[176:179]\n"
"ds_read_b64_tr_b16 v[162:163], v249 offset:50720\n"
"v_mfma_f32_16x16x32_bf16 a[200:203], v[80:83], v[16:19], a[200:203]\n"
"v_mfma_f32_16x16x32_bf16 a[200:203], v[116:119], v[20:23], a[200:203]\n"
"ds_read_b64_tr_b16 v[164:165], v249 offset:46144\n"
"v_mfma_f32_16x16x32_bf16 a[12:15], v[48:51], v[24:27], a[12:15]\n"
"buffer_load_dwordx4 v[16:19], v255, s[44:47], s61 offen offset:2048 sc1\n"
"v_mfma_f32_16x16x32_bf16 a[12:15], v[84:87], v[28:31], a[12:15]\n"
"ds_read_b64_tr_b16 v[166:167], v249 offset:50752\n"
"v_mfma_f32_16x16x32_bf16 a[36:39], v[52:55], v[24:27], a[36:39]\n"
"v_mfma_f32_16x16x32_bf16 a[36:39], v[88:91], v[28:31], a[36:39]\n"
"ds_read_b64_tr_b16 v[168:169], v249 offset:46176\n"
"v_mfma_f32_16x16x32_bf16 a[60:63], v[56:59], v[24:27], a[60:63]\n"
"v_mfma_f32_16x16x32_bf16 a[60:63], v[92:95], v[28:31], a[60:63]\n"
"buffer_load_dwordx4 v247, s[48:51], s59 offen offset:3072 lds\n"
"v_mfma_f32_16x16x32_bf16 a[84:87], v[60:63], v[24:27], a[84:87]\n"
"ds_read_b64_tr_b16 v[170:171], v249 offset:50784\n"
"v_mfma_f32_16x16x32_bf16 a[84:87], v[96:99], v[28:31], a[84:87]\n"
"v_mfma_f32_16x16x32_bf16 a[108:111], v[64:67], v[24:27], a[108:111]\n"
"ds_read_b64_tr_b16 v[172:173], v249 offset:46208\n"
"v_mfma_f32_16x16x32_bf16 a[108:111], v[100:103], v[28:31], a[108:111]\n"
"buffer_load_dwordx4 v[20:23], v255, s[44:47], s63 offen offset:0 sc1\n"
"v_mfma_f32_16x16x32_bf16 a[132:135], v[68:71], v[24:27], a[132:135]\n"
"ds_read_b64_tr_b16 v[174:175], v249 offset:50816\n"
"v_mfma_f32_16x16x32_bf16 a[132:135], v[104:107], v[28:31], a[132:135]\n"
"v_mfma_f32_16x16x32_bf16 a[156:159], v[72:75], v[24:27], a[156:159]\n"
"ds_read_b64_tr_b16 v[176:177], v249 offset:46240\n"
"v_mfma_f32_16x16x32_bf16 a[156:159], v[108:111], v[28:31], a[156:159]\n"
"s_add_u32 m0, s60, 26624\n"
"v_mfma_f32_16x16x32_bf16 a[180:183], v[76:79], v[24:27], a[180:183]\n"
"buffer_load_dwordx4 v248, s[48:51], s59 offen offset:0 lds\n"
"v_mfma_f32_16x16x32_bf16 a[180:183], v[112:115], v[28:31], a[180:183]\n"
"ds_read_b64_tr_b16 v[178:179], v249 offset:50848\n"
"v_mfma_f32_16x16x32_bf16 a[204:207], v[80:83], v[24:27], a[204:207]\n"
"v_mfma_f32_16x16x32_bf16 a[204:207], v[116:119], v[28:31], a[204:207]\n"
"ds_read_b64_tr_b16 v[180:181], v249 offset:46272\n"
"v_mfma_f32_16x16x32_bf16 a[16:19], v[48:51], v[32:35], a[16:19]\n"
"buffer_load_dwordx4 v[24:27], v253, s[44:47], s61 offen offset:0 sc1\n"
"v_mfma_f32_16x16x32_bf16 a[16:19], v[84:87], v[36:39], a[16:19]\n"
"ds_read_b64_tr_b16 v[182:183], v249 offset:50880\n"
"v_mfma_f32_16x16x32_bf16 a[40:43], v[52:55], v[32:35], a[40:43]\n"
"v_mfma_f32_16x16x32_bf16 a[40:43], v[88:91], v[36:39], a[40:43]\n"
"ds_read_b64_tr_b16 v[184:185], v249 offset:46304\n"
"v_mfma_f32_16x16x32_bf16 a[64:67], v[56:59], v[32:35], a[64:67]\n"
"v_mfma_f32_16x16x32_bf16 a[64:67], v[92:95], v[36:39], a[64:67]\n"
"ds_read_b64_tr_b16 v[186:187], v249 offset:50912\n"
"v_mfma_f32_16x16x32_bf16 a[88:91], v[60:63], v[32:35], a[88:91]\n"
"v_mfma_f32_16x16x32_bf16 a[88:91], v[96:99], v[36:39], a[88:91]\n"
"ds_read_b64_tr_b16 v[188:189], v249 offset:46336\n"
"v_mfma_f32_16x16x32_bf16 a[112:115], v[64:67], v[32:35], a[112:115]\n"
"v_mfma_f32_16x16x32_bf16 a[112:115], v[100:103], v[36:39], a[112:115]\n"
"buffer_load_dwordx4 v[28:31], v253, s[44:47], s62 offen offset:2048 sc1\n"
"v_mfma_f32_16x16x32_bf16 a[136:139], v[68:71], v[32:35], a[136:139]\n"
"ds_read_b64_tr_b16 v[190:191], v249 offset:50944\n"
"v_mfma_f32_16x16x32_bf16 a[136:139], v[104:107], v[36:39], a[136:139]\n"
"v_mfma_f32_16x16x32_bf16 a[160:163], v[72:75], v[32:35], a[160:163]\n"
"v_mfma_f32_16x16x32_bf16 a[160:163], v[108:111], v[36:39], a[160:163]\n"
"v_mfma_f32_16x16x32_bf16 a[184:187], v[76:79], v[32:35], a[184:187]\n"
"v_mfma_f32_16x16x32_bf16 a[184:187], v[112:115], v[36:39], a[184:187]\n"
"v_mfma_f32_16x16x32_bf16 a[208:211], v[80:83], v[32:35], a[208:211]\n"
"v_mfma_f32_16x16x32_bf16 a[208:211], v[116:119], v[36:39], a[208:211]\n"
"v_mfma_f32_16x16x32_bf16 a[20:23], v[48:51], v[40:43], a[20:23]\n"
"buffer_load_dwordx4 v[32:35], v253, s[44:47], s61 offen offset:1024 sc1\n"
"v_mfma_f32_16x16x32_bf16 a[20:23], v[84:87], v[44:47], a[20:23]\n"
"v_mfma_f32_16x16x32_bf16 a[44:47], v[52:55], v[40:43], a[44:47]\n"
"v_mfma_f32_16x16x32_bf16 a[44:47], v[88:91], v[44:47], a[44:47]\n"
"v_mfma_f32_16x16x32_bf16 a[68:71], v[56:59], v[40:43], a[68:71]\n"
"v_mfma_f32_16x16x32_bf16 a[68:71], v[92:95], v[44:47], a[68:71]\n"
"v_mfma_f32_16x16x32_bf16 a[92:95], v[60:63], v[40:43], a[92:95]\n"
"v_mfma_f32_16x16x32_bf16 a[92:95], v[96:99], v[44:47], a[92:95]\n"
"v_mfma_f32_16x16x32_bf16 a[116:119], v[64:67], v[40:43], a[116:119]\n"
"v_mfma_f32_16x16x32_bf16 a[116:119], v[100:103], v[44:47], a[116:119]\n"
"buffer_load_dwordx4 v[36:39], v253, s[44:47], s62 offen offset:3072 sc1\n"
"v_mfma_f32_16x16x32_bf16 a[140:143], v[68:71], v[40:43], a[140:143]\n"
"v_mfma_f32_16x16x32_bf16 a[140:143], v[104:107], v[44:47], a[140:143]\n"
"v_mfma_f32_16x16x32_bf16 a[164:167], v[72:75], v[40:43], a[164:167]\n"
"v_mfma_f32_16x16x32_bf16 a[164:167], v[108:111], v[44:47], a[164:167]\n"
"v_mfma_f32_16x16x32_bf16 a[188:191], v[76:79], v[40:43], a[188:191]\n"
"v_mfma_f32_16x16x32_bf16 a[188:191], v[112:115], v[44:47], a[188:191]\n"
"v_mfma_f32_16x16x32_bf16 a[212:215], v[80:83], v[40:43], a[212:215]\n"
"buffer_load_dwordx4 v[40:43], v253, s[44:47], s61 offen offset:2048 sc1\n"
"v_mfma_f32_16x16x32_bf16 a[212:215], v[116:119], v[44:47], a[212:215]\n"
"buffer_load_dwordx4 v[44:47], v253, s[44:47], s63 offen offset:0 sc1\n"
"s_waitcnt vmcnt(21) lgkmcnt(0)\n"
"s_barrier\n"
"v_mfma_f32_16x16x32_bf16 a[0:3], v[120:123], v[192:195], a[0:3]\n"
"v_mfma_f32_16x16x32_bf16 a[0:3], v[156:159], v[196:199], a[0:3]\n"
"s_and_b32 s59, s56, 15\n"
"v_mfma_f32_16x16x32_bf16 a[24:27], v[124:127], v[192:195], a[24:27]\n"
"s_lshr_b64 s[56:57], s[56:57], 4\n"
"v_mfma_f32_16x16x32_bf16 a[24:27], v[160:163], v[196:199], a[24:27]\n"
"s_mul_i32 s59, s59, 73728\n"
"ds_read_b64_tr_b16 v[48:49], v249 offset:0\n"
"v_mfma_f32_16x16x32_bf16 a[48:51], v[128:131], v[192:195], a[48:51]\n"
"s_add_u32 m0, s60, 36864\n"
"v_mfma_f32_16x16x32_bf16 a[48:51], v[164:167], v[196:199], a[48:51]\n"
"buffer_load_dwordx4 v240, s[48:51], s59 offen offset:0 lds\n"
"v_mfma_f32_16x16x32_bf16 a[72:75], v[132:135], v[192:195], a[72:75]\n"
"s_and_b32 s61, s70, 15\n"
"s_lshr_b64 s[70:71], s[70:71], 4\n"
"ds_read_b64_tr_b16 v[50:51], v249 offset:4608\n"
"v_mfma_f32_16x16x32_bf16 a[72:75], v[168:171], v[196:199], a[72:75]\n"
"s_mul_i32 s61, s61, 12288\n"
"s_add_u32 s62, s61, 4096\n"
"v_mfma_f32_16x16x32_bf16 a[96:99], v[136:139], v[192:195], a[96:99]\n"
"s_add_u32 s63, s61, 8192\n"
"ds_read_b64_tr_b16 v[52:53], v249 offset:32\n"
"v_mfma_f32_16x16x32_bf16 a[96:99], v[172:175], v[196:199], a[96:99]\n"
"v_mfma_f32_16x16x32_bf16 a[120:123], v[140:143], v[192:195], a[120:123]\n"
"buffer_load_dwordx4 v241, s[48:51], s59 offen offset:1024 lds\n"
"v_mfma_f32_16x16x32_bf16 a[120:123], v[176:179], v[196:199], a[120:123]\n"
"ds_read_b64_tr_b16 v[54:55], v249 offset:4640\n"
"v_mfma_f32_16x16x32_bf16 a[144:147], v[144:147], v[192:195], a[144:147]\n"
"v_mfma_f32_16x16x32_bf16 a[144:147], v[180:183], v[196:199], a[144:147]\n"
"ds_read_b64_tr_b16 v[56:57], v249 offset:64\n"
"v_mfma_f32_16x16x32_bf16 a[168:171], v[148:151], v[192:195], a[168:171]\n"
"buffer_load_dwordx4 v242, s[48:51], s59 offen offset:2048 lds\n"
"v_mfma_f32_16x16x32_bf16 a[168:171], v[184:187], v[196:199], a[168:171]\n"
"ds_read_b64_tr_b16 v[58:59], v249 offset:4672\n"
"v_mfma_f32_16x16x32_bf16 a[192:195], v[152:155], v[192:195], a[192:195]\n"
"v_mfma_f32_16x16x32_bf16 a[192:195], v[188:191], v[196:199], a[192:195]\n"
"ds_read_b64_tr_b16 v[60:61], v249 offset:96\n"
"v_mfma_f32_16x16x32_bf16 a[4:7], v[120:123], v[200:203], a[4:7]\n"
"buffer_load_dwordx4 v[192:195], v255, s[44:47], s61 offen offset:0 sc1\n"
"v_mfma_f32_16x16x32_bf16 a[4:7], v[156:159], v[204:207], a[4:7]\n"
"ds_read_b64_tr_b16 v[62:63], v249 offset:4704\n"
"v_mfma_f32_16x16x32_bf16 a[28:31], v[124:127], v[200:203], a[28:31]\n"
"v_mfma_f32_16x16x32_bf16 a[28:31], v[160:163], v[204:207], a[28:31]\n"
"ds_read_b64_tr_b16 v[64:65], v249 offset:128\n"
"v_mfma_f32_16x16x32_bf16 a[52:55], v[128:131], v[200:203], a[52:55]\n"
"v_mfma_f32_16x16x32_bf16 a[52:55], v[164:167], v[204:207], a[52:55]\n"
"buffer_load_dwordx4 v243, s[48:51], s59 offen offset:3072 lds\n"
"v_mfma_f32_16x16x32_bf16 a[76:79], v[132:135], v[200:203], a[76:79]\n"
"ds_read_b64_tr_b16 v[66:67], v249 offset:4736\n"
"v_mfma_f32_16x16x32_bf16 a[76:79], v[168:171], v[204:207], a[76:79]\n"
"v_mfma_f32_16x16x32_bf16 a[100:103], v[136:139], v[200:203], a[100:103]\n"
"ds_read_b64_tr_b16 v[68:69], v249 offset:160\n"
"v_mfma_f32_16x16x32_bf16 a[100:103], v[172:175], v[204:207], a[100:103]\n"
"buffer_load_dwordx4 v[196:199], v255, s[44:47], s62 offen offset:2048 sc1\n"
"v_mfma_f32_16x16x32_bf16 a[124:127], v[140:143], v[200:203], a[124:127]\n"
"ds_read_b64_tr_b16 v[70:71], v249 offset:4768\n"
"v_mfma_f32_16x16x32_bf16 a[124:127], v[176:179], v[204:207], a[124:127]\n"
"v_mfma_f32_16x16x32_bf16 a[148:151], v[144:147], v[200:203], a[148:151]\n"
"ds_read_b64_tr_b16 v[72:73], v249 offset:192\n"
"v_mfma_f32_16x16x32_bf16 a[148:151], v[180:183], v[204:207], a[148:151]\n"
"s_add_u32 m0, s60, 40960\n"
"v_mfma_f32_16x16x32_bf16 a[172:175], v[148:151], v[200:203], a[172:175]\n"
"buffer_load_dwordx4 v244, s[48:51], s59 offen offset:0 lds\n"
"v_mfma_f32_16x16x32_bf16 a[172:175], v[184:187], v[204:207], a[172:175]\n"
"ds_read_b64_tr_b16 v[74:75], v249 offset:4800\n"
"v_mfma_f32_16x16x32_bf16 a[196:199], v[152:155], v[200:203], a[196:199]\n"
"v_mfma_f32_16x16x32_bf16 a[196:199], v[188:191], v[204:207], a[196:199]\n"
"ds_read_b64_tr_b16 v[76:77], v249 offset:224\n"
"v_mfma_f32_16x16x32_bf16 a[8:11], v[120:123], v[208:211], a[8:11]\n"
"buffer_load_dwordx4 v[200:203], v255, s[44:47], s61 offen offset:1024 sc1\n"
"v_mfma_f32_16x16x32_bf16 a[8:11], v[156:159], v[212:215], a[8:11]\n"
"ds_read_b64_tr_b16 v[78:79], v249 offset:4832\n"
"v_mfma_f32_16x16x32_bf16 a[32:35], v[124:127], v[208:211], a[32:35]\n"
"v_mfma_f32_16x16x32_bf16 a[32:35], v[160:163], v[212:215], a[32:35]\n"
"ds_read_b64_tr_b16 v[80:81], v249 offset:256\n"
"v_mfma_f32_16x16x32_bf16 a[56:59], v[128:131], v[208:211], a[56:59]\n"
"global_load_dwordx2 a[216:217], v252, %16 sc1\n"
"v_mfma_f32_16x16x32_bf16 a[56:59], v[164:167], v[212:215], a[56:59]\n"
"buffer_load_dwordx4 v245, s[48:51], s59 offen offset:1024 lds\n"
"v_mfma_f32_16x16x32_bf16 a[80:83], v[132:135], v[208:211], a[80:83]\n"
"ds_read_b64_tr_b16 v[82:83], v249 offset:4864\n"
"v_mfma_f32_16x16x32_bf16 a[80:83], v[168:171], v[212:215], a[80:83]\n"
"v_mfma_f32_16x16x32_bf16 a[104:107], v[136:139], v[208:211], a[104:107]\n"
"ds_read_b64_tr_b16 v[84:85], v249 offset:9216\n"
"v_mfma_f32_16x16x32_bf16 a[104:107], v[172:175], v[212:215], a[104:107]\n"
"buffer_load_dwordx4 v[204:207], v255, s[44:47], s62 offen offset:3072 sc1\n"
"v_mfma_f32_16x16x32_bf16 a[128:131], v[140:143], v[208:211], a[128:131]\n"
"ds_read_b64_tr_b16 v[86:87], v249 offset:13824\n"
"v_mfma_f32_16x16x32_bf16 a[128:131], v[176:179], v[212:215], a[128:131]\n"
"v_mfma_f32_16x16x32_bf16 a[152:155], v[144:147], v[208:211], a[152:155]\n"
"ds_read_b64_tr_b16 v[88:89], v249 offset:9248\n"
"v_mfma_f32_16x16x32_bf16 a[152:155], v[180:183], v[212:215], a[152:155]\n"
"v_mfma_f32_16x16x32_bf16 a[176:179], v[148:151], v[208:211], a[176:179]\n"
"buffer_load_dwordx4 v246, s[48:51], s59 offen offset:2048 lds\n"
"v_mfma_f32_16x16x32_bf16 a[176:179], v[184:187], v[212:215], a[176:179]\n"
"ds_read_b64_tr_b16 v[90:91], v249 offset:13856\n"
"v_mfma_f32_16x16x32_bf16 a[200:203], v[152:155], v[208:211], a[200:203]\n"
"v_mfma_f32_16x16x32_bf16 a[200:203], v[188:191], v[212:215], a[200:203]\n"
"ds_read_b64_tr_b16 v[92:93], v249 offset:9280\n"
"v_mfma_f32_16x16x32_bf16 a[12:15], v[120:123], v[216:219], a[12:15]\n"
"buffer_load_dwordx4 v[208:211], v255, s[44:47], s61 offen offset:2048 sc1\n"
"v_mfma_f32_16x16x32_bf16 a[12:15], v[156:159], v[220:223], a[12:15]\n"
"ds_read_b64_tr_b16 v[94:95], v249 offset:13888\n"
"v_mfma_f32_16x16x32_bf16 a[36:39], v[124:127], v[216:219], a[36:39]\n"
"v_mfma_f32_16x16x32_bf16 a[36:39], v[160:163], v[220:223], a[36:39]\n"
"ds_read_b64_tr_b16 v[96:97], v249 offset:9312\n"
"v_mfma_f32_16x16x32_bf16 a[60:63], v[128:131], v[216:219], a[60:63]\n"
"v_mfma_f32_16x16x32_bf16 a[60:63], v[164:167], v[220:223], a[60:63]\n"
"buffer_load_dwordx4 v247, s[48:51], s59 offen offset:3072 lds\n"
"v_mfma_f32_16x16x32_bf16 a[84:87], v[132:135], v[216:219], a[84:87]\n"
"ds_read_b64_tr_b16 v[98:99], v249 offset:13920\n"
"v_mfma_f32_16x16x32_bf16 a[84:87], v[168:171], v[220:223], a[84:87]\n"
"v_mfma_f32_16x16x32_bf16 a[108:111], v[136:139], v[216:219], a[108:111]\n"
"ds_read_b64_tr_b16 v[100:101], v249 offset:9344\n"
"v_mfma_f32_16x16x32_bf16 a[108:111], v[172:175], v[220:223], a[108:111]\n"
"buffer_load_dwordx4 v[212:215], v255, s[44:47], s63 offen offset:0 sc1\n"
"v_mfma_f32_16x16x32_bf16 a[132:135], v[140:143], v[216:219], a[132:135]\n"
"ds_read_b64_tr_b16 v[102:103], v249 offset:13952\n"
"v_mfma_f32_16x16x32_bf16 a[132:135], v[176:179], v[220:223], a[132:135]\n"
"v_mfma_f32_16x16x32_bf16 a[156:159], v[144:147], v[216:219], a[156:159]\n"
"ds_read_b64_tr_b16 v[104:105], v249 offset:9376\n"
"v_mfma_f32_16x16x32_bf16 a[156:159], v[180:183], v[220:223], a[156:159]\n"
"s_add_u32 m0, s60, 45056\n"
"v_mfma_f32_16x16x32_bf16 a[180:183], v[148:151], v[216:219], a[180:183]\n"
"buffer_load_dwordx4 v248, s[48:51], s59 offen offset:0 lds\n"
"v_mfma_f32_16x16x32_bf16 a[180:183], v[184:187], v[220:223], a[180:183]\n"
"ds_read_b64_tr_b16 v[106:107], v249 offset:13984\n"
"v_mfma_f32_16x16x32_bf16 a[204:207], v[152:155], v[216:219], a[204:207]\n"
"v_mfma_f32_16x16x32_bf16 a[204:207], v[188:191], v[220:223], a[204:207]\n"
"ds_read_b64_tr_b16 v[108:109], v249 offset:9408\n"
"v_mfma_f32_16x16x32_bf16 a[16:19], v[120:123], v[224:227], a[16:19]\n"
"buffer_load_dwordx4 v[216:219], v253, s[44:47], s61 offen offset:0 sc1\n"
"v_mfma_f32_16x16x32_bf16 a[16:19], v[156:159], v[228:231], a[16:19]\n"
"ds_read_b64_tr_b16 v[110:111], v249 offset:14016\n"
"v_mfma_f32_16x16x32_bf16 a[40:43], v[124:127], v[224:227], a[40:43]\n"
"v_mfma_f32_16x16x32_bf16 a[40:43], v[160:163], v[228:231], a[40:43]\n"
"ds_read_b64_tr_b16 v[112:113], v249 offset:9440\n"
"v_mfma_f32_16x16x32_bf16 a[64:67], v[128:131], v[224:227], a[64:67]\n"
"v_mfma_f32_16x16x32_bf16 a[64:67], v[164:167], v[228:231], a[64:67]\n"
"ds_read_b64_tr_b16 v[114:115], v249 offset:14048\n"
"v_mfma_f32_16x16x32_bf16 a[88:91], v[132:135], v[224:227], a[88:91]\n"
"v_mfma_f32_16x16x32_bf16 a[88:91], v[168:171], v[228:231], a[88:91]\n"
"ds_read_b64_tr_b16 v[116:117], v249 offset:9472\n"
"v_mfma_f32_16x16x32_bf16 a[112:115], v[136:139], v[224:227], a[112:115]\n"
"v_mfma_f32_16x16x32_bf16 a[112:115], v[172:175], v[228:231], a[112:115]\n"
"buffer_load_dwordx4 v[220:223], v253, s[44:47], s62 offen offset:2048 sc1\n"
"v_mfma_f32_16x16x32_bf16 a[136:139], v[140:143], v[224:227], a[136:139]\n"
"ds_read_b64_tr_b16 v[118:119], v249 offset:14080\n"
"v_mfma_f32_16x16x32_bf16 a[136:139], v[176:179], v[228:231], a[136:139]\n"
"v_mfma_f32_16x16x32_bf16 a[160:163], v[144:147], v[224:227], a[160:163]\n"
"v_mfma_f32_16x16x32_bf16 a[160:163], v[180:183], v[228:231], a[160:163]\n"
"v_mfma_f32_16x16x32_bf16 a[184:187], v[148:151], v[224:227], a[184:187]\n"
"v_mfma_f32_16x16x32_bf16 a[184:187], v[184:187], v[228:231], a[184:187]\n"
"v_mfma_f32_16x16x32_bf16 a[208:211], v[152:155], v[224:227], a[208:211]\n"
"v_mfma_f32_16x16x32_bf16 a[208:211], v[188:191], v[228:231], a[208:211]\n"
"v_mfma_f32_16x16x32_bf16 a[20:23], v[120:123], v[232:235], a[20:23]\n"
"buffer_load_dwordx4 v[224:227], v253, s[44:47], s61 offen offset:1024 sc1\n"
"v_mfma_f32_16x16x32_bf16 a[20:23], v[156:159], v[236:239], a[20:23]\n"
"v_mfma_f32_16x16x32_bf16 a[44:47], v[124:127], v[232:235], a[44:47]\n"
"v_mfma_f32_16x16x32_bf16 a[44:47], v[160:163], v[236:239], a[44:47]\n"
"v_mfma_f32_16x16x32_bf16 a[68:71], v[128:131], v[232:235], a[68:71]\n"
"v_mfma_f32_16x16x32_bf16 a[68:71], v[164:167], v[236:239], a[68:71]\n"
"v_mfma_f32_16x16x32_bf16 a[92:95], v[132:135], v[232:235], a[92:95]\n"
"v_mfma_f32_16x16x32_bf16 a[92:95], v[168:171], v[236:239], a[92:95]\n"
"v_mfma_f32_16x16x32_bf16 a[116:119], v[136:139], v[232:235], a[116:119]\n"
"v_mfma_f32_16x16x32_bf16 a[116:119], v[172:175], v[236:239], a[116:119]\n"
"buffer_load_dwordx4 v[228:231], v253, s[44:47], s62 offen offset:3072 sc1\n"
"v_mfma_f32_16x16x32_bf16 a[140:143], v[140:143], v[232:235], a[140:143]\n"
"v_mfma_f32_16x16x32_bf16 a[140:143], v[176:179], v[236:239], a[140:143]\n"
"v_mfma_f32_16x16x32_bf16 a[164:167], v[144:147], v[232:235], a[164:167]\n"
"v_mfma_f32_16x16x32_bf16 a[164:167], v[180:183], v[236:239], a[164:167]\n"
"v_mfma_f32_16x16x32_bf16 a[188:191], v[148:151], v[232:235], a[188:191]\n"
"v_mfma_f32_16x16x32_bf16 a[188:191], v[184:187], v[236:239], a[188:191]\n"
"v_mfma_f32_16x16x32_bf16 a[212:215], v[152:155], v[232:235], a[212:215]\n"
"buffer_load_dwordx4 v[232:235], v253, s[44:47], s61 offen offset:2048 sc1\n"
"v_mfma_f32_16x16x32_bf16 a[212:215], v[188:191], v[236:239], a[212:215]\n"
"buffer_load_dwordx4 v[236:239], v253, s[44:47], s63 offen offset:0 sc1\n"
"s_waitcnt vmcnt(13)\n"
"v_accvgpr_read_b32 v251, a216\n"
"s_nop 0\n"
"v_readfirstlane_b32 s73, v251\n"
"v_accvgpr_read_b32 v251, a217\n"
"s_nop 0\n"
"v_readfirstlane_b32 s74, v251\n"
"s_nop 0\n"
"s_sub_u32 s73, s73, s85\n"
"s_sub_u32 s74, s74, s86\n"
"s_sub_u32 s73, s73, 2\n"
"s_sub_u32 s74, s74, 2\n"
"s_max_u32 s73, s73, s74\n"
"s_cmp_le_u32 s73, 0\n"
"s_cbranch_scc1 .Lf_p1ok\n"
"s_mov_b32 s75, 0x4000\n"
".Lf_p1spin:\n"
"s_sleep 2\n"
"global_load_dwordx2 a[216:217], v252, %16 sc1\n"
"s_waitcnt vmcnt(0)\n"
"v_accvgpr_read_b32 v251, a216\n"
"s_nop 0\n"
"v_readfirstlane_b32 s73, v251\n"
"v_accvgpr_read_b32 v251, a217\n"
"s_nop 0\n"
"v_readfirstlane_b32 s74, v251\n"
"s_nop 0\n"
"s_sub_u32 s73, s73, s85\n"
"s_sub_u32 s74, s74, s86\n"
"s_sub_u32 s73, s73, 2\n"
"s_sub_u32 s74, s74, 2\n"
"s_max_u32 s73, s73, s74\n"
"s_sub_u32 s75, s75, 1\n"
"s_cmp_eq_u32 s75, 0\n"
"s_cbranch_scc1 .Lf_p1ok\n"
"s_cmp_gt_u32 s73, 0\n"
"s_cbranch_scc1 .Lf_p1spin\n"
".Lf_p1ok:\n"
"s_waitcnt lgkmcnt(0)\n"
"s_barrier\n"
"v_mfma_f32_16x16x32_bf16 a[0:3], v[48:51], v[0:3], a[0:3]\n"
"v_mfma_f32_16x16x32_bf16 a[0:3], v[84:87], v[4:7], a[0:3]\n"
"s_and_b32 s59, s56, 15\n"
"v_mfma_f32_16x16x32_bf16 a[24:27], v[52:55], v[0:3], a[24:27]\n"
"s_lshr_b64 s[56:57], s[56:57], 4\n"
"v_mfma_f32_16x16x32_bf16 a[24:27], v[88:91], v[4:7], a[24:27]\n"
"s_mul_i32 s59, s59, 73728\n"
"ds_read_b64_tr_b16 v[120:121], v249 offset:18432\n"
"v_mfma_f32_16x16x32_bf16 a[48:51], v[56:59], v[0:3], a[48:51]\n"
"s_add_u32 m0, s60, 0\n"
"v_mfma_f32_16x16x32_bf16 a[48:51], v[92:95], v[4:7], a[48:51]\n"
"buffer_load_dwordx4 v240, s[48:51], s59 offen offset:0 lds\n"
"v_mfma_f32_16x16x32_bf16 a[72:75], v[60:63], v[0:3], a[72:75]\n"
"s_and_b32 s61, s70, 15\n"
"s_lshr_b64 s[70:71], s[70:71], 4\n"
"ds_read_b64_tr_b16 v[122:123], v249 offset:23040\n"
"v_mfma_f32_16x16x32_bf16 a[72:75], v[96:99], v[4:7], a[72:75]\n"
"s_mul_i32 s61, s61, 12288\n"
"s_add_u32 s62, s61, 4096\n"
"v_mfma_f32_16x16x32_bf16 a[96:99], v[64:67], v[0:3], a[96:99]\n"
"s_add_u32 s63, s61, 8192\n"
"ds_read_b64_tr_b16 v[124:125], v249 offset:18464\n"
"v_mfma_f32_16x16x32_bf16 a[96:99], v[100:103], v[4:7], a[96:99]\n"
"v_mfma_f32_16x16x32_bf16 a[120:123], v[68:71], v[0:3], a[120:123]\n"
"buffer_load_dwordx4 v241, s[48:51], s59 offen offset:1024 lds\n"
"v_mfma_f32_16x16x32_bf16 a[120:123], v[104:107], v[4:7], a[120:123]\n"
"ds_read_b64_tr_b16 v[126:127], v249 offset:23072\n"
"v_mfma_f32_16x16x32_bf16 a[144:147], v[72:75], v[0:3], a[144:147]\n"
"v_mfma_f32_16x16x32_bf16 a[144:147], v[108:111], v[4:7], a[144:147]\n"
"ds_read_b64_tr_b16 v[128:129], v249 offset:18496\n"
"v_mfma_f32_16x16x32_bf16 a[168:171], v[76:79], v[0:3], a[168:171]\n"
"buffer_load_dwordx4 v242, s[48:51], s59 offen offset:2048 lds\n"
"v_mfma_f32_16x16x32_bf16 a[168:171], v[112:115], v[4:7], a[168:171]\n"
"ds_read_b64_tr_b16 v[130:131], v249 offset:23104\n"
"v_mfma_f32_16x16x32_bf16 a[192:195], v[80:83], v[0:3], a[192:195]\n"
"v_mfma_f32_16x16x32_bf16 a[192:195], v[116:119], v[4:7], a[192:195]\n"
"ds_read_b64_tr_b16 v[132:133], v249 offset:18528\n"
"v_mfma_f32_16x16x32_bf16 a[4:7], v[48:51], v[8:11], a[4:7]\n"
"buffer_load_dwordx4 v[0:3], v255, s[44:47], s61 offen offset:0 sc1\n"
"v_mfma_f32_16x16x32_bf16 a[4:7], v[84:87], v[12:15], a[4:7]\n"
"ds_read_b64_tr_b16 v[134:135], v249 offset:23136\n"
"v_mfma_f32_16x16x32_bf16 a[28:31], v[52:55], v[8:11], a[28:31]\n"
"v_mfma_f32_16x16x32_bf16 a[28:31], v[88:91], v[12:15], a[28:31]\n"
"ds_read_b64_tr_b16 v[136:137], v249 offset:18560\n"
"v_mfma_f32_16x16x32_bf16 a[52:55], v[56:59], v[8:11], a[52:55]\n"
"v_mfma_f32_16x16x32_bf16 a[52:55], v[92:95], v[12:15], a[52:55]\n"
"buffer_load_dwordx4 v243, s[48:51], s59 offen offset:3072 lds\n"
"v_mfma_f32_16x16x32_bf16 a[76:79], v[60:63], v[8:11], a[76:79]\n"
"ds_read_b64_tr_b16 v[138:139], v249 offset:23168\n"
"v_mfma_f32_16x16x32_bf16 a[76:79], v[96:99], v[12:15], a[76:79]\n"
"v_mfma_f32_16x16x32_bf16 a[100:103], v[64:67], v[8:11], a[100:103]\n"
"ds_read_b64_tr_b16 v[140:141], v249 offset:18592\n"
"v_mfma_f32_16x16x32_bf16 a[100:103], v[100:103], v[12:15], a[100:103]\n"
"buffer_load_dwordx4 v[4:7], v255, s[44:47], s62 offen offset:2048 sc1\n"
"v_mfma_f32_16x16x32_bf16 a[124:127], v[68:71], v[8:11], a[124:127]\n"
"ds_read_b64_tr_b16 v[142:143], v249 offset:23200\n"
"v_mfma_f32_16x16x32_bf16 a[124:127], v[104:107], v[12:15], a[124:127]\n"
"v_mfma_f32_16x16x32_bf16 a[148:151], v[72:75], v[8:11], a[148:151]\n"
"ds_read_b64_tr_b16 v[144:145], v249 offset:18624\n"
"v_mfma_f32_16x16x32_bf16 a[148:151], v[108:111], v[12:15], a[148:151]\n"
"s_add_u32 m0, s60, 4096\n"
"v_mfma_f32_16x16x32_bf16 a[172:175], v[76:79], v[8:11], a[172:175]\n"
"buffer_load_dwordx4 v244, s[48:51], s59 offen offset:0 lds\n"
"v_mfma_f32_16x16x32_bf16 a[172:175], v[112:115], v[12:15], a[172:175]\n"
"ds_read_b64_tr_b16 v[146:147], v249 offset:23232\n"
"v_mfma_f32_16x16x32_bf16 a[196:199], v[80:83], v[8:11], a[196:199]\n"
"v_mfma_f32_16x16x32_bf16 a[196:199], v[116:119], v[12:15], a[196:199]\n"
"ds_read_b64_tr_b16 v[148:149], v249 offset:18656\n"
"v_mfma_f32_16x16x32_bf16 a[8:11], v[48:51], v[16:19], a[8:11]\n"
"buffer_load_dwordx4 v[8:11], v255, s[44:47], s61 offen offset:1024 sc1\n"
"v_mfma_f32_16x16x32_bf16 a[8:11], v[84:87], v[20:23], a[8:11]\n"
"ds_read_b64_tr_b16 v[150:151], v249 offset:23264\n"
"v_mfma_f32_16x16x32_bf16 a[32:35], v[52:55], v[16:19], a[32:35]\n"
"v_mfma_f32_16x16x32_bf16 a[32:35], v[88:91], v[20:23], a[32:35]\n"
"ds_read_b64_tr_b16 v[152:153], v249 offset:18688\n"
"v_mfma_f32_16x16x32_bf16 a[56:59], v[56:59], v[16:19], a[56:59]\n"
"v_mfma_f32_16x16x32_bf16 a[56:59], v[92:95], v[20:23], a[56:59]\n"
"buffer_load_dwordx4 v245, s[48:51], s59 offen offset:1024 lds\n"
"v_mfma_f32_16x16x32_bf16 a[80:83], v[60:63], v[16:19], a[80:83]\n"
"ds_read_b64_tr_b16 v[154:155], v249 offset:23296\n"
"v_mfma_f32_16x16x32_bf16 a[80:83], v[96:99], v[20:23], a[80:83]\n"
"v_mfma_f32_16x16x32_bf16 a[104:107], v[64:67], v[16:19], a[104:107]\n"
"ds_read_b64_tr_b16 v[156:157], v249 offset:27648\n"
"v_mfma_f32_16x16x32_bf16 a[104:107], v[100:103], v[20:23], a[104:107]\n"
"buffer_load_dwordx4 v[12:15], v255, s[44:47], s62 offen offset:3072 sc1\n"
"v_mfma_f32_16x16x32_bf16 a[128:131], v[68:71], v[16:19], a[128:131]\n"
"ds_read_b64_tr_b16 v[158:159], v249 offset:32256\n"
"v_mfma_f32_16x16x32_bf16 a[128:131], v[104:107], v[20:23], a[128:131]\n"
"v_mfma_f32_16x16x32_bf16 a[152:155], v[72:75], v[16:19], a[152:155]\n"
"ds_read_b64_tr_b16 v[160:161], v249 offset:27680\n"
"v_mfma_f32_16x16x32_bf16 a[152:155], v[108:111], v[20:23], a[152:155]\n"
"v_mfma_f32_16x16x32_bf16 a[176:179], v[76:79], v[16:19], a[176:179]\n"
"buffer_load_dwordx4 v246, s[48:51], s59 offen offset:2048 lds\n"
"v_mfma_f32_16x16x32_bf16 a[176:179], v[112:115], v[20:23], a[176:179]\n"
"ds_read_b64_tr_b16 v[162:163], v249 offset:32288\n"
"v_mfma_f32_16x16x32_bf16 a[200:203], v[80:83], v[16:19], a[200:203]\n"
"v_mfma_f32_16x16x32_bf16 a[200:203], v[116:119], v[20:23], a[200:203]\n"
"ds_read_b64_tr_b16 v[164:165], v249 offset:27712\n"
"v_mfma_f32_16x16x32_bf16 a[12:15], v[48:51], v[24:27], a[12:15]\n"
"buffer_load_dwordx4 v[16:19], v255, s[44:47], s61 offen offset:2048 sc1\n"
"v_mfma_f32_16x16x32_bf16 a[12:15], v[84:87], v[28:31], a[12:15]\n"
"ds_read_b64_tr_b16 v[166:167], v249 offset:32320\n"
"v_mfma_f32_16x16x32_bf16 a[36:39], v[52:55], v[24:27], a[36:39]\n"
"v_mfma_f32_16x16x32_bf16 a[36:39], v[88:91], v[28:31], a[36:39]\n"
"ds_read_b64_tr_b16 v[168:169], v249 offset:27744\n"
"v_mfma_f32_16x16x32_bf16 a[60:63], v[56:59], v[24:27], a[60:63]\n"
"v_mfma_f32_16x16x32_bf16 a[60:63], v[92:95], v[28:31], a[60:63]\n"
"buffer_load_dwordx4 v247, s[48:51], s59 offen offset:3072 lds\n"
"v_mfma_f32_16x16x32_bf16 a[84:87], v[60:63], v[24:27], a[84:87]\n"
"ds_read_b64_tr_b16 v[170:171], v249 offset:32352\n"
"v_mfma_f32_16x16x32_bf16 a[84:87], v[96:99], v[28:31], a[84:87]\n"
"v_mfma_f32_16x16x32_bf16 a[108:111], v[64:67], v[24:27], a[108:111]\n"
"ds_read_b64_tr_b16 v[172:173], v249 offset:27776\n"
"v_mfma_f32_16x16x32_bf16 a[108:111], v[100:103], v[28:31], a[108:111]\n"
"buffer_load_dwordx4 v[20:23], v255, s[44:47], s63 offen offset:0 sc1\n"
"v_mfma_f32_16x16x32_bf16 a[132:135], v[68:71], v[24:27], a[132:135]\n"
"ds_read_b64_tr_b16 v[174:175], v249 offset:32384\n"
"v_mfma_f32_16x16x32_bf16 a[132:135], v[104:107], v[28:31], a[132:135]\n"
"v_mfma_f32_16x16x32_bf16 a[156:159], v[72:75], v[24:27], a[156:159]\n"
"ds_read_b64_tr_b16 v[176:177], v249 offset:27808\n"
"v_mfma_f32_16x16x32_bf16 a[156:159], v[108:111], v[28:31], a[156:159]\n"
"s_add_u32 m0, s60, 8192\n"
"v_mfma_f32_16x16x32_bf16 a[180:183], v[76:79], v[24:27], a[180:183]\n"
"buffer_load_dwordx4 v248, s[48:51], s59 offen offset:0 lds\n"
"v_mfma_f32_16x16x32_bf16 a[180:183], v[112:115], v[28:31], a[180:183]\n"
"ds_read_b64_tr_b16 v[178:179], v249 offset:32416\n"
"v_mfma_f32_16x16x32_bf16 a[204:207], v[80:83], v[24:27], a[204:207]\n"
"v_mfma_f32_16x16x32_bf16 a[204:207], v[116:119], v[28:31], a[204:207]\n"
"ds_read_b64_tr_b16 v[180:181], v249 offset:27840\n"
"v_mfma_f32_16x16x32_bf16 a[16:19], v[48:51], v[32:35], a[16:19]\n"
"buffer_load_dwordx4 v[24:27], v253, s[44:47], s61 offen offset:0 sc1\n"
"v_mfma_f32_16x16x32_bf16 a[16:19], v[84:87], v[36:39], a[16:19]\n"
"ds_read_b64_tr_b16 v[182:183], v249 offset:32448\n"
"v_mfma_f32_16x16x32_bf16 a[40:43], v[52:55], v[32:35], a[40:43]\n"
"v_mfma_f32_16x16x32_bf16 a[40:43], v[88:91], v[36:39], a[40:43]\n"
"ds_read_b64_tr_b16 v[184:185], v249 offset:27872\n"
"v_mfma_f32_16x16x32_bf16 a[64:67], v[56:59], v[32:35], a[64:67]\n"
"v_mfma_f32_16x16x32_bf16 a[64:67], v[92:95], v[36:39], a[64:67]\n"
"ds_read_b64_tr_b16 v[186:187], v249 offset:32480\n"
"v_mfma_f32_16x16x32_bf16 a[88:91], v[60:63], v[32:35], a[88:91]\n"
"v_mfma_f32_16x16x32_bf16 a[88:91], v[96:99], v[36:39], a[88:91]\n"
"ds_read_b64_tr_b16 v[188:189], v249 offset:27904\n"
"v_mfma_f32_16x16x32_bf16 a[112:115], v[64:67], v[32:35], a[112:115]\n"
"v_mfma_f32_16x16x32_bf16 a[112:115], v[100:103], v[36:39], a[112:115]\n"
"buffer_load_dwordx4 v[28:31], v253, s[44:47], s62 offen offset:2048 sc1\n"
"v_mfma_f32_16x16x32_bf16 a[136:139], v[68:71], v[32:35], a[136:139]\n"
"ds_read_b64_tr_b16 v[190:191], v249 offset:32512\n"
"v_mfma_f32_16x16x32_bf16 a[136:139], v[104:107], v[36:39], a[136:139]\n"
"v_mfma_f32_16x16x32_bf16 a[160:163], v[72:75], v[32:35], a[160:163]\n"
"v_mfma_f32_16x16x32_bf16 a[160:163], v[108:111], v[36:39], a[160:163]\n"
"v_mfma_f32_16x16x32_bf16 a[184:187], v[76:79], v[32:35], a[184:187]\n"
"v_mfma_f32_16x16x32_bf16 a[184:187], v[112:115], v[36:39], a[184:187]\n"
"v_mfma_f32_16x16x32_bf16 a[208:211], v[80:83], v[32:35], a[208:211]\n"
"v_mfma_f32_16x16x32_bf16 a[208:211], v[116:119], v[36:39], a[208:211]\n"
"v_mfma_f32_16x16x32_bf16 a[20:23], v[48:51], v[40:43], a[20:23]\n"
"buffer_load_dwordx4 v[32:35], v253, s[44:47], s61 offen offset:1024 sc1\n"
"v_mfma_f32_16x16x32_bf16 a[20:23], v[84:87], v[44:47], a[20:23]\n"
"v_mfma_f32_16x16x32_bf16 a[44:47], v[52:55], v[40:43], a[44:47]\n"
"v_mfma_f32_16x16x32_bf16 a[44:47], v[88:91], v[44:47], a[44:47]\n"
"v_mfma_f32_16x16x32_bf16 a[68:71], v[56:59], v[40:43], a[68:71]\n"
"v_mfma_f32_16x16x32_bf16 a[68:71], v[92:95], v[44:47], a[68:71]\n"
"v_mfma_f32_16x16x32_bf16 a[92:95], v[60:63], v[40:43], a[92:95]\n"
"v_mfma_f32_16x16x32_bf16 a[92:95], v[96:99], v[44:47], a[92:95]\n"
"v_mfma_f32_16x16x32_bf16 a[116:119], v[64:67], v[40:43], a[116:119]\n"
"v_mfma_f32_16x16x32_bf16 a[116:119], v[100:103], v[44:47], a[116:119]\n"
"buffer_load_dwordx4 v[36:39], v253, s[44:47], s62 offen offset:3072 sc1\n"
"v_mfma_f32_16x16x32_bf16 a[140:143], v[68:71], v[40:43], a[140:143]\n"
"v_mfma_f32_16x16x32_bf16 a[140:143], v[104:107], v[44:47], a[140:143]\n"
"v_mfma_f32_16x16x32_bf16 a[164:167], v[72:75], v[40:43], a[164:167]\n"
"v_mfma_f32_16x16x32_bf16 a[164:167], v[108:111], v[44:47], a[164:167]\n"
"v_mfma_f32_16x16x32_bf16 a[188:191], v[76:79], v[40:43], a[188:191]\n"
"v_mfma_f32_16x16x32_bf16 a[188:191], v[112:115], v[44:47], a[188:191]\n"
"v_mfma_f32_16x16x32_bf16 a[212:215], v[80:83], v[40:43], a[212:215]\n"
"buffer_load_dwordx4 v[40:43], v253, s[44:47], s61 offen offset:2048 sc1\n"
"v_mfma_f32_16x16x32_bf16 a[212:215], v[116:119], v[44:47], a[212:215]\n"
"buffer_load_dwordx4 v[44:47], v253, s[44:47], s63 offen offset:0 sc1\n"
"s_waitcnt vmcnt(21) lgkmcnt(0)\n"
"s_barrier\n"
"v_mfma_f32_16x16x32_bf16 a[0:3], v[120:123], v[192:195], a[0:3]\n"
"v_mfma_f32_16x16x32_bf16 a[0:3], v[156:159], v[196:199], a[0:3]\n"
"s_and_b32 s59, s56, 15\n"
"v_mfma_f32_16x16x32_bf16 a[24:27], v[124:127], v[192:195], a[24:27]\n"
"s_lshr_b64 s[56:57], s[56:57], 4\n"
"v_mfma_f32_16x16x32_bf16 a[24:27], v[160:163], v[196:199], a[24:27]\n"
"s_mul_i32 s59, s59, 73728\n"
"ds_read_b64_tr_b16 v[48:49], v249 offset:36864\n"
"v_mfma_f32_16x16x32_bf16 a[48:51], v[128:131], v[192:195], a[48:51]\n"
"s_add_u32 m0, s60, 18432\n"
"v_mfma_f32_16x16x32_bf16 a[48:51], v[164:167], v[196:199], a[48:51]\n"
"buffer_load_dwordx4 v240, s[48:51], s59 offen offset:0 lds\n"
"v_mfma_f32_16x16x32_bf16 a[72:75], v[132:135], v[192:195], a[72:75]\n"
"s_and_b32 s61, s70, 15\n"
"s_lshr_b64 s[70:71], s[70:71], 4\n"
"ds_read_b64_tr_b16 v[50:51], v249 offset:41472\n"
"v_mfma_f32_16x16x32_bf16 a[72:75], v[168:171], v[196:199], a[72:75]\n"
"s_mul_i32 s61, s61, 12288\n"
"s_add_u32 s62, s61, 4096\n"
"v_mfma_f32_16x16x32_bf16 a[96:99], v[136:139], v[192:195], a[96:99]\n"
"s_add_u32 s63, s61, 8192\n"
"ds_read_b64_tr_b16 v[52:53], v249 offset:36896\n"
"v_mfma_f32_16x16x32_bf16 a[96:99], v[172:175], v[196:199], a[96:99]\n"
"v_mfma_f32_16x16x32_bf16 a[120:123], v[140:143], v[192:195], a[120:123]\n"
"buffer_load_dwordx4 v241, s[48:51], s59 offen offset:1024 lds\n"
"v_mfma_f32_16x16x32_bf16 a[120:123], v[176:179], v[196:199], a[120:123]\n"
"ds_read_b64_tr_b16 v[54:55], v249 offset:41504\n"
"v_mfma_f32_16x16x32_bf16 a[144:147], v[144:147], v[192:195], a[144:147]\n"
"v_mfma_f32_16x16x32_bf16 a[144:147], v[180:183], v[196:199], a[144:147]\n"
"ds_read_b64_tr_b16 v[56:57], v249 offset:36928\n"
"v_mfma_f32_16x16x32_bf16 a[168:171], v[148:151], v[192:195], a[168:171]\n"
"buffer_load_dwordx4 v242, s[48:51], s59 offen offset:2048 lds\n"
"v_mfma_f32_16x16x32_bf16 a[168:171], v[184:187], v[196:199], a[168:171]\n"
"ds_read_b64_tr_b16 v[58:59], v249 offset:41536\n"
"v_mfma_f32_16x16x32_bf16 a[192:195], v[152:155], v[192:195], a[192:195]\n"
"v_mfma_f32_16x16x32_bf16 a[192:195], v[188:191], v[196:199], a[192:195]\n"
"ds_read_b64_tr_b16 v[60:61], v249 offset:36960\n"
"v_mfma_f32_16x16x32_bf16 a[4:7], v[120:123], v[200:203], a[4:7]\n"
"buffer_load_dwordx4 v[192:195], v255, s[44:47], s61 offen offset:0 sc1\n"
"v_mfma_f32_16x16x32_bf16 a[4:7], v[156:159], v[204:207], a[4:7]\n"
"ds_read_b64_tr_b16 v[62:63], v249 offset:41568\n"
"v_mfma_f32_16x16x32_bf16 a[28:31], v[124:127], v[200:203], a[28:31]\n"
"v_mfma_f32_16x16x32_bf16 a[28:31], v[160:163], v[204:207], a[28:31]\n"
"ds_read_b64_tr_b16 v[64:65], v249 offset:36992\n"
"v_mfma_f32_16x16x32_bf16 a[52:55], v[128:131], v[200:203], a[52:55]\n"
"v_mfma_f32_16x16x32_bf16 a[52:55], v[164:167], v[204:207], a[52:55]\n"
"buffer_load_dwordx4 v243, s[48:51], s59 offen offset:3072 lds\n"
"v_mfma_f32_16x16x32_bf16 a[76:79], v[132:135], v[200:203], a[76:79]\n"
"ds_read_b64_tr_b16 v[66:67], v249 offset:41600\n"
"v_mfma_f32_16x16x32_bf16 a[76:79], v[168:171], v[204:207], a[76:79]\n"
"v_mfma_f32_16x16x32_bf16 a[100:103], v[136:139], v[200:203], a[100:103]\n"
"ds_read_b64_tr_b16 v[68:69], v249 offset:37024\n"
"v_mfma_f32_16x16x32_bf16 a[100:103], v[172:175], v[204:207], a[100:103]\n"
"buffer_load_dwordx4 v[196:199], v255, s[44:47], s62 offen offset:2048 sc1\n"
"v_mfma_f32_16x16x32_bf16 a[124:127], v[140:143], v[200:203], a[124:127]\n"
"ds_read_b64_tr_b16 v[70:71], v249 offset:41632\n"
"v_mfma_f32_16x16x32_bf16 a[124:127], v[176:179], v[204:207], a[124:127]\n"
"v_mfma_f32_16x16x32_bf16 a[148:151], v[144:147], v[200:203], a[148:151]\n"
"ds_read_b64_tr_b16 v[72:73], v249 offset:37056\n"
"v_mfma_f32_16x16x32_bf16 a[148:151], v[180:183], v[204:207], a[148:151]\n"
"s_add_u32 m0, s60, 22528\n"
"v_mfma_f32_16x16x32_bf16 a[172:175], v[148:151], v[200:203], a[172:175]\n"
"buffer_load_dwordx4 v244, s[48:51], s59 offen offset:0 lds\n"
"v_mfma_f32_16x16x32_bf16 a[172:175], v[184:187], v[204:207], a[172:175]\n"
"ds_read_b64_tr_b16 v[74:75], v249 offset:41664\n"
"v_mfma_f32_16x16x32_bf16 a[196:199], v[152:155], v[200:203], a[196:199]\n"
"v_mfma_f32_16x16x32_bf16 a[196:199], v[188:191], v[204:207], a[196:199]\n"
"ds_read_b64_tr_b16 v[76:77], v249 offset:37088\n"
"v_mfma_f32_16x16x32_bf16 a[8:11], v[120:123], v[208:211], a[8:11]\n"
"buffer_load_dwordx4 v[200:203], v255, s[44:47], s61 offen offset:1024 sc1\n"
"v_mfma_f32_16x16x32_bf16 a[8:11], v[156:159], v[212:215], a[8:11]\n"
"ds_read_b64_tr_b16 v[78:79], v249 offset:41696\n"
"v_mfma_f32_16x16x32_bf16 a[32:35], v[124:127], v[208:211], a[32:35]\n"
"v_mfma_f32_16x16x32_bf16 a[32:35], v[160:163], v[212:215], a[32:35]\n"
"ds_read_b64_tr_b16 v[80:81], v249 offset:37120\n"
"v_mfma_f32_16x16x32_bf16 a[56:59], v[128:131], v[208:211], a[56:59]\n"
"v_mfma_f32_16x16x32_bf16 a[56:59], v[164:167], v[212:215], a[56:59]\n"
"buffer_load_dwordx4 v245, s[48:51], s59 offen offset:1024 lds\n"
"v_mfma_f32_16x16x32_bf16 a[80:83], v[132:135], v[208:211], a[80:83]\n"
"ds_read_b64_tr_b16 v[82:83], v249 offset:41728\n"
"v_mfma_f32_16x16x32_bf16 a[80:83], v[168:171], v[212:215], a[80:83]\n"
"v_mfma_f32_16x16x32_bf16 a[104:107], v[136:139], v[208:211], a[104:107]\n"
"ds_read_b64_tr_b16 v[84:85], v249 offset:46080\n"
"v_mfma_f32_16x16x32_bf16 a[104:107], v[172:175], v[212:215], a[104:107]\n"
"buffer_load_dwordx4 v[204:207], v255, s[44:47], s62 offen offset:3072 sc1\n"
"v_mfma_f32_16x16x32_bf16 a[128:131], v[140:143], v[208:211], a[128:131]\n"
"ds_read_b64_tr_b16 v[86:87], v249 offset:50688\n"
"v_mfma_f32_16x16x32_bf16 a[128:131], v[176:179], v[212:215], a[128:131]\n"
"v_mfma_f32_16x16x32_bf16 a[152:155], v[144:147], v[208:211], a[152:155]\n"
"ds_read_b64_tr_b16 v[88:89], v249 offset:46112\n"
"v_mfma_f32_16x16x32_bf16 a[152:155], v[180:183], v[212:215], a[152:155]\n"
"v_mfma_f32_16x16x32_bf16 a[176:179], v[148:151], v[208:211], a[176:179]\n"
"buffer_load_dwordx4 v246, s[48:51], s59 offen offset:2048 lds\n"
"v_mfma_f32_16x16x32_bf16 a[176:179], v[184:187], v[212:215], a[176:179]\n"
"ds_read_b64_tr_b16 v[90:91], v249 offset:50720\n"
"v_mfma_f32_16x16x32_bf16 a[200:203], v[152:155], v[208:211], a[200:203]\n"
"v_mfma_f32_16x16x32_bf16 a[200:203], v[188:191], v[212:215], a[200:203]\n"
"ds_read_b64_tr_b16 v[92:93], v249 offset:46144\n"
"v_mfma_f32_16x16x32_bf16 a[12:15], v[120:123], v[216:219], a[12:15]\n"
"buffer_load_dwordx4 v[208:211], v255, s[44:47], s61 offen offset:2048 sc1\n"
"v_mfma_f32_16x16x32_bf16 a[12:15], v[156:159], v[220:223], a[12:15]\n"
"ds_read_b64_tr_b16 v[94:95], v249 offset:50752\n"
"v_mfma_f32_16x16x32_bf16 a[36:39], v[124:127], v[216:219], a[36:39]\n"
"v_mfma_f32_16x16x32_bf16 a[36:39], v[160:163], v[220:223], a[36:39]\n"
"ds_read_b64_tr_b16 v[96:97], v249 offset:46176\n"
"v_mfma_f32_16x16x32_bf16 a[60:63], v[128:131], v[216:219], a[60:63]\n"
"v_mfma_f32_16x16x32_bf16 a[60:63], v[164:167], v[220:223], a[60:63]\n"
"buffer_load_dwordx4 v247, s[48:51], s59 offen offset:3072 lds\n"
"v_mfma_f32_16x16x32_bf16 a[84:87], v[132:135], v[216:219], a[84:87]\n"
"ds_read_b64_tr_b16 v[98:99], v249 offset:50784\n"
"v_mfma_f32_16x16x32_bf16 a[84:87], v[168:171], v[220:223], a[84:87]\n"
"v_mfma_f32_16x16x32_bf16 a[108:111], v[136:139], v[216:219], a[108:111]\n"
"ds_read_b64_tr_b16 v[100:101], v249 offset:46208\n"
"v_mfma_f32_16x16x32_bf16 a[108:111], v[172:175], v[220:223], a[108:111]\n"
"buffer_load_dwordx4 v[212:215], v255, s[44:47], s63 offen offset:0 sc1\n"
"v_mfma_f32_16x16x32_bf16 a[132:135], v[140:143], v[216:219], a[132:135]\n"
"ds_read_b64_tr_b16 v[102:103], v249 offset:50816\n"
"v_mfma_f32_16x16x32_bf16 a[132:135], v[176:179], v[220:223], a[132:135]\n"
"v_mfma_f32_16x16x32_bf16 a[156:159], v[144:147], v[216:219], a[156:159]\n"
"ds_read_b64_tr_b16 v[104:105], v249 offset:46240\n"
"v_mfma_f32_16x16x32_bf16 a[156:159], v[180:183], v[220:223], a[156:159]\n"
"s_add_u32 m0, s60, 26624\n"
"v_mfma_f32_16x16x32_bf16 a[180:183], v[148:151], v[216:219], a[180:183]\n"
"buffer_load_dwordx4 v248, s[48:51], s59 offen offset:0 lds\n"
"v_mfma_f32_16x16x32_bf16 a[180:183], v[184:187], v[220:223], a[180:183]\n"
"ds_read_b64_tr_b16 v[106:107], v249 offset:50848\n"
"v_mfma_f32_16x16x32_bf16 a[204:207], v[152:155], v[216:219], a[204:207]\n"
"v_mfma_f32_16x16x32_bf16 a[204:207], v[188:191], v[220:223], a[204:207]\n"
"ds_read_b64_tr_b16 v[108:109], v249 offset:46272\n"
"v_mfma_f32_16x16x32_bf16 a[16:19], v[120:123], v[224:227], a[16:19]\n"
"buffer_load_dwordx4 v[216:219], v253, s[44:47], s61 offen offset:0 sc1\n"
"v_mfma_f32_16x16x32_bf16 a[16:19], v[156:159], v[228:231], a[16:19]\n"
"ds_read_b64_tr_b16 v[110:111], v249 offset:50880\n"
"v_mfma_f32_16x16x32_bf16 a[40:43], v[124:127], v[224:227], a[40:43]\n"
"v_mfma_f32_16x16x32_bf16 a[40:43], v[160:163], v[228:231], a[40:43]\n"
"ds_read_b64_tr_b16 v[112:113], v249 offset:46304\n"
"v_mfma_f32_16x16x32_bf16 a[64:67], v[128:131], v[224:227], a[64:67]\n"
"v_mfma_f32_16x16x32_bf16 a[64:67], v[164:167], v[228:231], a[64:67]\n"
"ds_read_b64_tr_b16 v[114:115], v249 offset:50912\n"
"v_mfma_f32_16x16x32_bf16 a[88:91], v[132:135], v[224:227], a[88:91]\n"
"v_mfma_f32_16x16x32_bf16 a[88:91], v[168:171], v[228:231], a[88:91]\n"
"ds_read_b64_tr_b16 v[116:117], v249 offset:46336\n"
"v_mfma_f32_16x16x32_bf16 a[112:115], v[136:139], v[224:227], a[112:115]\n"
"v_mfma_f32_16x16x32_bf16 a[112:115], v[172:175], v[228:231], a[112:115]\n"
"buffer_load_dwordx4 v[220:223], v253, s[44:47], s62 offen offset:2048 sc1\n"
"v_mfma_f32_16x16x32_bf16 a[136:139], v[140:143], v[224:227], a[136:139]\n"
"ds_read_b64_tr_b16 v[118:119], v249 offset:50944\n"
"v_mfma_f32_16x16x32_bf16 a[136:139], v[176:179], v[228:231], a[136:139]\n"
"v_mfma_f32_16x16x32_bf16 a[160:163], v[144:147], v[224:227], a[160:163]\n"
"v_mfma_f32_16x16x32_bf16 a[160:163], v[180:183], v[228:231], a[160:163]\n"
"v_mfma_f32_16x16x32_bf16 a[184:187], v[148:151], v[224:227], a[184:187]\n"
"v_mfma_f32_16x16x32_bf16 a[184:187], v[184:187], v[228:231], a[184:187]\n"
"v_mfma_f32_16x16x32_bf16 a[208:211], v[152:155], v[224:227], a[208:211]\n"
"v_mfma_f32_16x16x32_bf16 a[208:211], v[188:191], v[228:231], a[208:211]\n"
"v_mfma_f32_16x16x32_bf16 a[20:23], v[120:123], v[232:235], a[20:23]\n"
"buffer_load_dwordx4 v[224:227], v253, s[44:47], s61 offen offset:1024 sc1\n"
"v_mfma_f32_16x16x32_bf16 a[20:23], v[156:159], v[236:239], a[20:23]\n"
"v_mfma_f32_16x16x32_bf16 a[44:47], v[124:127], v[232:235], a[44:47]\n"
"v_mfma_f32_16x16x32_bf16 a[44:47], v[160:163], v[236:239], a[44:47]\n"
"v_mfma_f32_16x16x32_bf16 a[68:71], v[128:131], v[232:235], a[68:71]\n"
"v_mfma_f32_16x16x32_bf16 a[68:71], v[164:167], v[236:239], a[68:71]\n"
"v_mfma_f32_16x16x32_bf16 a[92:95], v[132:135], v[232:235], a[92:95]\n"
"v_mfma_f32_16x16x32_bf16 a[92:95], v[168:171], v[236:239], a[92:95]\n"
"v_mfma_f32_16x16x32_bf16 a[116:119], v[136:139], v[232:235], a[116:119]\n"
"v_mfma_f32_16x16x32_bf16 a[116:119], v[172:175], v[236:239], a[116:119]\n"
"buffer_load_dwordx4 v[228:231], v253, s[44:47], s62 offen offset:3072 sc1\n"
"v_mfma_f32_16x16x32_bf16 a[140:143], v[140:143], v[232:235], a[140:143]\n"
"v_mfma_f32_16x16x32_bf16 a[140:143], v[176:179], v[236:239], a[140:143]\n"
"v_mfma_f32_16x16x32_bf16 a[164:167], v[144:147], v[232:235], a[164:167]\n"
"v_mfma_f32_16x16x32_bf16 a[164:167], v[180:183], v[236:239], a[164:167]\n"
"v_mfma_f32_16x16x32_bf16 a[188:191], v[148:151], v[232:235], a[188:191]\n"
"v_mfma_f32_16x16x32_bf16 a[188:191], v[184:187], v[236:239], a[188:191]\n"
"v_mfma_f32_16x16x32_bf16 a[212:215], v[152:155], v[232:235], a[212:215]\n"
"buffer_load_dwordx4 v[232:235], v253, s[44:47], s61 offen offset:2048 sc1\n"
"v_mfma_f32_16x16x32_bf16 a[212:215], v[188:191], v[236:239], a[212:215]\n"
"buffer_load_dwordx4 v[236:239], v253, s[44:47], s63 offen offset:0 sc1\n"
"s_sub_u32 s65, s65, 1\n"
"s_cmp_lg_u32 s65, 0\n"
"s_cbranch_scc1 .Lf_op\n"
"s_waitcnt vmcnt(6) lgkmcnt(0)\n"
"s_barrier\n"
"v_mfma_f32_16x16x32_bf16 a[0:3], v[48:51], v[0:3], a[0:3]\n"
"v_mfma_f32_16x16x32_bf16 a[0:3], v[84:87], v[4:7], a[0:3]\n"
"ds_read_b64_tr_b16 v[120:121], v249 offset:0\n"
"v_mfma_f32_16x16x32_bf16 a[24:27], v[52:55], v[0:3], a[24:27]\n"
"ds_read_b64_tr_b16 v[122:123], v249 offset:4608\n"
"v_mfma_f32_16x16x32_bf16 a[24:27], v[88:91], v[4:7], a[24:27]\n"
"ds_read_b64_tr_b16 v[124:125], v249 offset:32\n"
"v_mfma_f32_16x16x32_bf16 a[48:51], v[56:59], v[0:3], a[48:51]\n"
"ds_read_b64_tr_b16 v[126:127], v249 offset:4640\n"
"v_mfma_f32_16x16x32_bf16 a[48:51], v[92:95], v[4:7], a[48:51]\n"
"ds_read_b64_tr_b16 v[128:129], v249 offset:64\n"
"v_mfma_f32_16x16x32_bf16 a[72:75], v[60:63], v[0:3], a[72:75]\n"
"ds_read_b64_tr_b16 v[130:131], v249 offset:4672\n"
"v_mfma_f32_16x16x32_bf16 a[72:75], v[96:99], v[4:7], a[72:75]\n"
"ds_read_b64_tr_b16 v[132:133], v249 offset:96\n"
"v_mfma_f32_16x16x32_bf16 a[96:99], v[64:67], v[0:3], a[96:99]\n"
"ds_read_b64_tr_b16 v[134:135], v249 offset:4704\n"
"v_mfma_f32_16x16x32_bf16 a[96:99], v[100:103], v[4:7], a[96:99]\n"
"ds_read_b64_tr_b16 v[136:137], v249 offset:128\n"
"v_mfma_f32_16x16x32_bf16 a[120:123], v[68:71], v[0:3], a[120:123]\n"
"ds_read_b64_tr_b16 v[138:139], v249 offset:4736\n"
"v_mfma_f32_16x16x32_bf16 a[120:123], v[104:107], v[4:7], a[120:123]\n"
"ds_read_b64_tr_b16 v[140:141], v249 offset:160\n"
"v_mfma_f32_16x16x32_bf16 a[144:147], v[72:75], v[0:3], a[144:147]\n"
"ds_read_b64_tr_b16 v[142:143], v249 offset:4768\n"
"v_mfma_f32_16x16x32_bf16 a[144:147], v[108:111], v[4:7], a[144:147]\n"
"ds_read_b64_tr_b16 v[144:145], v249 offset:192\n"
"v_mfma_f32_16x16x32_bf16 a[168:171], v[76:79], v[0:3], a[168:171]\n"
"ds_read_b64_tr_b16 v[146:147], v249 offset:4800\n"
"v_mfma_f32_16x16x32_bf16 a[168:171], v[112:115], v[4:7], a[168:171]\n"
"ds_read_b64_tr_b16 v[148:149], v249 offset:224\n"
"v_mfma_f32_16x16x32_bf16 a[192:195], v[80:83], v[0:3], a[192:195]\n"
"ds_read_b64_tr_b16 v[150:151], v249 offset:4832\n"
"v_mfma_f32_16x16x32_bf16 a[192:195], v[116:119], v[4:7], a[192:195]\n"
"ds_read_b64_tr_b16 v[152:153], v249 offset:256\n"
"v_mfma_f32_16x16x32_bf16 a[4:7], v[48:51], v[8:11], a[4:7]\n"
"ds_read_b64_tr_b16 v[154:155], v249 offset:4864\n"
"v_mfma_f32_16x16x32_bf16 a[4:7], v[84:87], v[12:15], a[4:7]\n"
"ds_read_b64_tr_b16 v[156:157], v249 offset:9216\n"
"v_mfma_f32_16x16x32_bf16 a[28:31], v[52:55], v[8:11], a[28:31]\n"
"ds_read_b64_tr_b16 v[158:159], v249 offset:13824\n"
"v_mfma_f32_16x16x32_bf16 a[28:31], v[88:91], v[12:15], a[28:31]\n"
"ds_read_b64_tr_b16 v[160:161], v249 offset:9248\n"
"v_mfma_f32_16x16x32_bf16 a[52:55], v[56:59], v[8:11], a[52:55]\n"
"ds_read_b64_tr_b16 v[162:163], v249 offset:13856\n"
"v_mfma_f32_16x16x32_bf16 a[52:55], v[92:95], v[12:15], a[52:55]\n"
"ds_read_b64_tr_b16 v[164:165], v249 offset:9280\n"
"v_mfma_f32_16x16x32_bf16 a[76:79], v[60:63], v[8:11], a[76:79]\n"
"ds_read_b64_tr_b16 v[166:167], v249 offset:13888\n"
"v_mfma_f32_16x16x32_bf16 a[76:79], v[96:99], v[12:15], a[76:79]\n"
"ds_read_b64_tr_b16 v[168:169], v249 offset:9312\n"
"v_mfma_f32_16x16x32_bf16 a[100:103], v[64:67], v[8:11], a[100:103]\n"
"ds_read_b64_tr_b16 v[170:171], v249 offset:13920\n"
"v_mfma_f32_16x16x32_bf16 a[100:103], v[100:103], v[12:15], a[100:103]\n"
"ds_read_b64_tr_b16 v[172:173], v249 offset:9344\n"
"v_mfma_f32_16x16x32_bf16 a[124:127], v[68:71], v[8:11], a[124:127]\n"
"ds_read_b64_tr_b16 v[174:175], v249 offset:13952\n"
"v_mfma_f32_16x16x32_bf16 a[124:127], v[104:107], v[12:15], a[124:127]\n"
"ds_read_b64_tr_b16 v[176:177], v249 offset:9376\n"
"v_mfma_f32_16x16x32_bf16 a[148:151], v[72:75], v[8:11], a[148:151]\n"
"ds_read_b64_tr_b16 v[178:179], v249 offset:13984\n"
"v_mfma_f32_16x16x32_bf16 a[148:151], v[108:111], v[12:15], a[148:151]\n"
"ds_read_b64_tr_b16 v[180:181], v249 offset:9408\n"
"v_mfma_f32_16x16x32_bf16 a[172:175], v[76:79], v[8:11], a[172:175]\n"
"ds_read_b64_tr_b16 v[182:183], v249 offset:14016\n"
"v_mfma_f32_16x16x32_bf16 a[172:175], v[112:115], v[12:15], a[172:175]\n"
"ds_read_b64_tr_b16 v[184:185], v249 offset:9440\n"
"v_mfma_f32_16x16x32_bf16 a[196:199], v[80:83], v[8:11], a[196:199]\n"
"ds_read_b64_tr_b16 v[186:187], v249 offset:14048\n"
"v_mfma_f32_16x16x32_bf16 a[196:199], v[116:119], v[12:15], a[196:199]\n"
"ds_read_b64_tr_b16 v[188:189], v249 offset:9472\n"
"v_mfma_f32_16x16x32_bf16 a[8:11], v[48:51], v[16:19], a[8:11]\n"
"ds_read_b64_tr_b16 v[190:191], v249 offset:14080\n"
"v_mfma_f32_16x16x32_bf16 a[8:11], v[84:87], v[20:23], a[8:11]\n"
"v_mfma_f32_16x16x32_bf16 a[32:35], v[52:55], v[16:19], a[32:35]\n"
"s_lshr_b32 s73, %5, 1\n"
"s_mul_i32 s73, s73, 14336\n"
"v_mfma_f32_16x16x32_bf16 a[32:35], v[88:91], v[20:23], a[32:35]\n"
"s_add_u32 s73, s73, 18432\n"
"s_add_u32 s74, s73, s64\n"
"v_mfma_f32_16x16x32_bf16 a[56:59], v[56:59], v[16:19], a[56:59]\n"
"v_add_u32 v240, s74, v250\n"
"s_and_b32 s75, %5, 1\n"
"v_mfma_f32_16x16x32_bf16 a[56:59], v[92:95], v[20:23], a[56:59]\n"
"s_xor_b32 s74, s75, 1\n"
"s_mul_i32 s65, s74, 55296\n"
"v_mfma_f32_16x16x32_bf16 a[80:83], v[60:63], v[16:19], a[80:83]\n"
"s_add_u32 s65, s65, %7\n"
"s_add_u32 s73, s73, s65\n"
"v_mfma_f32_16x16x32_bf16 a[80:83], v[96:99], v[20:23], a[80:83]\n"
"v_add_u32 v241, s73, v250\n"
"s_mul_i32 s73, s75, 14336\n"
"v_mfma_f32_16x16x32_bf16 a[104:107], v[64:67], v[16:19], a[104:107]\n"
"s_add_u32 s73, s73, 110592\n"
"s_add_u32 s73, s73, %7\n"
"v_mfma_f32_16x16x32_bf16 a[104:107], v[100:103], v[20:23], a[104:107]\n"
"s_mul_i32 s74, s74, 14336\n"
"s_add_u32 s74, s74, 110592\n"
"v_mfma_f32_16x16x32_bf16 a[128:131], v[68:71], v[16:19], a[128:131]\n"
"s_add_u32 s74, s74, %7\n"
"s_cmp_lt_u32 %5, 2\n"
"v_mfma_f32_16x16x32_bf16 a[128:131], v[104:107], v[20:23], a[128:131]\n"
"s_cselect_b32 s73, s64, s73\n"
"s_cselect_b32 s74, s65, s74\n"
"v_mfma_f32_16x16x32_bf16 a[152:155], v[72:75], v[16:19], a[152:155]\n"
"v_add_u32 v242, s73, v250\n"
"v_add_u32 v243, s74, v250\n"
"v_mfma_f32_16x16x32_bf16 a[152:155], v[108:111], v[20:23], a[152:155]\n"
"v_mfma_f32_16x16x32_bf16 a[176:179], v[76:79], v[16:19], a[176:179]\n"
"v_mfma_f32_16x16x32_bf16 a[176:179], v[112:115], v[20:23], a[176:179]\n"
"v_mfma_f32_16x16x32_bf16 a[200:203], v[80:83], v[16:19], a[200:203]\n"
"v_mfma_f32_16x16x32_bf16 a[200:203], v[116:119], v[20:23], a[200:203]\n"
"s_waitcnt vmcnt(0) lgkmcnt(0)\n"
"v_mfma_f32_16x16x32_bf16 a[0:3], v[120:123], v[192:195], a[0:3]\n"
"v_mfma_f32_16x16x32_bf16 a[0:3], v[156:159], v[196:199], a[0:3]\n"
"v_mfma_f32_16x16x32_bf16 a[24:27], v[124:127], v[192:195], a[24:27]\n"
"v_mfma_f32_16x16x32_bf16 a[24:27], v[160:163], v[196:199], a[24:27]\n"
"v_mfma_f32_16x16x32_bf16 a[48:51], v[128:131], v[192:195], a[48:51]\n"
"v_mfma_f32_16x16x32_bf16 a[48:51], v[164:167], v[196:199], a[48:51]\n"
"v_mfma_f32_16x16x32_bf16 a[72:75], v[132:135], v[192:195], a[72:75]\n"
"v_mfma_f32_16x16x32_bf16 a[72:75], v[168:171], v[196:199], a[72:75]\n"
"v_mfma_f32_16x16x32_bf16 a[96:99], v[136:139], v[192:195], a[96:99]\n"
"v_mfma_f32_16x16x32_bf16 a[96:99], v[172:175], v[196:199], a[96:99]\n"
"v_mfma_f32_16x16x32_bf16 a[120:123], v[140:143], v[192:195], a[120:123]\n"
"v_mfma_f32_16x16x32_bf16 a[120:123], v[176:179], v[196:199], a[120:123]\n"
"v_mfma_f32_16x16x32_bf16 a[144:147], v[144:147], v[192:195], a[144:147]\n"
"v_mfma_f32_16x16x32_bf16 a[144:147], v[180:183], v[196:199], a[144:147]\n"
"v_mfma_f32_16x16x32_bf16 a[168:171], v[148:151], v[192:195], a[168:171]\n"
"v_mfma_f32_16x16x32_bf16 a[168:171], v[184:187], v[196:199], a[168:171]\n"
"v_mfma_f32_16x16x32_bf16 a[192:195], v[152:155], v[192:195], a[192:195]\n"
"v_mfma_f32_16x16x32_bf16 a[192:195], v[188:191], v[196:199], a[192:195]\n"
"v_mfma_f32_16x16x32_bf16 a[4:7], v[120:123], v[200:203], a[4:7]\n"
"v_mfma_f32_16x16x32_bf16 a[4:7], v[156:159], v[204:207], a[4:7]\n"
"v_mfma_f32_16x16x32_bf16 a[28:31], v[124:127], v[200:203], a[28:31]\n"
"v_mfma_f32_16x16x32_bf16 a[28:31], v[160:163], v[204:207], a[28:31]\n"
"v_mfma_f32_16x16x32_bf16 a[52:55], v[128:131], v[200:203], a[52:55]\n"
"v_mfma_f32_16x16x32_bf16 a[52:55], v[164:167], v[204:207], a[52:55]\n"
"v_mfma_f32_16x16x32_bf16 a[76:79], v[132:135], v[200:203], a[76:79]\n"
"v_mfma_f32_16x16x32_bf16 a[76:79], v[168:171], v[204:207], a[76:79]\n"
"v_mfma_f32_16x16x32_bf16 a[100:103], v[136:139], v[200:203], a[100:103]\n"
"v_mfma_f32_16x16x32_bf16 a[100:103], v[172:175], v[204:207], a[100:103]\n"
"v_mfma_f32_16x16x32_bf16 a[124:127], v[140:143], v[200:203], a[124:127]\n"
"v_mfma_f32_16x16x32_bf16 a[124:127], v[176:179], v[204:207], a[124:127]\n"
"v_mfma_f32_16x16x32_bf16 a[148:151], v[144:147], v[200:203], a[148:151]\n"
"v_mfma_f32_16x16x32_bf16 a[148:151], v[180:183], v[204:207], a[148:151]\n"
"v_mfma_f32_16x16x32_bf16 a[172:175], v[148:151], v[200:203], a[172:175]\n"
"v_mfma_f32_16x16x32_bf16 a[172:175], v[184:187], v[204:207], a[172:175]\n"
"v_mfma_f32_16x16x32_bf16 a[196:199], v[152:155], v[200:203], a[196:199]\n"
"v_mfma_f32_16x16x32_bf16 a[196:199], v[188:191], v[204:207], a[196:199]\n"
"v_mfma_f32_16x16x32_bf16 a[8:11], v[120:123], v[208:211], a[8:11]\n"
"v_mfma_f32_16x16x32_bf16 a[8:11], v[156:159], v[212:215], a[8:11]\n"
"v_mfma_f32_16x16x32_bf16 a[32:35], v[124:127], v[208:211], a[32:35]\n"
"v_mfma_f32_16x16x32_bf16 a[32:35], v[160:163], v[212:215], a[32:35]\n"
"v_mfma_f32_16x16x32_bf16 a[56:59], v[128:131], v[208:211], a[56:59]\n"
"v_mfma_f32_16x16x32_bf16 a[56:59], v[164:167], v[212:215], a[56:59]\n"
"v_mfma_f32_16x16x32_bf16 a[80:83], v[132:135], v[208:211], a[80:83]\n"
"v_mfma_f32_16x16x32_bf16 a[80:83], v[168:171], v[212:215], a[80:83]\n"
"v_mfma_f32_16x16x32_bf16 a[104:107], v[136:139], v[208:211], a[104:107]\n"
"v_mfma_f32_16x16x32_bf16 a[104:107], v[172:175], v[212:215], a[104:107]\n"
"v_mfma_f32_16x16x32_bf16 a[128:131], v[140:143], v[208:211], a[128:131]\n"
"v_mfma_f32_16x16x32_bf16 a[128:131], v[176:179], v[212:215], a[128:131]\n"
"v_mfma_f32_16x16x32_bf16 a[152:155], v[144:147], v[208:211], a[152:155]\n"
"v_mfma_f32_16x16x32_bf16 a[152:155], v[180:183], v[212:215], a[152:155]\n"
"v_mfma_f32_16x16x32_bf16 a[176:179], v[148:151], v[208:211], a[176:179]\n"
"v_mfma_f32_16x16x32_bf16 a[176:179], v[184:187], v[212:215], a[176:179]\n"
"v_mfma_f32_16x16x32_bf16 a[200:203], v[152:155], v[208:211], a[200:203]\n"
"v_mfma_f32_16x16x32_bf16 a[200:203], v[188:191], v[212:215], a[200:203]\n"
"s_cmp_eq_u32 s75, 1\n"
"s_cbranch_scc1 .Lf_fk1\n"
"v_mfma_f32_16x16x32_bf16 a[12:15], v[48:51], v[24:27], a[12:15]\n"
"v_mfma_f32_16x16x32_bf16 a[12:15], v[84:87], v[28:31], a[12:15]\n"
"v_mfma_f32_16x16x32_bf16 a[36:39], v[52:55], v[24:27], a[36:39]\n"
"v_mfma_f32_16x16x32_bf16 a[36:39], v[88:91], v[28:31], a[36:39]\n"
"v_mfma_f32_16x16x32_bf16 a[60:63], v[56:59], v[24:27], a[60:63]\n"
"ds_write_b128 v240, a[124:127] offset:0\n"
"v_mfma_f32_16x16x32_bf16 a[60:63], v[92:95], v[28:31], a[60:63]\n"
"v_mfma_f32_16x16x32_bf16 a[84:87], v[60:63], v[24:27], a[84:87]\n"
"v_mfma_f32_16x16x32_bf16 a[84:87], v[96:99], v[28:31], a[84:87]\n"
"ds_write_b128 v240, a[148:151] offset:1024\n"
"v_mfma_f32_16x16x32_bf16 a[108:111], v[64:67], v[24:27], a[108:111]\n"
"v_mfma_f32_16x16x32_bf16 a[108:111], v[100:103], v[28:31], a[108:111]\n"
"v_mfma_f32_16x16x32_bf16 a[132:135], v[68:71], v[24:27], a[132:135]\n"
"ds_write_b128 v240, a[172:175] offset:2048\n"
"v_mfma_f32_16x16x32_bf16 a[132:135], v[104:107], v[28:31], a[132:135]\n"
"v_mfma_f32_16x16x32_bf16 a[156:159], v[72:75], v[24:27], a[156:159]\n"
"v_mfma_f32_16x16x32_bf16 a[156:159], v[108:111], v[28:31], a[156:159]\n"
"ds_write_b128 v240, a[196:199] offset:3072\n"
"v_mfma_f32_16x16x32_bf16 a[180:183], v[76:79], v[24:27], a[180:183]\n"
"v_mfma_f32_16x16x32_bf16 a[180:183], v[112:115], v[28:31], a[180:183]\n"
"v_mfma_f32_16x16x32_bf16 a[204:207], v[80:83], v[24:27], a[204:207]\n"
"ds_write_b128 v240, a[8:11] offset:4096\n"
"v_mfma_f32_16x16x32_bf16 a[204:207], v[116:119], v[28:31], a[204:207]\n"
"v_mfma_f32_16x16x32_bf16 a[16:19], v[48:51], v[32:35], a[16:19]\n"
"v_mfma_f32_16x16x32_bf16 a[16:19], v[84:87], v[36:39], a[16:19]\n"
"ds_write_b128 v240, a[32:35] offset:5120\n"
"v_mfma_f32_16x16x32_bf16 a[40:43], v[52:55], v[32:35], a[40:43]\n"
"v_mfma_f32_16x16x32_bf16 a[40:43], v[88:91], v[36:39], a[40:43]\n"
"v_mfma_f32_16x16x32_bf16 a[64:67], v[56:59], v[32:35], a[64:67]\n"
"ds_write_b128 v240, a[56:59] offset:6144\n"
"v_mfma_f32_16x16x32_bf16 a[64:67], v[92:95], v[36:39], a[64:67]\n"
"v_mfma_f32_16x16x32_bf16 a[88:91], v[60:63], v[32:35], a[88:91]\n"
"v_mfma_f32_16x16x32_bf16 a[88:91], v[96:99], v[36:39], a[88:91]\n"
"ds_write_b128 v240, a[80:83] offset:7168\n"
"v_mfma_f32_16x16x32_bf16 a[112:115], v[64:67], v[32:35], a[112:115]\n"
"v_mfma_f32_16x16x32_bf16 a[112:115], v[100:103], v[36:39], a[112:115]\n"
"v_mfma_f32_16x16x32_bf16 a[136:139], v[68:71], v[32:35], a[136:139]\n"
"ds_write_b128 v240, a[104:107] offset:8192\n"
"v_mfma_f32_16x16x32_bf16 a[136:139], v[104:107], v[36:39], a[136:139]\n"
"v_mfma_f32_16x16x32_bf16 a[160:163], v[72:75], v[32:35], a[160:163]\n"
"v_mfma_f32_16x16x32_bf16 a[160:163], v[108:111], v[36:39], a[160:163]\n"
"ds_write_b128 v240, a[128:131] offset:9216\n"
"v_mfma_f32_16x16x32_bf16 a[184:187], v[76:79], v[32:35], a[184:187]\n"
"v_mfma_f32_16x16x32_bf16 a[184:187], v[112:115], v[36:39], a[184:187]\n"
"v_mfma_f32_16x16x32_bf16 a[208:211], v[80:83], v[32:35], a[208:211]\n"
"ds_write_b128 v240, a[152:155] offset:10240\n"
"v_mfma_f32_16x16x32_bf16 a[208:211], v[116:119], v[36:39], a[208:211]\n"
"v_mfma_f32_16x16x32_bf16 a[20:23], v[48:51], v[40:43], a[20:23]\n"
"v_mfma_f32_16x16x32_bf16 a[20:23], v[84:87], v[44:47], a[20:23]\n"
"ds_write_b128 v240, a[176:179] offset:11264\n"
"v_mfma_f32_16x16x32_bf16 a[44:47], v[52:55], v[40:43], a[44:47]\n"
"v_mfma_f32_16x16x32_bf16 a[44:47], v[88:91], v[44:47], a[44:47]\n"
"v_mfma_f32_16x16x32_bf16 a[68:71], v[56:59], v[40:43], a[68:71]\n"
"ds_write_b128 v240, a[200:203] offset:12288\n"
"v_mfma_f32_16x16x32_bf16 a[68:71], v[92:95], v[44:47], a[68:71]\n"
"v_mfma_f32_16x16x32_bf16 a[92:95], v[60:63], v[40:43], a[92:95]\n"
"v_mfma_f32_16x16x32_bf16 a[92:95], v[96:99], v[44:47], a[92:95]\n"
"v_mfma_f32_16x16x32_bf16 a[116:119], v[64:67], v[40:43], a[116:119]\n"
"v_mfma_f32_16x16x32_bf16 a[116:119], v[100:103], v[44:47], a[116:119]\n"
"v_mfma_f32_16x16x32_bf16 a[140:143], v[68:71], v[40:43], a[140:143]\n"
"v_mfma_f32_16x16x32_bf16 a[140:143], v[104:107], v[44:47], a[140:143]\n"
"v_mfma_f32_16x16x32_bf16 a[164:167], v[72:75], v[40:43], a[164:167]\n"
"v_mfma_f32_16x16x32_bf16 a[164:167], v[108:111], v[44:47], a[164:167]\n"
"v_mfma_f32_16x16x32_bf16 a[188:191], v[76:79], v[40:43], a[188:191]\n"
"v_mfma_f32_16x16x32_bf16 a[188:191], v[112:115], v[44:47], a[188:191]\n"
"v_mfma_f32_16x16x32_bf16 a[212:215], v[80:83], v[40:43], a[212:215]\n"
"v_mfma_f32_16x16x32_bf16 a[212:215], v[116:119], v[44:47], a[212:215]\n"
"s_waitcnt lgkmcnt(0)\n"
"s_barrier\n"
"v_mfma_f32_16x16x32_bf16 a[12:15], v[120:123], v[216:219], a[12:15]\n"
"ds_read_b128 v[48:51], v241 offset:0\n"
"v_mfma_f32_16x16x32_bf16 a[12:15], v[156:159], v[220:223], a[12:15]\n"
"ds_read_b128 v[52:55], v241 offset:1024\n"
"v_mfma_f32_16x16x32_bf16 a[36:39], v[124:127], v[216:219], a[36:39]\n"
"ds_read_b128 v[56:59], v241 offset:2048\n"
"v_accvgpr_read_b32 v0, a0\n"
"v_accvgpr_read_b32 v1, a1\n"
"v_mfma_f32_16x16x32_bf16 a[36:39], v[160:163], v[220:223], a[36:39]\n"
"ds_read_b128 v[60:63], v241 offset:3072\n"
"v_accvgpr_read_b32 v2, a2\n"
"v_accvgpr_read_b32 v3, a3\n"
"v_mfma_f32_16x16x32_bf16 a[60:63], v[128:131], v[216:219], a[60:63]\n"
"ds_read_b128 v[64:67], v241 offset:4096\n"
"v_mfma_f32_16x16x32_bf16 a[60:63], v[164:167], v[220:223], a[60:63]\n"
"ds_read_b128 v[68:71], v241 offset:5120\n"
"v_accvgpr_read_b32 v4, a24\n"
"v_accvgpr_read_b32 v5, a25\n"
"v_mfma_f32_16x16x32_bf16 a[84:87], v[132:135], v[216:219], a[84:87]\n"
"ds_read_b128 v[72:75], v241 offset:6144\n"
"s_waitcnt lgkmcnt(6)\n"
"v_add_f32 v48, v48, v0\n"
"v_add_f32 v49, v49, v1\n"
"v_accvgpr_read_b32 v6, a26\n"
"v_accvgpr_read_b32 v7, a27\n"
"v_mfma_f32_16x16x32_bf16 a[84:87], v[168:171], v[220:223], a[84:87]\n"
"ds_read_b128 v[76:79], v241 offset:7168\n"
"v_add_f32 v50, v50, v2\n"
"v_add_f32 v51, v51, v3\n"
"v_mfma_f32_16x16x32_bf16 a[108:111], v[136:139], v[216:219], a[108:111]\n"
"ds_read_b128 v[80:83], v241 offset:8192\n"
"v_cvt_pk_bf16_f32 v48, v48, v49\n"
"v_cvt_pk_bf16_f32 v49, v50, v51\n"
"v_accvgpr_read_b32 v8, a48\n"
"v_accvgpr_read_b32 v9, a49\n"
"v_mfma_f32_16x16x32_bf16 a[108:111], v[172:175], v[220:223], a[108:111]\n"
"ds_read_b128 v[84:87], v241 offset:9216\n"
"s_waitcnt lgkmcnt(8)\n"
"v_add_f32 v52, v52, v4\n"
"v_add_f32 v53, v53, v5\n"
"v_accvgpr_read_b32 v10, a50\n"
"v_accvgpr_read_b32 v11, a51\n"
"v_mfma_f32_16x16x32_bf16 a[132:135], v[140:143], v[216:219], a[132:135]\n"
"ds_read_b128 v[88:91], v241 offset:10240\n"
"v_add_f32 v54, v54, v6\n"
"v_add_f32 v55, v55, v7\n"
"v_mfma_f32_16x16x32_bf16 a[132:135], v[176:179], v[220:223], a[132:135]\n"
"ds_read_b128 v[92:95], v241 offset:11264\n"
"v_cvt_pk_bf16_f32 v50, v52, v53\n"
"v_cvt_pk_bf16_f32 v51, v54, v55\n"
"v_accvgpr_read_b32 v12, a72\n"
"v_accvgpr_read_b32 v13, a73\n"
"v_mfma_f32_16x16x32_bf16 a[156:159], v[144:147], v[216:219], a[156:159]\n"
"ds_read_b128 v[96:99], v241 offset:12288\n"
"s_mov_b32 s73, 0\n"
"s_waitcnt lgkmcnt(10)\n"
"v_add_f32 v56, v56, v8\n"
"v_add_f32 v57, v57, v9\n"
"v_accvgpr_read_b32 v14, a74\n"
"v_accvgpr_read_b32 v15, a75\n"
"v_mfma_f32_16x16x32_bf16 a[156:159], v[180:183], v[220:223], a[156:159]\n"
"ds_read_b128 v[100:103], v241 offset:13312\n"
"buffer_store_dwordx4 v[48:51], v250, s[40:43], s73 offen offset:0 sc1\n"
"v_add_f32 v58, v58, v10\n"
"v_add_f32 v59, v59, v11\n"
"v_mfma_f32_16x16x32_bf16 a[180:183], v[148:151], v[216:219], a[180:183]\n"
"v_cvt_pk_bf16_f32 v56, v56, v57\n"
"v_cvt_pk_bf16_f32 v57, v58, v59\n"
"v_accvgpr_read_b32 v16, a96\n"
"v_accvgpr_read_b32 v17, a97\n"
"v_mfma_f32_16x16x32_bf16 a[180:183], v[184:187], v[220:223], a[180:183]\n"
"s_waitcnt lgkmcnt(10)\n"
"v_add_f32 v60, v60, v12\n"
"v_add_f32 v61, v61, v13\n"
"v_accvgpr_read_b32 v18, a98\n"
"v_accvgpr_read_b32 v19, a99\n"
"v_mfma_f32_16x16x32_bf16 a[204:207], v[152:155], v[216:219], a[204:207]\n"
"v_add_f32 v62, v62, v14\n"
"v_add_f32 v63, v63, v15\n"
"v_mfma_f32_16x16x32_bf16 a[204:207], v[188:191], v[220:223], a[204:207]\n"
"v_cvt_pk_bf16_f32 v58, v60, v61\n"
"v_cvt_pk_bf16_f32 v59, v62, v63\n"
"v_accvgpr_read_b32 v20, a120\n"
"v_accvgpr_read_b32 v21, a121\n"
"v_mfma_f32_16x16x32_bf16 a[16:19], v[120:123], v[224:227], a[16:19]\n"
"s_waitcnt lgkmcnt(9)\n"
"v_add_f32 v64, v64, v16\n"
"v_add_f32 v65, v65, v17\n"
"v_accvgpr_read_b32 v22, a122\n"
"v_accvgpr_read_b32 v23, a123\n"
"v_mfma_f32_16x16x32_bf16 a[16:19], v[156:159], v[228:231], a[16:19]\n"
"buffer_store_dwordx4 v[56:59], v250, s[40:43], s73 offen offset:1024 sc1\n"
"v_add_f32 v66, v66, v18\n"
"v_add_f32 v67, v67, v19\n"
"v_mfma_f32_16x16x32_bf16 a[40:43], v[124:127], v[224:227], a[40:43]\n"
"v_cvt_pk_bf16_f32 v64, v64, v65\n"
"v_cvt_pk_bf16_f32 v65, v66, v67\n"
"v_accvgpr_read_b32 v192, a144\n"
"v_accvgpr_read_b32 v193, a145\n"
"v_mfma_f32_16x16x32_bf16 a[40:43], v[160:163], v[228:231], a[40:43]\n"
"s_waitcnt lgkmcnt(8)\n"
"v_add_f32 v68, v68, v20\n"
"v_add_f32 v69, v69, v21\n"
"v_accvgpr_read_b32 v194, a146\n"
"v_accvgpr_read_b32 v195, a147\n"
"v_mfma_f32_16x16x32_bf16 a[64:67], v[128:131], v[224:227], a[64:67]\n"
"v_add_f32 v70, v70, v22\n"
"v_add_f32 v71, v71, v23\n"
"v_mfma_f32_16x16x32_bf16 a[64:67], v[164:167], v[228:231], a[64:67]\n"
"v_cvt_pk_bf16_f32 v66, v68, v69\n"
"v_cvt_pk_bf16_f32 v67, v70, v71\n"
"v_accvgpr_read_b32 v196, a168\n"
"v_accvgpr_read_b32 v197, a169\n"
"v_mfma_f32_16x16x32_bf16 a[88:91], v[132:135], v[224:227], a[88:91]\n"
"s_waitcnt lgkmcnt(7)\n"
"v_add_f32 v72, v72, v192\n"
"v_add_f32 v73, v73, v193\n"
"v_accvgpr_read_b32 v198, a170\n"
"v_accvgpr_read_b32 v199, a171\n"
"v_mfma_f32_16x16x32_bf16 a[88:91], v[168:171], v[228:231], a[88:91]\n"
"buffer_store_dwordx4 v[64:67], v250, s[40:43], s73 offen offset:2048 sc1\n"
"v_add_f32 v74, v74, v194\n"
"v_add_f32 v75, v75, v195\n"
"v_mfma_f32_16x16x32_bf16 a[112:115], v[136:139], v[224:227], a[112:115]\n"
"v_cvt_pk_bf16_f32 v72, v72, v73\n"
"v_cvt_pk_bf16_f32 v73, v74, v75\n"
"v_accvgpr_read_b32 v200, a192\n"
"v_accvgpr_read_b32 v201, a193\n"
"v_mfma_f32_16x16x32_bf16 a[112:115], v[172:175], v[228:231], a[112:115]\n"
"s_waitcnt lgkmcnt(6)\n"
"v_add_f32 v76, v76, v196\n"
"v_add_f32 v77, v77, v197\n"
"v_accvgpr_read_b32 v202, a194\n"
"v_accvgpr_read_b32 v203, a195\n"
"v_mfma_f32_16x16x32_bf16 a[136:139], v[140:143], v[224:227], a[136:139]\n"
"v_add_f32 v78, v78, v198\n"
"v_add_f32 v79, v79, v199\n"
"v_mfma_f32_16x16x32_bf16 a[136:139], v[176:179], v[228:231], a[136:139]\n"
"v_cvt_pk_bf16_f32 v74, v76, v77\n"
"v_cvt_pk_bf16_f32 v75, v78, v79\n"
"v_accvgpr_read_b32 v204, a4\n"
"v_accvgpr_read_b32 v205, a5\n"
"v_mfma_f32_16x16x32_bf16 a[160:163], v[144:147], v[224:227], a[160:163]\n"
"s_waitcnt lgkmcnt(5)\n"
"v_add_f32 v80, v80, v200\n"
"v_add_f32 v81, v81, v201\n"
"v_accvgpr_read_b32 v206, a6\n"
"v_accvgpr_read_b32 v207, a7\n"
"v_mfma_f32_16x16x32_bf16 a[160:163], v[180:183], v[228:231], a[160:163]\n"
"buffer_store_dwordx4 v[72:75], v250, s[40:43], s73 offen offset:3072 sc1\n"
"v_add_f32 v82, v82, v202\n"
"v_add_f32 v83, v83, v203\n"
"v_mfma_f32_16x16x32_bf16 a[184:187], v[148:151], v[224:227], a[184:187]\n"
"v_cvt_pk_bf16_f32 v80, v80, v81\n"
"v_cvt_pk_bf16_f32 v81, v82, v83\n"
"v_accvgpr_read_b32 v208, a28\n"
"v_accvgpr_read_b32 v209, a29\n"
"v_mfma_f32_16x16x32_bf16 a[184:187], v[184:187], v[228:231], a[184:187]\n"
"s_waitcnt lgkmcnt(4)\n"
"v_add_f32 v84, v84, v204\n"
"v_add_f32 v85, v85, v205\n"
"v_accvgpr_read_b32 v210, a30\n"
"v_accvgpr_read_b32 v211, a31\n"
"v_mfma_f32_16x16x32_bf16 a[208:211], v[152:155], v[224:227], a[208:211]\n"
"v_add_f32 v86, v86, v206\n"
"v_add_f32 v87, v87, v207\n"
"v_mfma_f32_16x16x32_bf16 a[208:211], v[188:191], v[228:231], a[208:211]\n"
"v_cvt_pk_bf16_f32 v82, v84, v85\n"
"v_cvt_pk_bf16_f32 v83, v86, v87\n"
"v_accvgpr_read_b32 v212, a52\n"
"v_accvgpr_read_b32 v213, a53\n"
"v_mfma_f32_16x16x32_bf16 a[20:23], v[120:123], v[232:235], a[20:23]\n"
"s_mov_b32 s73, 4096\n"
"s_waitcnt lgkmcnt(3)\n"
"v_add_f32 v88, v88, v208\n"
"v_add_f32 v89, v89, v209\n"
"v_accvgpr_read_b32 v214, a54\n"
"v_accvgpr_read_b32 v215, a55\n"
"v_mfma_f32_16x16x32_bf16 a[20:23], v[156:159], v[236:239], a[20:23]\n"
"buffer_store_dwordx4 v[80:83], v250, s[40:43], s73 offen offset:0 sc1\n"
"v_add_f32 v90, v90, v210\n"
"v_add_f32 v91, v91, v211\n"
"v_mfma_f32_16x16x32_bf16 a[44:47], v[124:127], v[232:235], a[44:47]\n"
"v_cvt_pk_bf16_f32 v88, v88, v89\n"
"v_cvt_pk_bf16_f32 v89, v90, v91\n"
"v_accvgpr_read_b32 v0, a76\n"
"v_accvgpr_read_b32 v1, a77\n"
"v_mfma_f32_16x16x32_bf16 a[44:47], v[160:163], v[236:239], a[44:47]\n"
"s_waitcnt lgkmcnt(2)\n"
"v_add_f32 v92, v92, v212\n"
"v_add_f32 v93, v93, v213\n"
"v_accvgpr_read_b32 v2, a78\n"
"v_accvgpr_read_b32 v3, a79\n"
"v_mfma_f32_16x16x32_bf16 a[68:71], v[128:131], v[232:235], a[68:71]\n"
"v_add_f32 v94, v94, v214\n"
"v_add_f32 v95, v95, v215\n"
"v_mfma_f32_16x16x32_bf16 a[68:71], v[164:167], v[236:239], a[68:71]\n"
"v_cvt_pk_bf16_f32 v90, v92, v93\n"
"v_cvt_pk_bf16_f32 v91, v94, v95\n"
"v_accvgpr_read_b32 v4, a100\n"
"v_accvgpr_read_b32 v5, a101\n"
"v_mfma_f32_16x16x32_bf16 a[92:95], v[132:135], v[232:235], a[92:95]\n"
"s_waitcnt lgkmcnt(1)\n"
"v_add_f32 v96, v96, v0\n"
"v_add_f32 v97, v97, v1\n"
"v_accvgpr_read_b32 v6, a102\n"
"v_accvgpr_read_b32 v7, a103\n"
"v_mfma_f32_16x16x32_bf16 a[92:95], v[168:171], v[236:239], a[92:95]\n"
"buffer_store_dwordx4 v[88:91], v250, s[40:43], s73 offen offset:1024 sc1\n"
"v_add_f32 v98, v98, v2\n"
"v_add_f32 v99, v99, v3\n"
"v_mfma_f32_16x16x32_bf16 a[116:119], v[136:139], v[232:235], a[116:119]\n"
"v_cvt_pk_bf16_f32 v96, v96, v97\n"
"v_cvt_pk_bf16_f32 v97, v98, v99\n"
"v_mfma_f32_16x16x32_bf16 a[116:119], v[172:175], v[236:239], a[116:119]\n"
"s_waitcnt lgkmcnt(0)\n"
"v_add_f32 v100, v100, v4\n"
"v_add_f32 v101, v101, v5\n"
"v_mfma_f32_16x16x32_bf16 a[140:143], v[140:143], v[232:235], a[140:143]\n"
"v_add_f32 v102, v102, v6\n"
"v_add_f32 v103, v103, v7\n"
"v_mfma_f32_16x16x32_bf16 a[140:143], v[176:179], v[236:239], a[140:143]\n"
"v_cvt_pk_bf16_f32 v98, v100, v101\n"
"v_cvt_pk_bf16_f32 v99, v102, v103\n"
"v_mfma_f32_16x16x32_bf16 a[164:167], v[144:147], v[232:235], a[164:167]\n"
"v_mfma_f32_16x16x32_bf16 a[164:167], v[180:183], v[236:239], a[164:167]\n"
"buffer_store_dwordx4 v[96:99], v250, s[40:43], s73 offen offset:2048 sc1\n"
"v_mfma_f32_16x16x32_bf16 a[188:191], v[148:151], v[232:235], a[188:191]\n"
"v_mfma_f32_16x16x32_bf16 a[188:191], v[184:187], v[236:239], a[188:191]\n"
"v_mfma_f32_16x16x32_bf16 a[212:215], v[152:155], v[232:235], a[212:215]\n"
"v_mfma_f32_16x16x32_bf16 a[212:215], v[188:191], v[236:239], a[212:215]\n"
"ds_write_b128 v242, a[136:139] offset:0\n"
"ds_write_b128 v242, a[160:163] offset:1024\n"
"ds_write_b128 v242, a[184:187] offset:2048\n"
"ds_write_b128 v242, a[208:211] offset:3072\n"
"ds_write_b128 v242, a[20:23] offset:4096\n"
"ds_write_b128 v242, a[44:47] offset:5120\n"
"ds_write_b128 v242, a[68:71] offset:6144\n"
"ds_write_b128 v242, a[92:95] offset:7168\n"
"ds_write_b128 v242, a[116:119] offset:8192\n"
"ds_write_b128 v242, a[140:143] offset:9216\n"
"ds_write_b128 v242, a[164:167] offset:10240\n"
"ds_write_b128 v242, a[188:191] offset:11264\n"
"ds_write_b128 v242, a[212:215] offset:12288\n"
"v_accvgpr_read_b32 v0, a12\n"
"v_accvgpr_read_b32 v1, a13\n"
"v_accvgpr_read_b32 v2, a14\n"
"v_accvgpr_read_b32 v3, a15\n"
"v_accvgpr_read_b32 v4, a36\n"
"v_accvgpr_read_b32 v5, a37\n"
"v_accvgpr_read_b32 v6, a38\n"
"v_accvgpr_read_b32 v7, a39\n"
"v_accvgpr_read_b32 v8, a60\n"
"v_accvgpr_read_b32 v9, a61\n"
"v_accvgpr_read_b32 v10, a62\n"
"v_accvgpr_read_b32 v11, a63\n"
"v_accvgpr_read_b32 v12, a84\n"
"v_accvgpr_read_b32 v13, a85\n"
"v_accvgpr_read_b32 v14, a86\n"
"v_accvgpr_read_b32 v15, a87\n"
"v_accvgpr_read_b32 v16, a108\n"
"v_accvgpr_read_b32 v17, a109\n"
"v_accvgpr_read_b32 v18, a110\n"
"v_accvgpr_read_b32 v19, a111\n"
"v_accvgpr_read_b32 v20, a132\n"
"v_accvgpr_read_b32 v21, a133\n"
"v_accvgpr_read_b32 v22, a134\n"
"v_accvgpr_read_b32 v23, a135\n"
"v_accvgpr_read_b32 v24, a156\n"
"v_accvgpr_read_b32 v25, a157\n"
"v_accvgpr_read_b32 v26, a158\n"
"v_accvgpr_read_b32 v27, a159\n"
"v_accvgpr_read_b32 v28, a180\n"
"v_accvgpr_read_b32 v29, a181\n"
"v_accvgpr_read_b32 v30, a182\n"
"v_accvgpr_read_b32 v31, a183\n"
"v_accvgpr_read_b32 v32, a204\n"
"v_accvgpr_read_b32 v33, a205\n"
"v_accvgpr_read_b32 v34, a206\n"
"v_accvgpr_read_b32 v35, a207\n"
"v_accvgpr_read_b32 v36, a16\n"
"v_accvgpr_read_b32 v37, a17\n"
"v_accvgpr_read_b32 v38, a18\n"
"v_accvgpr_read_b32 v39, a19\n"
"v_accvgpr_read_b32 v40, a40\n"
"v_accvgpr_read_b32 v41, a41\n"
"v_accvgpr_read_b32 v42, a42\n"
"v_accvgpr_read_b32 v43, a43\n"
"v_accvgpr_read_b32 v44, a64\n"
"v_accvgpr_read_b32 v45, a65\n"
"v_accvgpr_read_b32 v46, a66\n"
"v_accvgpr_read_b32 v47, a67\n"
"v_accvgpr_read_b32 v48, a88\n"
"v_accvgpr_read_b32 v49, a89\n"
"v_accvgpr_read_b32 v50, a90\n"
"v_accvgpr_read_b32 v51, a91\n"
"v_accvgpr_read_b32 v52, a112\n"
"v_accvgpr_read_b32 v53, a113\n"
"v_accvgpr_read_b32 v54, a114\n"
"v_accvgpr_read_b32 v55, a115\n"
"s_branch .Lf_fk2\n"
".Lf_fk1:\n"
"v_mfma_f32_16x16x32_bf16 a[12:15], v[48:51], v[24:27], a[12:15]\n"
"v_mfma_f32_16x16x32_bf16 a[12:15], v[84:87], v[28:31], a[12:15]\n"
"v_mfma_f32_16x16x32_bf16 a[36:39], v[52:55], v[24:27], a[36:39]\n"
"v_mfma_f32_16x16x32_bf16 a[36:39], v[88:91], v[28:31], a[36:39]\n"
"v_mfma_f32_16x16x32_bf16 a[60:63], v[56:59], v[24:27], a[60:63]\n"
"ds_write_b128 v240, a[0:3] offset:0\n"
"v_mfma_f32_16x16x32_bf16 a[60:63], v[92:95], v[28:31], a[60:63]\n"
"v_mfma_f32_16x16x32_bf16 a[84:87], v[60:63], v[24:27], a[84:87]\n"
"v_mfma_f32_16x16x32_bf16 a[84:87], v[96:99], v[28:31], a[84:87]\n"
"ds_write_b128 v240, a[24:27] offset:1024\n"
"v_mfma_f32_16x16x32_bf16 a[108:111], v[64:67], v[24:27], a[108:111]\n"
"v_mfma_f32_16x16x32_bf16 a[108:111], v[100:103], v[28:31], a[108:111]\n"
"v_mfma_f32_16x16x32_bf16 a[132:135], v[68:71], v[24:27], a[132:135]\n"
"ds_write_b128 v240, a[48:51] offset:2048\n"
"v_mfma_f32_16x16x32_bf16 a[132:135], v[104:107], v[28:31], a[132:135]\n"
"v_mfma_f32_16x16x32_bf16 a[156:159], v[72:75], v[24:27], a[156:159]\n"
"v_mfma_f32_16x16x32_bf16 a[156:159], v[108:111], v[28:31], a[156:159]\n"
"ds_write_b128 v240, a[72:75] offset:3072\n"
"v_mfma_f32_16x16x32_bf16 a[180:183], v[76:79], v[24:27], a[180:183]\n"
"v_mfma_f32_16x16x32_bf16 a[180:183], v[112:115], v[28:31], a[180:183]\n"
"v_mfma_f32_16x16x32_bf16 a[204:207], v[80:83], v[24:27], a[204:207]\n"
"ds_write_b128 v240, a[96:99] offset:4096\n"
"v_mfma_f32_16x16x32_bf16 a[204:207], v[116:119], v[28:31], a[204:207]\n"
"v_mfma_f32_16x16x32_bf16 a[16:19], v[48:51], v[32:35], a[16:19]\n"
"v_mfma_f32_16x16x32_bf16 a[16:19], v[84:87], v[36:39], a[16:19]\n"
"ds_write_b128 v240, a[120:123] offset:5120\n"
"v_mfma_f32_16x16x32_bf16 a[40:43], v[52:55], v[32:35], a[40:43]\n"
"v_mfma_f32_16x16x32_bf16 a[40:43], v[88:91], v[36:39], a[40:43]\n"
"v_mfma_f32_16x16x32_bf16 a[64:67], v[56:59], v[32:35], a[64:67]\n"
"ds_write_b128 v240, a[144:147] offset:6144\n"
"v_mfma_f32_16x16x32_bf16 a[64:67], v[92:95], v[36:39], a[64:67]\n"
"v_mfma_f32_16x16x32_bf16 a[88:91], v[60:63], v[32:35], a[88:91]\n"
"v_mfma_f32_16x16x32_bf16 a[88:91], v[96:99], v[36:39], a[88:91]\n"
"ds_write_b128 v240, a[168:171] offset:7168\n"
"v_mfma_f32_16x16x32_bf16 a[112:115], v[64:67], v[32:35], a[112:115]\n"
"v_mfma_f32_16x16x32_bf16 a[112:115], v[100:103], v[36:39], a[112:115]\n"
"v_mfma_f32_16x16x32_bf16 a[136:139], v[68:71], v[32:35], a[136:139]\n"
"ds_write_b128 v240, a[192:195] offset:8192\n"
"v_mfma_f32_16x16x32_bf16 a[136:139], v[104:107], v[36:39], a[136:139]\n"
"v_mfma_f32_16x16x32_bf16 a[160:163], v[72:75], v[32:35], a[160:163]\n"
"v_mfma_f32_16x16x32_bf16 a[160:163], v[108:111], v[36:39], a[160:163]\n"
"ds_write_b128 v240, a[4:7] offset:9216\n"
"v_mfma_f32_16x16x32_bf16 a[184:187], v[76:79], v[32:35], a[184:187]\n"
"v_mfma_f32_16x16x32_bf16 a[184:187], v[112:115], v[36:39], a[184:187]\n"
"v_mfma_f32_16x16x32_bf16 a[208:211], v[80:83], v[32:35], a[208:211]\n"
"ds_write_b128 v240, a[28:31] offset:10240\n"
"v_mfma_f32_16x16x32_bf16 a[208:211], v[116:119], v[36:39], a[208:211]\n"
"v_mfma_f32_16x16x32_bf16 a[20:23], v[48:51], v[40:43], a[20:23]\n"
"v_mfma_f32_16x16x32_bf16 a[20:23], v[84:87], v[44:47], a[20:23]\n"
"ds_write_b128 v240, a[52:55] offset:11264\n"
"v_mfma_f32_16x16x32_bf16 a[44:47], v[52:55], v[40:43], a[44:47]\n"
"v_mfma_f32_16x16x32_bf16 a[44:47], v[88:91], v[44:47], a[44:47]\n"
"v_mfma_f32_16x16x32_bf16 a[68:71], v[56:59], v[40:43], a[68:71]\n"
"ds_write_b128 v240, a[76:79] offset:12288\n"
"v_mfma_f32_16x16x32_bf16 a[68:71], v[92:95], v[44:47], a[68:71]\n"
"v_mfma_f32_16x16x32_bf16 a[92:95], v[60:63], v[40:43], a[92:95]\n"
"v_mfma_f32_16x16x32_bf16 a[92:95], v[96:99], v[44:47], a[92:95]\n"
"ds_write_b128 v240, a[100:103] offset:13312\n"
"v_mfma_f32_16x16x32_bf16 a[116:119], v[64:67], v[40:43], a[116:119]\n"
"v_mfma_f32_16x16x32_bf16 a[116:119], v[100:103], v[44:47], a[116:119]\n"
"v_mfma_f32_16x16x32_bf16 a[140:143], v[68:71], v[40:43], a[140:143]\n"
"v_mfma_f32_16x16x32_bf16 a[140:143], v[104:107], v[44:47], a[140:143]\n"
"v_mfma_f32_16x16x32_bf16 a[164:167], v[72:75], v[40:43], a[164:167]\n"
"v_mfma_f32_16x16x32_bf16 a[164:167], v[108:111], v[44:47], a[164:167]\n"
"v_mfma_f32_16x16x32_bf16 a[188:191], v[76:79], v[40:43], a[188:191]\n"
"v_mfma_f32_16x16x32_bf16 a[188:191], v[112:115], v[44:47], a[188:191]\n"
"v_mfma_f32_16x16x32_bf16 a[212:215], v[80:83], v[40:43], a[212:215]\n"
"v_mfma_f32_16x16x32_bf16 a[212:215], v[116:119], v[44:47], a[212:215]\n"
"s_waitcnt lgkmcnt(0)\n"
"s_barrier\n"
"v_mfma_f32_16x16x32_bf16 a[12:15], v[120:123], v[216:219], a[12:15]\n"
"ds_read_b128 v[48:51], v241 offset:0\n"
"v_mfma_f32_16x16x32_bf16 a[12:15], v[156:159], v[220:223], a[12:15]\n"
"ds_read_b128 v[52:55], v241 offset:1024\n"
"v_mfma_f32_16x16x32_bf16 a[36:39], v[124:127], v[216:219], a[36:39]\n"
"ds_read_b128 v[56:59], v241 offset:2048\n"
"v_accvgpr_read_b32 v0, a124\n"
"v_accvgpr_read_b32 v1, a125\n"
"v_mfma_f32_16x16x32_bf16 a[36:39], v[160:163], v[220:223], a[36:39]\n"
"ds_read_b128 v[60:63], v241 offset:3072\n"
"v_accvgpr_read_b32 v2, a126\n"
"v_accvgpr_read_b32 v3, a127\n"
"v_mfma_f32_16x16x32_bf16 a[60:63], v[128:131], v[216:219], a[60:63]\n"
"ds_read_b128 v[64:67], v241 offset:4096\n"
"v_mfma_f32_16x16x32_bf16 a[60:63], v[164:167], v[220:223], a[60:63]\n"
"ds_read_b128 v[68:71], v241 offset:5120\n"
"v_accvgpr_read_b32 v4, a148\n"
"v_accvgpr_read_b32 v5, a149\n"
"v_mfma_f32_16x16x32_bf16 a[84:87], v[132:135], v[216:219], a[84:87]\n"
"ds_read_b128 v[72:75], v241 offset:6144\n"
"s_waitcnt lgkmcnt(6)\n"
"v_add_f32 v48, v48, v0\n"
"v_add_f32 v49, v49, v1\n"
"v_accvgpr_read_b32 v6, a150\n"
"v_accvgpr_read_b32 v7, a151\n"
"v_mfma_f32_16x16x32_bf16 a[84:87], v[168:171], v[220:223], a[84:87]\n"
"ds_read_b128 v[76:79], v241 offset:7168\n"
"v_add_f32 v50, v50, v2\n"
"v_add_f32 v51, v51, v3\n"
"v_mfma_f32_16x16x32_bf16 a[108:111], v[136:139], v[216:219], a[108:111]\n"
"ds_read_b128 v[80:83], v241 offset:8192\n"
"v_cvt_pk_bf16_f32 v48, v48, v49\n"
"v_cvt_pk_bf16_f32 v49, v50, v51\n"
"v_accvgpr_read_b32 v8, a172\n"
"v_accvgpr_read_b32 v9, a173\n"
"v_mfma_f32_16x16x32_bf16 a[108:111], v[172:175], v[220:223], a[108:111]\n"
"ds_read_b128 v[84:87], v241 offset:9216\n"
"s_waitcnt lgkmcnt(8)\n"
"v_add_f32 v52, v52, v4\n"
"v_add_f32 v53, v53, v5\n"
"v_accvgpr_read_b32 v10, a174\n"
"v_accvgpr_read_b32 v11, a175\n"
"v_mfma_f32_16x16x32_bf16 a[132:135], v[140:143], v[216:219], a[132:135]\n"
"ds_read_b128 v[88:91], v241 offset:10240\n"
"v_add_f32 v54, v54, v6\n"
"v_add_f32 v55, v55, v7\n"
"v_mfma_f32_16x16x32_bf16 a[132:135], v[176:179], v[220:223], a[132:135]\n"
"ds_read_b128 v[92:95], v241 offset:11264\n"
"v_cvt_pk_bf16_f32 v50, v52, v53\n"
"v_cvt_pk_bf16_f32 v51, v54, v55\n"
"v_accvgpr_read_b32 v12, a196\n"
"v_accvgpr_read_b32 v13, a197\n"
"v_mfma_f32_16x16x32_bf16 a[156:159], v[144:147], v[216:219], a[156:159]\n"
"ds_read_b128 v[96:99], v241 offset:12288\n"
"s_mov_b32 s73, 0\n"
"s_waitcnt lgkmcnt(10)\n"
"v_add_f32 v56, v56, v8\n"
"v_add_f32 v57, v57, v9\n"
"v_accvgpr_read_b32 v14, a198\n"
"v_accvgpr_read_b32 v15, a199\n"
"v_mfma_f32_16x16x32_bf16 a[156:159], v[180:183], v[220:223], a[156:159]\n"
"buffer_store_dwordx4 v[48:51], v250, s[40:43], s73 offen offset:0 sc1\n"
"v_add_f32 v58, v58, v10\n"
"v_add_f32 v59, v59, v11\n"
"v_mfma_f32_16x16x32_bf16 a[180:183], v[148:151], v[216:219], a[180:183]\n"
"v_cvt_pk_bf16_f32 v56, v56, v57\n"
"v_cvt_pk_bf16_f32 v57, v58, v59\n"
"v_accvgpr_read_b32 v16, a8\n"
"v_accvgpr_read_b32 v17, a9\n"
"v_mfma_f32_16x16x32_bf16 a[180:183], v[184:187], v[220:223], a[180:183]\n"
"s_waitcnt lgkmcnt(9)\n"
"v_add_f32 v60, v60, v12\n"
"v_add_f32 v61, v61, v13\n"
"v_accvgpr_read_b32 v18, a10\n"
"v_accvgpr_read_b32 v19, a11\n"
"v_mfma_f32_16x16x32_bf16 a[204:207], v[152:155], v[216:219], a[204:207]\n"
"v_add_f32 v62, v62, v14\n"
"v_add_f32 v63, v63, v15\n"
"v_mfma_f32_16x16x32_bf16 a[204:207], v[188:191], v[220:223], a[204:207]\n"
"v_cvt_pk_bf16_f32 v58, v60, v61\n"
"v_cvt_pk_bf16_f32 v59, v62, v63\n"
"v_accvgpr_read_b32 v20, a32\n"
"v_accvgpr_read_b32 v21, a33\n"
"v_mfma_f32_16x16x32_bf16 a[16:19], v[120:123], v[224:227], a[16:19]\n"
"s_waitcnt lgkmcnt(8)\n"
"v_add_f32 v64, v64, v16\n"
"v_add_f32 v65, v65, v17\n"
"v_accvgpr_read_b32 v22, a34\n"
"v_accvgpr_read_b32 v23, a35\n"
"v_mfma_f32_16x16x32_bf16 a[16:19], v[156:159], v[228:231], a[16:19]\n"
"buffer_store_dwordx4 v[56:59], v250, s[40:43], s73 offen offset:1024 sc1\n"
"v_add_f32 v66, v66, v18\n"
"v_add_f32 v67, v67, v19\n"
"v_mfma_f32_16x16x32_bf16 a[40:43], v[124:127], v[224:227], a[40:43]\n"
"v_cvt_pk_bf16_f32 v64, v64, v65\n"
"v_cvt_pk_bf16_f32 v65, v66, v67\n"
"v_accvgpr_read_b32 v192, a56\n"
"v_accvgpr_read_b32 v193, a57\n"
"v_mfma_f32_16x16x32_bf16 a[40:43], v[160:163], v[228:231], a[40:43]\n"
"s_waitcnt lgkmcnt(7)\n"
"v_add_f32 v68, v68, v20\n"
"v_add_f32 v69, v69, v21\n"
"v_accvgpr_read_b32 v194, a58\n"
"v_accvgpr_read_b32 v195, a59\n"
"v_mfma_f32_16x16x32_bf16 a[64:67], v[128:131], v[224:227], a[64:67]\n"
"v_add_f32 v70, v70, v22\n"
"v_add_f32 v71, v71, v23\n"
"v_mfma_f32_16x16x32_bf16 a[64:67], v[164:167], v[228:231], a[64:67]\n"
"v_cvt_pk_bf16_f32 v66, v68, v69\n"
"v_cvt_pk_bf16_f32 v67, v70, v71\n"
"v_accvgpr_read_b32 v196, a80\n"
"v_accvgpr_read_b32 v197, a81\n"
"v_mfma_f32_16x16x32_bf16 a[88:91], v[132:135], v[224:227], a[88:91]\n"
"s_waitcnt lgkmcnt(6)\n"
"v_add_f32 v72, v72, v192\n"
"v_add_f32 v73, v73, v193\n"
"v_accvgpr_read_b32 v198, a82\n"
"v_accvgpr_read_b32 v199, a83\n"
"v_mfma_f32_16x16x32_bf16 a[88:91], v[168:171], v[228:231], a[88:91]\n"
"buffer_store_dwordx4 v[64:67], v250, s[40:43], s73 offen offset:2048 sc1\n"
"v_add_f32 v74, v74, v194\n"
"v_add_f32 v75, v75, v195\n"
"v_mfma_f32_16x16x32_bf16 a[112:115], v[136:139], v[224:227], a[112:115]\n"
"v_cvt_pk_bf16_f32 v72, v72, v73\n"
"v_cvt_pk_bf16_f32 v73, v74, v75\n"
"v_accvgpr_read_b32 v200, a104\n"
"v_accvgpr_read_b32 v201, a105\n"
"v_mfma_f32_16x16x32_bf16 a[112:115], v[172:175], v[228:231], a[112:115]\n"
"s_waitcnt lgkmcnt(5)\n"
"v_add_f32 v76, v76, v196\n"
"v_add_f32 v77, v77, v197\n"
"v_accvgpr_read_b32 v202, a106\n"
"v_accvgpr_read_b32 v203, a107\n"
"v_mfma_f32_16x16x32_bf16 a[136:139], v[140:143], v[224:227], a[136:139]\n"
"v_add_f32 v78, v78, v198\n"
"v_add_f32 v79, v79, v199\n"
"v_mfma_f32_16x16x32_bf16 a[136:139], v[176:179], v[228:231], a[136:139]\n"
"v_cvt_pk_bf16_f32 v74, v76, v77\n"
"v_cvt_pk_bf16_f32 v75, v78, v79\n"
"v_accvgpr_read_b32 v204, a128\n"
"v_accvgpr_read_b32 v205, a129\n"
"v_mfma_f32_16x16x32_bf16 a[160:163], v[144:147], v[224:227], a[160:163]\n"
"s_waitcnt lgkmcnt(4)\n"
"v_add_f32 v80, v80, v200\n"
"v_add_f32 v81, v81, v201\n"
"v_accvgpr_read_b32 v206, a130\n"
"v_accvgpr_read_b32 v207, a131\n"
"v_mfma_f32_16x16x32_bf16 a[160:163], v[180:183], v[228:231], a[160:163]\n"
"buffer_store_dwordx4 v[72:75], v250, s[40:43], s73 offen offset:3072 sc1\n"
"v_add_f32 v82, v82, v202\n"
"v_add_f32 v83, v83, v203\n"
"v_mfma_f32_16x16x32_bf16 a[184:187], v[148:151], v[224:227], a[184:187]\n"
"v_cvt_pk_bf16_f32 v80, v80, v81\n"
"v_cvt_pk_bf16_f32 v81, v82, v83\n"
"v_accvgpr_read_b32 v208, a152\n"
"v_accvgpr_read_b32 v209, a153\n"
"v_mfma_f32_16x16x32_bf16 a[184:187], v[184:187], v[228:231], a[184:187]\n"
"s_waitcnt lgkmcnt(3)\n"
"v_add_f32 v84, v84, v204\n"
"v_add_f32 v85, v85, v205\n"
"v_accvgpr_read_b32 v210, a154\n"
"v_accvgpr_read_b32 v211, a155\n"
"v_mfma_f32_16x16x32_bf16 a[208:211], v[152:155], v[224:227], a[208:211]\n"
"v_add_f32 v86, v86, v206\n"
"v_add_f32 v87, v87, v207\n"
"v_mfma_f32_16x16x32_bf16 a[208:211], v[188:191], v[228:231], a[208:211]\n"
"v_cvt_pk_bf16_f32 v82, v84, v85\n"
"v_cvt_pk_bf16_f32 v83, v86, v87\n"
"v_accvgpr_read_b32 v212, a176\n"
"v_accvgpr_read_b32 v213, a177\n"
"v_mfma_f32_16x16x32_bf16 a[20:23], v[120:123], v[232:235], a[20:23]\n"
"s_mov_b32 s73, 4096\n"
"s_waitcnt lgkmcnt(2)\n"
"v_add_f32 v88, v88, v208\n"
"v_add_f32 v89, v89, v209\n"
"v_accvgpr_read_b32 v214, a178\n"
"v_accvgpr_read_b32 v215, a179\n"
"v_mfma_f32_16x16x32_bf16 a[20:23], v[156:159], v[236:239], a[20:23]\n"
"buffer_store_dwordx4 v[80:83], v250, s[40:43], s73 offen offset:0 sc1\n"
"v_add_f32 v90, v90, v210\n"
"v_add_f32 v91, v91, v211\n"
"v_mfma_f32_16x16x32_bf16 a[44:47], v[124:127], v[232:235], a[44:47]\n"
"v_cvt_pk_bf16_f32 v88, v88, v89\n"
"v_cvt_pk_bf16_f32 v89, v90, v91\n"
"v_accvgpr_read_b32 v0, a200\n"
"v_accvgpr_read_b32 v1, a201\n"
"v_mfma_f32_16x16x32_bf16 a[44:47], v[160:163], v[236:239], a[44:47]\n"
"s_waitcnt lgkmcnt(1)\n"
"v_add_f32 v92, v92, v212\n"
"v_add_f32 v93, v93, v213\n"
"v_accvgpr_read_b32 v2, a202\n"
"v_accvgpr_read_b32 v3, a203\n"
"v_mfma_f32_16x16x32_bf16 a[68:71], v[128:131], v[232:235], a[68:71]\n"
"v_add_f32 v94, v94, v214\n"
"v_add_f32 v95, v95, v215\n"
"v_mfma_f32_16x16x32_bf16 a[68:71], v[164:167], v[236:239], a[68:71]\n"
"v_cvt_pk_bf16_f32 v90, v92, v93\n"
"v_cvt_pk_bf16_f32 v91, v94, v95\n"
"v_mfma_f32_16x16x32_bf16 a[92:95], v[132:135], v[232:235], a[92:95]\n"
"s_waitcnt lgkmcnt(0)\n"
"v_add_f32 v96, v96, v0\n"
"v_add_f32 v97, v97, v1\n"
"v_mfma_f32_16x16x32_bf16 a[92:95], v[168:171], v[236:239], a[92:95]\n"
"buffer_store_dwordx4 v[88:91], v250, s[40:43], s73 offen offset:1024 sc1\n"
"v_add_f32 v98, v98, v2\n"
"v_add_f32 v99, v99, v3\n"
"v_mfma_f32_16x16x32_bf16 a[116:119], v[136:139], v[232:235], a[116:119]\n"
"v_cvt_pk_bf16_f32 v96, v96, v97\n"
"v_cvt_pk_bf16_f32 v97, v98, v99\n"
"v_mfma_f32_16x16x32_bf16 a[116:119], v[172:175], v[236:239], a[116:119]\n"
"v_mfma_f32_16x16x32_bf16 a[140:143], v[140:143], v[232:235], a[140:143]\n"
"buffer_store_dwordx4 v[96:99], v250, s[40:43], s73 offen offset:2048 sc1\n"
"v_mfma_f32_16x16x32_bf16 a[140:143], v[176:179], v[236:239], a[140:143]\n"
"v_mfma_f32_16x16x32_bf16 a[164:167], v[144:147], v[232:235], a[164:167]\n"
"v_mfma_f32_16x16x32_bf16 a[164:167], v[180:183], v[236:239], a[164:167]\n"
"v_mfma_f32_16x16x32_bf16 a[188:191], v[148:151], v[232:235], a[188:191]\n"
"v_mfma_f32_16x16x32_bf16 a[188:191], v[184:187], v[236:239], a[188:191]\n"
"v_mfma_f32_16x16x32_bf16 a[212:215], v[152:155], v[232:235], a[212:215]\n"
"v_mfma_f32_16x16x32_bf16 a[212:215], v[188:191], v[236:239], a[212:215]\n"
"ds_write_b128 v242, a[12:15] offset:0\n"
"ds_write_b128 v242, a[36:39] offset:1024\n"
"ds_write_b128 v242, a[60:63] offset:2048\n"
"ds_write_b128 v242, a[84:87] offset:3072\n"
"ds_write_b128 v242, a[108:111] offset:4096\n"
"ds_write_b128 v242, a[132:135] offset:5120\n"
"ds_write_b128 v242, a[156:159] offset:6144\n"
"ds_write_b128 v242, a[180:183] offset:7168\n"
"ds_write_b128 v242, a[204:207] offset:8192\n"
"ds_write_b128 v242, a[16:19] offset:9216\n"
"ds_write_b128 v242, a[40:43] offset:10240\n"
"ds_write_b128 v242, a[64:67] offset:11264\n"
"ds_write_b128 v242, a[88:91] offset:12288\n"
"ds_write_b128 v242, a[112:115] offset:13312\n"
"v_accvgpr_read_b32 v0, a136\n"
"v_accvgpr_read_b32 v1, a137\n"
"v_accvgpr_read_b32 v2, a138\n"
"v_accvgpr_read_b32 v3, a139\n"
"v_accvgpr_read_b32 v4, a160\n"
"v_accvgpr_read_b32 v5, a161\n"
"v_accvgpr_read_b32 v6, a162\n"
"v_accvgpr_read_b32 v7, a163\n"
"v_accvgpr_read_b32 v8, a184\n"
"v_accvgpr_read_b32 v9, a185\n"
"v_accvgpr_read_b32 v10, a186\n"
"v_accvgpr_read_b32 v11, a187\n"
"v_accvgpr_read_b32 v12, a208\n"
"v_accvgpr_read_b32 v13, a209\n"
"v_accvgpr_read_b32 v14, a210\n"
"v_accvgpr_read_b32 v15, a211\n"
"v_accvgpr_read_b32 v16, a20\n"
"v_accvgpr_read_b32 v17, a21\n"
"v_accvgpr_read_b32 v18, a22\n"
"v_accvgpr_read_b32 v19, a23\n"
"v_accvgpr_read_b32 v20, a44\n"
"v_accvgpr_read_b32 v21, a45\n"
"v_accvgpr_read_b32 v22, a46\n"
"v_accvgpr_read_b32 v23, a47\n"
"v_accvgpr_read_b32 v24, a68\n"
"v_accvgpr_read_b32 v25, a69\n"
"v_accvgpr_read_b32 v26, a70\n"
"v_accvgpr_read_b32 v27, a71\n"
"v_accvgpr_read_b32 v28, a92\n"
"v_accvgpr_read_b32 v29, a93\n"
"v_accvgpr_read_b32 v30, a94\n"
"v_accvgpr_read_b32 v31, a95\n"
"v_accvgpr_read_b32 v32, a116\n"
"v_accvgpr_read_b32 v33, a117\n"
"v_accvgpr_read_b32 v34, a118\n"
"v_accvgpr_read_b32 v35, a119\n"
"v_accvgpr_read_b32 v36, a140\n"
"v_accvgpr_read_b32 v37, a141\n"
"v_accvgpr_read_b32 v38, a142\n"
"v_accvgpr_read_b32 v39, a143\n"
"v_accvgpr_read_b32 v40, a164\n"
"v_accvgpr_read_b32 v41, a165\n"
"v_accvgpr_read_b32 v42, a166\n"
"v_accvgpr_read_b32 v43, a167\n"
"v_accvgpr_read_b32 v44, a188\n"
"v_accvgpr_read_b32 v45, a189\n"
"v_accvgpr_read_b32 v46, a190\n"
"v_accvgpr_read_b32 v47, a191\n"
"v_accvgpr_read_b32 v48, a212\n"
"v_accvgpr_read_b32 v49, a213\n"
"v_accvgpr_read_b32 v50, a214\n"
"v_accvgpr_read_b32 v51, a215\n"
".Lf_fk2:\n"
"s_mov_b64 s[40:41], %26\n"
"s_add_u32 s73, s87, 3\n"
"v_mov_b32 v251, s73\n"
"s_waitcnt vmcnt(0) lgkmcnt(0)\n"
"global_store_dword v252, v251, %20 sc1\n"
"s_barrier\n"
"ds_read_b128 v[120:123], v243 offset:0\n"
"ds_read_b128 v[124:127], v243 offset:1024\n"
"ds_read_b128 v[128:131], v243 offset:2048\n"
"ds_read_b128 v[132:135], v243 offset:3072\n"
"ds_read_b128 v[136:139], v243 offset:4096\n"
"ds_read_b128 v[140:143], v243 offset:5120\n"
"ds_read_b128 v[144:147], v243 offset:6144\n"
"ds_read_b128 v[148:151], v243 offset:7168\n"
"ds_read_b128 v[152:155], v243 offset:8192\n"
"ds_read_b128 v[156:159], v243 offset:9216\n"
"ds_read_b128 v[160:163], v243 offset:10240\n"
"ds_read_b128 v[164:167], v243 offset:11264\n"
"ds_read_b128 v[168:171], v243 offset:12288\n"
"ds_read_b128 v[172:175], v243 offset:13312\n"
"global_load_dword v251, v252, %27 sc1\n"
"v_lshrrev_b32 v240, 4, v250\n"
"v_and_b32 v241, 15, v240\n"
"v_mul_u32_u24 v241, 2304, v241\n"
"v_lshrrev_b32 v240, 4, v240\n"
"v_and_b32 v242, 1, v240\n"
"v_lshrrev_b32 v240, 1, v240\n"
"v_lshl_add_u32 v240, v240, 4, v241\n"
"s_mul_i32 s75, s75, 14\n"
"s_mul_i32 s70, %21, 110592\n"
"s_add_u32 s73, s75, 0\n"
"s_mul_i32 s74, s73, 57\n"
"s_lshr_b32 s74, s74, 9\n"
"s_mul_i32 s56, s74, 36576\n"
"s_lshl_b32 s73, s73, 5\n"
"s_add_u32 s56, s56, s73\n"
"s_add_u32 s56, s56, s70\n"
"s_add_u32 s73, s75, 1\n"
"s_mul_i32 s74, s73, 57\n"
"s_lshr_b32 s74, s74, 9\n"
"s_mul_i32 s57, s74, 36576\n"
"s_lshl_b32 s73, s73, 5\n"
"s_add_u32 s57, s57, s73\n"
"s_add_u32 s57, s57, s70\n"
"s_add_u32 s73, s75, 2\n"
"s_mul_i32 s74, s73, 57\n"
"s_lshr_b32 s74, s74, 9\n"
"s_mul_i32 s58, s74, 36576\n"
"s_lshl_b32 s73, s73, 5\n"
"s_add_u32 s58, s58, s73\n"
"s_add_u32 s58, s58, s70\n"
"s_add_u32 s73, s75, 3\n"
"s_mul_i32 s74, s73, 57\n"
"s_lshr_b32 s74, s74, 9\n"
"s_mul_i32 s59, s74, 36576\n"
"s_lshl_b32 s73, s73, 5\n"
"s_add_u32 s59, s59, s73\n"
"s_add_u32 s59, s59, s70\n"
"s_add_u32 s73, s75, 4\n"
"s_mul_i32 s74, s73, 57\n"
"s_lshr_b32 s74, s74, 9\n"
"s_mul_i32 s60, s74, 36576\n"
"s_lshl_b32 s73, s73, 5\n"
"s_add_u32 s60, s60, s73\n"
"s_add_u32 s60, s60, s70\n"
"s_add_u32 s73, s75, 5\n"
"s_mul_i32 s74, s73, 57\n"
"s_lshr_b32 s74, s74, 9\n"
"s_mul_i32 s61, s74, 36576\n"
"s_lshl_b32 s73, s73, 5\n"
"s_add_u32 s61, s61, s73\n"
"s_add_u32 s61, s61, s70\n"
"s_add_u32 s73, s75, 6\n"
"s_mul_i32 s74, s73, 57\n"
"s_lshr_b32 s74, s74, 9\n"
"s_mul_i32 s62, s74, 36576\n"
"s_lshl_b32 s73, s73, 5\n"
"s_add_u32 s62, s62, s73\n"
"s_add_u32 s62, s62, s70\n"
"s_add_u32 s73, s75, 7\n"
"s_mul_i32 s74, s73, 57\n"
"s_lshr_b32 s74, s74, 9\n"
"s_mul_i32 s63, s74, 36576\n"
"s_lshl_b32 s73, s73, 5\n"
"s_add_u32 s63, s63, s73\n"
"s_add_u32 s63, s63, s70\n"
"s_add_u32 s73, s75, 8\n"
"s_mul_i32 s74, s73, 57\n"
"s_lshr_b32 s74, s74, 9\n"
"s_mul_i32 s64, s74, 36576\n"
"s_lshl_b32 s73, s73, 5\n"
"s_add_u32 s64, s64, s73\n"
"s_add_u32 s64, s64, s70\n"
"s_add_u32 s73, s75, 9\n"
"s_mul_i32 s74, s73, 57\n"
"s_lshr_b32 s74, s74, 9\n"
"s_mul_i32 s65, s74, 36576\n"
"s_lshl_b32 s73, s73, 5\n"
"s_add_u32 s65, s65, s73\n"
"s_add_u32 s65, s65, s70\n"
"s_add_u32 s73, s75, 10\n"
"s_mul_i32 s74, s73, 57\n"
"s_lshr_b32 s74, s74, 9\n"
"s_mul_i32 s66, s74, 36576\n"
"s_lshl_b32 s73, s73, 5\n"
"s_add_u32 s66, s66, s73\n"
"s_add_u32 s66, s66, s70\n"
"s_add_u32 s73, s75, 11\n"
"s_mul_i32 s74, s73, 57\n"
"s_lshr_b32 s74, s74, 9\n"
"s_mul_i32 s67, s74, 36576\n"
"s_lshl_b32 s73, s73, 5\n"
"s_add_u32 s67, s67, s73\n"
"s_add_u32 s67, s67, s70\n"
"s_add_u32 s73, s75, 12\n"
"s_mul_i32 s74, s73, 57\n"
"s_lshr_b32 s74, s74, 9\n"
"s_mul_i32 s68, s74, 36576\n"
"s_lshl_b32 s73, s73, 5\n"
"s_add_u32 s68, s68, s73\n"
"s_add_u32 s68, s68, s70\n"
"s_add_u32 s73, s75, 13\n"
"s_mul_i32 s74, s73, 57\n"
"s_lshr_b32 s74, s74, 9\n"
"s_mul_i32 s69, s74, 36576\n"
"s_lshl_b32 s73, s73, 5\n"
"s_add_u32 s69, s69, s73\n"
"s_add_u32 s69, s69, s70\n"
"s_cmp_lg_u32 s75, 0\n"
"s_cselect_b32 s69, 0x800000, s69\n"
"s_sub_u32 s57, s57, s56\n"
"v_mad_u32_u24 v180, v242, s57, v240\n"
"s_sub_u32 s59, s59, s58\n"
"v_mad_u32_u24 v181, v242, s59, v240\n"
"s_sub_u32 s61, s61, s60\n"
"v_mad_u32_u24 v182, v242, s61, v240\n"
"s_sub_u32 s63, s63, s62\n"
"v_mad_u32_u24 v183, v242, s63, v240\n"
"s_sub_u32 s65, s65, s64\n"
"v_mad_u32_u24 v184, v242, s65, v240\n"
"s_sub_u32 s67, s67, s66\n"
"v_mad_u32_u24 v185, v242, s67, v240\n"
"s_sub_u32 s69, s69, s68\n"
"v_mad_u32_u24 v186, v242, s69, v240\n"
"s_waitcnt lgkmcnt(13)\n"
"v_pk_add_f32 v[0:1], v[0:1], v[120:121]\n"
"v_pk_add_f32 v[2:3], v[2:3], v[122:123]\n"
"s_waitcnt lgkmcnt(12)\n"
"v_pk_add_f32 v[4:5], v[4:5], v[124:125]\n"
"v_pk_add_f32 v[6:7], v[6:7], v[126:127]\n"
"s_waitcnt lgkmcnt(11)\n"
"v_pk_add_f32 v[8:9], v[8:9], v[128:129]\n"
"v_pk_add_f32 v[10:11], v[10:11], v[130:131]\n"
"s_waitcnt lgkmcnt(10)\n"
"v_pk_add_f32 v[12:13], v[12:13], v[132:133]\n"
"v_pk_add_f32 v[14:15], v[14:15], v[134:135]\n"
"s_waitcnt lgkmcnt(9)\n"
"v_pk_add_f32 v[16:17], v[16:17], v[136:137]\n"
"v_pk_add_f32 v[18:19], v[18:19], v[138:139]\n"
"s_waitcnt lgkmcnt(8)\n"
"v_pk_add_f32 v[20:21], v[20:21], v[140:141]\n"
"v_pk_add_f32 v[22:23], v[22:23], v[142:143]\n"
"s_waitcnt lgkmcnt(7)\n"
"v_pk_add_f32 v[24:25], v[24:25], v[144:145]\n"
"v_pk_add_f32 v[26:27], v[26:27], v[146:147]\n"
"s_waitcnt lgkmcnt(6)\n"
"v_pk_add_f32 v[28:29], v[28:29], v[148:149]\n"
"v_pk_add_f32 v[30:31], v[30:31], v[150:151]\n"
"s_waitcnt lgkmcnt(5)\n"
"v_pk_add_f32 v[32:33], v[32:33], v[152:153]\n"
"v_pk_add_f32 v[34:35], v[34:35], v[154:155]\n"
"s_waitcnt lgkmcnt(4)\n"
"v_pk_add_f32 v[36:37], v[36:37], v[156:157]\n"
"v_pk_add_f32 v[38:39], v[38:39], v[158:159]\n"
"s_waitcnt lgkmcnt(3)\n"
"v_pk_add_f32 v[40:41], v[40:41], v[160:161]\n"
"v_pk_add_f32 v[42:43], v[42:43], v[162:163]\n"
"s_waitcnt lgkmcnt(2)\n"
"v_pk_add_f32 v[44:45], v[44:45], v[164:165]\n"
"v_pk_add_f32 v[46:47], v[46:47], v[166:167]\n"
"s_waitcnt lgkmcnt(1)\n"
"v_pk_add_f32 v[48:49], v[48:49], v[168:169]\n"
"v_pk_add_f32 v[50:51], v[50:51], v[170:171]\n"
"s_waitcnt lgkmcnt(0)\n"
"v_pk_add_f32 v[52:53], v[52:53], v[172:173]\n"
"v_pk_add_f32 v[54:55], v[54:55], v[174:175]\n"
"s_mov_b32 s75, 0x4000\n"
"s_add_u32 s74, s84, 3\n"
"s_waitcnt vmcnt(0)\n"
"v_readfirstlane_b32 s73, v251\n"
"s_nop 0\n"
"s_cmp_eq_u32 s73, s74\n"
"s_cbranch_scc1 .Lf_xgo\n"
".Lf_xspin:\n"
"global_load_dword v251, v252, %27 sc1\n"
"s_waitcnt vmcnt(0)\n"
"v_readfirstlane_b32 s73, v251\n"
"s_sub_u32 s75, s75, 1\n"
"s_cmp_eq_u32 s75, 0\n"
"s_cbranch_scc1 .Lf_xgo\n"
"s_cmp_eq_u32 s73, s74\n"
"s_cbranch_scc0 .Lf_xspin\n"
".Lf_xgo:\n"
"s_mov_b32 s73, 0\n"
"buffer_load_dwordx4 v[120:123], v250, s[40:43], s73 offen offset:0 sc1\n"
"buffer_load_dwordx4 v[124:127], v250, s[40:43], s73 offen offset:1024 sc1\n"
"buffer_load_dwordx4 v[128:131], v250, s[40:43], s73 offen offset:2048 sc1\n"
"buffer_load_dwordx4 v[132:135], v250, s[40:43], s73 offen offset:3072 sc1\n"
"s_mov_b32 s73, 4096\n"
"buffer_load_dwordx4 v[136:139], v250, s[40:43], s73 offen offset:0 sc1\n"
"buffer_load_dwordx4 v[140:143], v250, s[40:43], s73 offen offset:1024 sc1\n"
"buffer_load_dwordx4 v[144:147], v250, s[40:43], s73 offen offset:2048 sc1\n"
"s_sub_u32 s73, s84, 0x9e3779a1\n"
"v_mov_b32 v251, s73\n"
"s_lshl_b32 s73, %24, 2\n"
"v_mov_b32 v241, s73\n"
"global_store_dword v241, v251, %23 sc1\n"
"s_waitcnt vmcnt(7)\n"
"v_lshlrev_b32 v160, 16, v120\n"
"v_and_b32 v161, 0xffff0000, v120\n"
"v_lshlrev_b32 v162, 16, v121\n"
"v_and_b32 v163, 0xffff0000, v121\n"
"v_pk_add_f32 v[0:1], v[0:1], v[160:161]\n"
"v_pk_add_f32 v[2:3], v[2:3], v[162:163]\n"
"v_cvt_pk_bf16_f32 v0, v0, v1\n"
"v_cvt_pk_bf16_f32 v1, v2, v3\n"
"v_lshlrev_b32 v164, 16, v122\n"
"v_and_b32 v165, 0xffff0000, v122\n"
"v_lshlrev_b32 v166, 16, v123\n"
"v_and_b32 v167, 0xffff0000, v123\n"
"v_pk_add_f32 v[4:5], v[4:5], v[164:165]\n"
"v_pk_add_f32 v[6:7], v[6:7], v[166:167]\n"
"v_cvt_pk_bf16_f32 v2, v4, v5\n"
"v_cvt_pk_bf16_f32 v3, v6, v7\n"
"s_nop 0\n"
"v_permlane16_swap_b32 v0, v2\n"
"v_permlane16_swap_b32 v1, v3\n"
"buffer_store_dwordx4 v[0:3], v180, s[36:39], s56 offen\n"
"s_waitcnt vmcnt(7)\n"
"v_lshlrev_b32 v168, 16, v124\n"
"v_and_b32 v169, 0xffff0000, v124\n"
"v_lshlrev_b32 v170, 16, v125\n"
"v_and_b32 v171, 0xffff0000, v125\n"
"v_pk_add_f32 v[8:9], v[8:9], v[168:169]\n"
"v_pk_add_f32 v[10:11], v[10:11], v[170:171]\n"
"v_cvt_pk_bf16_f32 v8, v8, v9\n"
"v_cvt_pk_bf16_f32 v9, v10, v11\n"
"v_lshlrev_b32 v172, 16, v126\n"
"v_and_b32 v173, 0xffff0000, v126\n"
"v_lshlrev_b32 v174, 16, v127\n"
"v_and_b32 v175, 0xffff0000, v127\n"
"v_pk_add_f32 v[12:13], v[12:13], v[172:173]\n"
"v_pk_add_f32 v[14:15], v[14:15], v[174:175]\n"
"v_cvt_pk_bf16_f32 v10, v12, v13\n"
"v_cvt_pk_bf16_f32 v11, v14, v15\n"
"s_nop 0\n"
"v_permlane16_swap_b32 v8, v10\n"
"v_permlane16_swap_b32 v9, v11\n"
"buffer_store_dwordx4 v[8:11], v181, s[36:39], s58 offen\n"
"s_waitcnt vmcnt(7)\n"
"v_lshlrev_b32 v160, 16, v128\n"
"v_and_b32 v161, 0xffff0000, v128\n"
"v_lshlrev_b32 v162, 16, v129\n"
"v_and_b32 v163, 0xffff0000, v129\n"
"v_pk_add_f32 v[16:17], v[16:17], v[160:161]\n"
"v_pk_add_f32 v[18:19], v[18:19], v[162:163]\n"
"v_cvt_pk_bf16_f32 v16, v16, v17\n"
"v_cvt_pk_bf16_f32 v17, v18, v19\n"
"v_lshlrev_b32 v164, 16, v130\n"
"v_and_b32 v165, 0xffff0000, v130\n"
"v_lshlrev_b32 v166, 16, v131\n"
"v_and_b32 v167, 0xffff0000, v131\n"
"v_pk_add_f32 v[20:21], v[20:21], v[164:165]\n"
"v_pk_add_f32 v[22:23], v[22:23], v[166:167]\n"
"v_cvt_pk_bf16_f32 v18, v20, v21\n"
"v_cvt_pk_bf16_f32 v19, v22, v23\n"
"s_nop 0\n"
"v_permlane16_swap_b32 v16, v18\n"
"v_permlane16_swap_b32 v17, v19\n"
"buffer_store_dwordx4 v[16:19], v182, s[36:39], s60 offen\n"
"s_waitcnt vmcnt(7)\n"
"v_lshlrev_b32 v168, 16, v132\n"
"v_and_b32 v169, 0xffff0000, v132\n"
"v_lshlrev_b32 v170, 16, v133\n"
"v_and_b32 v171, 0xffff0000, v133\n"
"v_pk_add_f32 v[24:25], v[24:25], v[168:169]\n"
"v_pk_add_f32 v[26:27], v[26:27], v[170:171]\n"
"v_cvt_pk_bf16_f32 v24, v24, v25\n"
"v_cvt_pk_bf16_f32 v25, v26, v27\n"
"v_lshlrev_b32 v172, 16, v134\n"
"v_and_b32 v173, 0xffff0000, v134\n"
"v_lshlrev_b32 v174, 16, v135\n"
"v_and_b32 v175, 0xffff0000, v135\n"
"v_pk_add_f32 v[28:29], v[28:29], v[172:173]\n"
"v_pk_add_f32 v[30:31], v[30:31], v[174:175]\n"
"v_cvt_pk_bf16_f32 v26, v28, v29\n"
"v_cvt_pk_bf16_f32 v27, v30, v31\n"
"s_nop 0\n"
"v_permlane16_swap_b32 v24, v26\n"
"v_permlane16_swap_b32 v25, v27\n"
"buffer_store_dwordx4 v[24:27], v183, s[36:39], s62 offen\n"
"s_waitcnt vmcnt(7)\n"
"v_lshlrev_b32 v160, 16, v136\n"
"v_and_b32 v161, 0xffff0000, v136\n"
"v_lshlrev_b32 v162, 16, v137\n"
"v_and_b32 v163, 0xffff0000, v137\n"
"v_pk_add_f32 v[32:33], v[32:33], v[160:161]\n"
"v_pk_add_f32 v[34:35], v[34:35], v[162:163]\n"
"v_cvt_pk_bf16_f32 v32, v32, v33\n"
"v_cvt_pk_bf16_f32 v33, v34, v35\n"
"v_lshlrev_b32 v164, 16, v138\n"
"v_and_b32 v165, 0xffff0000, v138\n"
"v_lshlrev_b32 v166, 16, v139\n"
"v_and_b32 v167, 0xffff0000, v139\n"
"v_pk_add_f32 v[36:37], v[36:37], v[164:165]\n"
"v_pk_add_f32 v[38:39], v[38:39], v[166:167]\n"
"v_cvt_pk_bf16_f32 v34, v36, v37\n"
"v_cvt_pk_bf16_f32 v35, v38, v39\n"
"s_nop 0\n"
"v_permlane16_swap_b32 v32, v34\n"
"v_permlane16_swap_b32 v33, v35\n"
"buffer_store_dwordx4 v[32:35], v184, s[36:39], s64 offen\n"
"s_waitcnt vmcnt(7)\n"
"v_lshlrev_b32 v168, 16, v140\n"
"v_and_b32 v169, 0xffff0000, v140\n"
"v_lshlrev_b32 v170, 16, v141\n"
"v_and_b32 v171, 0xffff0000, v141\n"
"v_pk_add_f32 v[40:41], v[40:41], v[168:169]\n"
"v_pk_add_f32 v[42:43], v[42:43], v[170:171]\n"
"v_cvt_pk_bf16_f32 v40, v40, v41\n"
"v_cvt_pk_bf16_f32 v41, v42, v43\n"
"v_lshlrev_b32 v172, 16, v142\n"
"v_and_b32 v173, 0xffff0000, v142\n"
"v_lshlrev_b32 v174, 16, v143\n"
"v_and_b32 v175, 0xffff0000, v143\n"
"v_pk_add_f32 v[44:45], v[44:45], v[172:173]\n"
"v_pk_add_f32 v[46:47], v[46:47], v[174:175]\n"
"v_cvt_pk_bf16_f32 v42, v44, v45\n"
"v_cvt_pk_bf16_f32 v43, v46, v47\n"
"s_nop 0\n"
"v_permlane16_swap_b32 v40, v42\n"
"v_permlane16_swap_b32 v41, v43\n"
"buffer_store_dwordx4 v[40:43], v185, s[36:39], s66 offen\n"
"s_waitcnt vmcnt(7)\n"
"v_lshlrev_b32 v160, 16, v144\n"
"v_and_b32 v161, 0xffff0000, v144\n"
"v_lshlrev_b32 v162, 16, v145\n"
"v_and_b32 v163, 0xffff0000, v145\n"
"v_pk_add_f32 v[48:49], v[48:49], v[160:161]\n"
"v_pk_add_f32 v[50:51], v[50:51], v[162:163]\n"
"v_cvt_pk_bf16_f32 v48, v48, v49\n"
"v_cvt_pk_bf16_f32 v49, v50, v51\n"
"v_lshlrev_b32 v164, 16, v146\n"
"v_and_b32 v165, 0xffff0000, v146\n"
"v_lshlrev_b32 v166, 16, v147\n"
"v_and_b32 v167, 0xffff0000, v147\n"
"v_pk_add_f32 v[52:53], v[52:53], v[164:165]\n"
"v_pk_add_f32 v[54:55], v[54:55], v[166:167]\n"
"v_cvt_pk_bf16_f32 v50, v52, v53\n"
"v_cvt_pk_bf16_f32 v51, v54, v55\n"
"s_nop 0\n"
"v_permlane16_swap_b32 v48, v50\n"
"v_permlane16_swap_b32 v49, v51\n"
"buffer_store_dwordx4 v[48:51], v186, s[36:39], s68 offen\n"
".Lf_fin:\n"
"s_branch .Lf_end\n"
".Lf_slow:\n"
"s_waitcnt vmcnt(0) lgkmcnt(0)\n"
"s_nop 15\n"
"s_sub_u32 s36, s36, 0x20000\n"
"s_subb_u32 s37, s37, 0\n"
"s_sub_u32 s40, s40, 0x20000\n"
"s_subb_u32 s41, s41, 0\n"
"v_mbcnt_lo_u32_b32 v98, -1, 0\n"
"v_mbcnt_hi_u32_b32 v98, -1, v98\n"
"v_bfe_u32 v96, v98, 2, 2\n"
"v_lshlrev_b32 v96, 3, v96\n"
"v_and_b32 v97, 3, v98\n"
"v_add_u32 v96, v96, v97\n"
"v_lshlrev_b32 v96, 8, v96\n"
"v_lshrrev_b32 v97, 4, v98\n"
"v_lshl_add_u32 v96, v97, 4, v96\n"
"v_lshlrev_b32 v97, 11, v97\n"
"v_and_b32 v98, 15, v98\n"
"v_lshl_add_u32 v97, v98, 1, v97\n"
"v_mov_b32 v176, 0xff800000\n"
"v_mov_b32 v216, 0\n"
"v_mov_b32 v177, 0xff800000\n"
"v_mov_b32 v217, 0\n"
"v_mov_b32 v178, 0xff800000\n"
"v_mov_b32 v218, 0\n"
"v_mov_b32 v179, 0xff800000\n"
"v_mov_b32 v219, 0\n"
"v_mov_b32 v180, 0xff800000\n"
"v_mov_b32 v220, 0\n"
"v_mov_b32 v181, 0xff800000\n"
"v_mov_b32 v221, 0\n"
"v_accvgpr_write_b32 a0, 0\n"
"v_accvgpr_write_b32 a1, 0\n"
"v_accvgpr_write_b32 a2, 0\n"
"v_accvgpr_write_b32 a3, 0\n"
"v_accvgpr_write_b32 a4, 0\n"
"v_accvgpr_write_b32 a5, 0\n"
"v_accvgpr_write_b32 a6, 0\n"
"v_accvgpr_write_b32 a7, 0\n"
"v_accvgpr_write_b32 a8, 0\n"
"v_accvgpr_write_b32 a9, 0\n"
"v_accvgpr_write_b32 a10, 0\n"
"v_accvgpr_write_b32 a11, 0\n"
"v_accvgpr_write_b32 a12, 0\n"
"v_accvgpr_write_b32 a13, 0\n"
"v_accvgpr_write_b32 a14, 0\n"
"v_accvgpr_write_b32 a15, 0\n"
"v_accvgpr_write_b32 a16, 0\n"
"v_accvgpr_write_b32 a17, 0\n"
"v_accvgpr_write_b32 a18, 0\n"
"v_accvgpr_write_b32 a19, 0\n"
"v_accvgpr_write_b32 a20, 0\n"
"v_accvgpr_write_b32 a21, 0\n"
"v_accvgpr_write_b32 a22, 0\n"
"v_accvgpr_write_b32 a23, 0\n"
"v_accvgpr_write_b32 a24, 0\n"
"v_accvgpr_write_b32 a25, 0\n"
"v_accvgpr_write_b32 a26, 0\n"
"v_accvgpr_write_b32 a27, 0\n"
"v_accvgpr_write_b32 a28, 0\n"
"v_accvgpr_write_b32 a29, 0\n"
"v_accvgpr_write_b32 a30, 0\n"
"v_accvgpr_write_b32 a31, 0\n"
"v_accvgpr_write_b32 a32, 0\n"
"v_accvgpr_write_b32 a33, 0\n"
"v_accvgpr_write_b32 a34, 0\n"
"v_accvgpr_write_b32 a35, 0\n"
"v_accvgpr_write_b32 a36, 0\n"
"v_accvgpr_write_b32 a37, 0\n"
"v_accvgpr_write_b32 a38, 0\n"
"v_accvgpr_write_b32 a39, 0\n"
"v_accvgpr_write_b32 a40, 0\n"
"v_accvgpr_write_b32 a41, 0\n"
"v_accvgpr_write_b32 a42, 0\n"
"v_accvgpr_write_b32 a43, 0\n"
"v_accvgpr_write_b32 a44, 0\n"
"v_accvgpr_write_b32 a45, 0\n"
"v_accvgpr_write_b32 a46, 0\n"
"v_accvgpr_write_b32 a47, 0\n"
"v_accvgpr_write_b32 a48, 0\n"
"v_accvgpr_write_b32 a49, 0\n"
"v_accvgpr_write_b32 a50, 0\n"
"v_accvgpr_write_b32 a51, 0\n"
"v_accvgpr_write_b32 a52, 0\n"
"v_accvgpr_write_b32 a53, 0\n"
"v_accvgpr_write_b32 a54, 0\n"
"v_accvgpr_write_b32 a55, 0\n"
"v_accvgpr_write_b32 a56, 0\n"
"v_accvgpr_write_b32 a57, 0\n"
"v_accvgpr_write_b32 a58, 0\n"
"v_accvgpr_write_b32 a59, 0\n"
"v_accvgpr_write_b32 a60, 0\n"
"v_accvgpr_write_b32 a61, 0\n"
"v_accvgpr_write_b32 a62, 0\n"
"v_accvgpr_write_b32 a63, 0\n"
"v_accvgpr_write_b32 a64, 0\n"
"v_accvgpr_write_b32 a65, 0\n"
"v_accvgpr_write_b32 a66, 0\n"
"v_accvgpr_write_b32 a67, 0\n"
"v_accvgpr_write_b32 a68, 0\n"
"v_accvgpr_write_b32 a69, 0\n"
"v_accvgpr_write_b32 a70, 0\n"
"v_accvgpr_write_b32 a71, 0\n"
"v_accvgpr_write_b32 a72, 0\n"
"v_accvgpr_write_b32 a73, 0\n"
"v_accvgpr_write_b32 a74, 0\n"
"v_accvgpr_write_b32 a75, 0\n"
"v_accvgpr_write_b32 a76, 0\n"
"v_accvgpr_write_b32 a77, 0\n"
"v_accvgpr_write_b32 a78, 0\n"
"v_accvgpr_write_b32 a79, 0\n"
"v_accvgpr_write_b32 a80, 0\n"
"v_accvgpr_write_b32 a81, 0\n"
"v_accvgpr_write_b32 a82, 0\n"
"v_accvgpr_write_b32 a83, 0\n"
"v_accvgpr_write_b32 a84, 0\n"
"v_accvgpr_write_b32 a85, 0\n"
"v_accvgpr_write_b32 a86, 0\n"
"v_accvgpr_write_b32 a87, 0\n"
"v_accvgpr_write_b32 a88, 0\n"
"v_accvgpr_write_b32 a89, 0\n"
"v_accvgpr_write_b32 a90, 0\n"
"v_accvgpr_write_b32 a91, 0\n"
"v_accvgpr_write_b32 a92, 0\n"
"v_accvgpr_write_b32 a93, 0\n"
"v_accvgpr_write_b32 a94, 0\n"
"v_accvgpr_write_b32 a95, 0\n"
"v_accvgpr_write_b32 a96, 0\n"
"v_accvgpr_write_b32 a97, 0\n"
"v_accvgpr_write_b32 a98, 0\n"
"v_accvgpr_write_b32 a99, 0\n"
"v_accvgpr_write_b32 a100, 0\n"
"v_accvgpr_write_b32 a101, 0\n"
"v_accvgpr_write_b32 a102, 0\n"
"v_accvgpr_write_b32 a103, 0\n"
"v_accvgpr_write_b32 a104, 0\n"
"v_accvgpr_write_b32 a105, 0\n"
"v_accvgpr_write_b32 a106, 0\n"
"v_accvgpr_write_b32 a107, 0\n"
"v_accvgpr_write_b32 a108, 0\n"
"v_accvgpr_write_b32 a109, 0\n"
"v_accvgpr_write_b32 a110, 0\n"
"v_accvgpr_write_b32 a111, 0\n"
"v_accvgpr_write_b32 a112, 0\n"
"v_accvgpr_write_b32 a113, 0\n"
"v_accvgpr_write_b32 a114, 0\n"
"v_accvgpr_write_b32 a115, 0\n"
"v_accvgpr_write_b32 a116, 0\n"
"v_accvgpr_write_b32 a117, 0\n"
"v_accvgpr_write_b32 a118, 0\n"
"v_accvgpr_write_b32 a119, 0\n"
"v_accvgpr_write_b32 a120, 0\n"
"v_accvgpr_write_b32 a121, 0\n"
"v_accvgpr_write_b32 a122, 0\n"
"v_accvgpr_write_b32 a123, 0\n"
"v_accvgpr_write_b32 a124, 0\n"
"v_accvgpr_write_b32 a125, 0\n"
"v_accvgpr_write_b32 a126, 0\n"
"v_accvgpr_write_b32 a127, 0\n"
"v_accvgpr_write_b32 a128, 0\n"
"v_accvgpr_write_b32 a129, 0\n"
"v_accvgpr_write_b32 a130, 0\n"
"v_accvgpr_write_b32 a131, 0\n"
"v_accvgpr_write_b32 a132, 0\n"
"v_accvgpr_write_b32 a133, 0\n"
"v_accvgpr_write_b32 a134, 0\n"
"v_accvgpr_write_b32 a135, 0\n"
"v_accvgpr_write_b32 a136, 0\n"
"v_accvgpr_write_b32 a137, 0\n"
"v_accvgpr_write_b32 a138, 0\n"
"v_accvgpr_write_b32 a139, 0\n"
"v_accvgpr_write_b32 a140, 0\n"
"v_accvgpr_write_b32 a141, 0\n"
"v_accvgpr_write_b32 a142, 0\n"
"v_accvgpr_write_b32 a143, 0\n"
"v_accvgpr_write_b32 a144, 0\n"
"v_accvgpr_write_b32 a145, 0\n"
"v_accvgpr_write_b32 a146, 0\n"
"v_accvgpr_write_b32 a147, 0\n"
"v_accvgpr_write_b32 a148, 0\n"
"v_accvgpr_write_b32 a149, 0\n"
"v_accvgpr_write_b32 a150, 0\n"
"v_accvgpr_write_b32 a151, 0\n"
"v_accvgpr_write_b32 a152, 0\n"
"v_accvgpr_write_b32 a153, 0\n"
"v_accvgpr_write_b32 a154, 0\n"
"v_accvgpr_write_b32 a155, 0\n"
"v_accvgpr_write_b32 a156, 0\n"
"v_accvgpr_write_b32 a157, 0\n"
"v_accvgpr_write_b32 a158, 0\n"
"v_accvgpr_write_b32 a159, 0\n"
"v_accvgpr_write_b32 a160, 0\n"
"v_accvgpr_write_b32 a161, 0\n"
"v_accvgpr_write_b32 a162, 0\n"
"v_accvgpr_write_b32 a163, 0\n"
"v_accvgpr_write_b32 a164, 0\n"
"v_accvgpr_write_b32 a165, 0\n"
"v_accvgpr_write_b32 a166, 0\n"
"v_accvgpr_write_b32 a167, 0\n"
"v_accvgpr_write_b32 a168, 0\n"
"v_accvgpr_write_b32 a169, 0\n"
"v_accvgpr_write_b32 a170, 0\n"
"v_accvgpr_write_b32 a171, 0\n"
"v_accvgpr_write_b32 a172, 0\n"
"v_accvgpr_write_b32 a173, 0\n"
"v_accvgpr_write_b32 a174, 0\n"
"v_accvgpr_write_b32 a175, 0\n"
"v_accvgpr_write_b32 a176, 0\n"
"v_accvgpr_write_b32 a177, 0\n"
"v_accvgpr_write_b32 a178, 0\n"
"v_accvgpr_write_b32 a179, 0\n"
"v_accvgpr_write_b32 a180, 0\n"
"v_accvgpr_write_b32 a181, 0\n"
"v_accvgpr_write_b32 a182, 0\n"
"v_accvgpr_write_b32 a183, 0\n"
"v_accvgpr_write_b32 a184, 0\n"
"v_accvgpr_write_b32 a185, 0\n"
"v_accvgpr_write_b32 a186, 0\n"
"v_accvgpr_write_b32 a187, 0\n"
"v_accvgpr_write_b32 a188, 0\n"
"v_accvgpr_write_b32 a189, 0\n"
"v_accvgpr_write_b32 a190, 0\n"
"v_accvgpr_write_b32 a191, 0\n"
"s_mov_b32 s73, 0\n"
".Lf_slow1:\n"
"buffer_load_dwordx4 v[100:103], v96, s[36:39], s73 offen offset:0\n"
"buffer_load_dwordx4 v[104:107], v96, s[36:39], s73 offen offset:64\n"
"buffer_load_dwordx4 v[108:111], v96, s[36:39], s73 offen offset:128\n"
"buffer_load_dwordx4 v[112:115], v96, s[36:39], s73 offen offset:192\n"
"buffer_load_dwordx4 v[116:119], v96, s[36:39], s73 offen offset:1024\n"
"buffer_load_dwordx4 v[120:123], v96, s[36:39], s73 offen offset:1088\n"
"buffer_load_dwordx4 v[124:127], v96, s[36:39], s73 offen offset:1152\n"
"buffer_load_dwordx4 v[128:131], v96, s[36:39], s73 offen offset:1216\n"
"s_waitcnt vmcnt(0)\n"
"v_mfma_f32_16x16x32_bf16 v[164:167], v[100:103], v[0:3], 0\n"
"v_mfma_f32_16x16x32_bf16 v[164:167], v[104:107], v[4:7], v[164:167]\n"
"v_mfma_f32_16x16x32_bf16 v[164:167], v[108:111], v[8:11], v[164:167]\n"
"v_mfma_f32_16x16x32_bf16 v[164:167], v[112:115], v[12:15], v[164:167]\n"
"v_mfma_f32_16x16x32_bf16 v[168:171], v[116:119], v[0:3], 0\n"
"v_mfma_f32_16x16x32_bf16 v[168:171], v[120:123], v[4:7], v[168:171]\n"
"v_mfma_f32_16x16x32_bf16 v[168:171], v[124:127], v[8:11], v[168:171]\n"
"v_mfma_f32_16x16x32_bf16 v[168:171], v[128:131], v[12:15], v[168:171]\n"
"s_nop 3\n"
"v_max3_f32 v176, v176, v164, v165\n"
"v_max3_f32 v176, v176, v166, v167\n"
"s_nop 1\n"
"v_max3_f32 v176, v176, v168, v169\n"
"v_max3_f32 v176, v176, v170, v171\n"
"v_mfma_f32_16x16x32_bf16 v[164:167], v[100:103], v[16:19], 0\n"
"v_mfma_f32_16x16x32_bf16 v[164:167], v[104:107], v[20:23], v[164:167]\n"
"v_mfma_f32_16x16x32_bf16 v[164:167], v[108:111], v[24:27], v[164:167]\n"
"v_mfma_f32_16x16x32_bf16 v[164:167], v[112:115], v[28:31], v[164:167]\n"
"v_mfma_f32_16x16x32_bf16 v[168:171], v[116:119], v[16:19], 0\n"
"v_mfma_f32_16x16x32_bf16 v[168:171], v[120:123], v[20:23], v[168:171]\n"
"v_mfma_f32_16x16x32_bf16 v[168:171], v[124:127], v[24:27], v[168:171]\n"
"v_mfma_f32_16x16x32_bf16 v[168:171], v[128:131], v[28:31], v[168:171]\n"
"s_nop 3\n"
"v_max3_f32 v177, v177, v164, v165\n"
"v_max3_f32 v177, v177, v166, v167\n"
"s_nop 1\n"
"v_max3_f32 v177, v177, v168, v169\n"
"v_max3_f32 v177, v177, v170, v171\n"
"v_mfma_f32_16x16x32_bf16 v[164:167], v[100:103], v[32:35], 0\n"
"v_mfma_f32_16x16x32_bf16 v[164:167], v[104:107], v[36:39], v[164:167]\n"
"v_mfma_f32_16x16x32_bf16 v[164:167], v[108:111], v[40:43], v[164:167]\n"
"v_mfma_f32_16x16x32_bf16 v[164:167], v[112:115], v[44:47], v[164:167]\n"
"v_mfma_f32_16x16x32_bf16 v[168:171], v[116:119], v[32:35], 0\n"
"v_mfma_f32_16x16x32_bf16 v[168:171], v[120:123], v[36:39], v[168:171]\n"
"v_mfma_f32_16x16x32_bf16 v[168:171], v[124:127], v[40:43], v[168:171]\n"
"v_mfma_f32_16x16x32_bf16 v[168:171], v[128:131], v[44:47], v[168:171]\n"
"s_nop 3\n"
"v_max3_f32 v178, v178, v164, v165\n"
"v_max3_f32 v178, v178, v166, v167\n"
"s_nop 1\n"
"v_max3_f32 v178, v178, v168, v169\n"
"v_max3_f32 v178, v178, v170, v171\n"
"v_mfma_f32_16x16x32_bf16 v[164:167], v[100:103], v[48:51], 0\n"
"v_mfma_f32_16x16x32_bf16 v[164:167], v[104:107], v[52:55], v[164:167]\n"
"v_mfma_f32_16x16x32_bf16 v[164:167], v[108:111], v[56:59], v[164:167]\n"
"v_mfma_f32_16x16x32_bf16 v[164:167], v[112:115], v[60:63], v[164:167]\n"
"v_mfma_f32_16x16x32_bf16 v[168:171], v[116:119], v[48:51], 0\n"
"v_mfma_f32_16x16x32_bf16 v[168:171], v[120:123], v[52:55], v[168:171]\n"
"v_mfma_f32_16x16x32_bf16 v[168:171], v[124:127], v[56:59], v[168:171]\n"
"v_mfma_f32_16x16x32_bf16 v[168:171], v[128:131], v[60:63], v[168:171]\n"
"s_nop 3\n"
"v_max3_f32 v179, v179, v164, v165\n"
"v_max3_f32 v179, v179, v166, v167\n"
"s_nop 1\n"
"v_max3_f32 v179, v179, v168, v169\n"
"v_max3_f32 v179, v179, v170, v171\n"
"v_mfma_f32_16x16x32_bf16 v[164:167], v[100:103], v[64:67], 0\n"
"v_mfma_f32_16x16x32_bf16 v[164:167], v[104:107], v[68:71], v[164:167]\n"
"v_mfma_f32_16x16x32_bf16 v[164:167], v[108:111], v[72:75], v[164:167]\n"
"v_mfma_f32_16x16x32_bf16 v[164:167], v[112:115], v[76:79], v[164:167]\n"
"v_mfma_f32_16x16x32_bf16 v[168:171], v[116:119], v[64:67], 0\n"
"v_mfma_f32_16x16x32_bf16 v[168:171], v[120:123], v[68:71], v[168:171]\n"
"v_mfma_f32_16x16x32_bf16 v[168:171], v[124:127], v[72:75], v[168:171]\n"
"v_mfma_f32_16x16x32_bf16 v[168:171], v[128:131], v[76:79], v[168:171]\n"
"s_nop 3\n"
"v_max3_f32 v180, v180, v164, v165\n"
"v_max3_f32 v180, v180, v166, v167\n"
"s_nop 1\n"
"v_max3_f32 v180, v180, v168, v169\n"
"v_max3_f32 v180, v180, v170, v171\n"
"v_mfma_f32_16x16x32_bf16 v[164:167], v[100:103], v[80:83], 0\n"
"v_mfma_f32_16x16x32_bf16 v[164:167], v[104:107], v[84:87], v[164:167]\n"
"v_mfma_f32_16x16x32_bf16 v[164:167], v[108:111], v[88:91], v[164:167]\n"
"v_mfma_f32_16x16x32_bf16 v[164:167], v[112:115], v[92:95], v[164:167]\n"
"v_mfma_f32_16x16x32_bf16 v[168:171], v[116:119], v[80:83], 0\n"
"v_mfma_f32_16x16x32_bf16 v[168:171], v[120:123], v[84:87], v[168:171]\n"
"v_mfma_f32_16x16x32_bf16 v[168:171], v[124:127], v[88:91], v[168:171]\n"
"v_mfma_f32_16x16x32_bf16 v[168:171], v[128:131], v[92:95], v[168:171]\n"
"s_nop 3\n"
"v_max3_f32 v181, v181, v164, v165\n"
"v_max3_f32 v181, v181, v166, v167\n"
"s_nop 1\n"
"v_max3_f32 v181, v181, v168, v169\n"
"v_max3_f32 v181, v181, v170, v171\n"
"s_add_u32 s73, s73, 8192\n"
"s_cmp_lt_u32 s73, 0x20000\n"
"s_cbranch_scc1 .Lf_slow1\n"
"v_mov_b32 v98, v176\n"
"s_nop 1\n"
"v_permlane32_swap_b32 v176, v98\n"
"v_max_f32 v176, v176, v98\n"
"v_mov_b32 v98, v176\n"
"s_nop 1\n"
"v_permlane16_swap_b32 v176, v98\n"
"v_max_f32 v176, v176, v98\n"
"v_sub_f32 v192, 0, v176\n"
"v_sub_f32 v193, 0, v176\n"
"v_sub_f32 v194, 0, v176\n"
"v_sub_f32 v195, 0, v176\n"
"v_mov_b32 v98, v177\n"
"s_nop 1\n"
"v_permlane32_swap_b32 v177, v98\n"
"v_max_f32 v177, v177, v98\n"
"v_mov_b32 v98, v177\n"
"s_nop 1\n"
"v_permlane16_swap_b32 v177, v98\n"
"v_max_f32 v177, v177, v98\n"
"v_sub_f32 v196, 0, v177\n"
"v_sub_f32 v197, 0, v177\n"
"v_sub_f32 v198, 0, v177\n"
"v_sub_f32 v199, 0, v177\n"
"v_mov_b32 v98, v178\n"
"s_nop 1\n"
"v_permlane32_swap_b32 v178, v98\n"
"v_max_f32 v178, v178, v98\n"
"v_mov_b32 v98, v178\n"
"s_nop 1\n"
"v_permlane16_swap_b32 v178, v98\n"
"v_max_f32 v178, v178, v98\n"
"v_sub_f32 v200, 0, v178\n"
"v_sub_f32 v201, 0, v178\n"
"v_sub_f32 v202, 0, v178\n"
"v_sub_f32 v203, 0, v178\n"
"v_mov_b32 v98, v179\n"
"s_nop 1\n"
"v_permlane32_swap_b32 v179, v98\n"
"v_max_f32 v179, v179, v98\n"
"v_mov_b32 v98, v179\n"
"s_nop 1\n"
"v_permlane16_swap_b32 v179, v98\n"
"v_max_f32 v179, v179, v98\n"
"v_sub_f32 v204, 0, v179\n"
"v_sub_f32 v205, 0, v179\n"
"v_sub_f32 v206, 0, v179\n"
"v_sub_f32 v207, 0, v179\n"
"v_mov_b32 v98, v180\n"
"s_nop 1\n"
"v_permlane32_swap_b32 v180, v98\n"
"v_max_f32 v180, v180, v98\n"
"v_mov_b32 v98, v180\n"
"s_nop 1\n"
"v_permlane16_swap_b32 v180, v98\n"
"v_max_f32 v180, v180, v98\n"
"v_sub_f32 v208, 0, v180\n"
"v_sub_f32 v209, 0, v180\n"
"v_sub_f32 v210, 0, v180\n"
"v_sub_f32 v211, 0, v180\n"
"v_mov_b32 v98, v181\n"
"s_nop 1\n"
"v_permlane32_swap_b32 v181, v98\n"
"v_max_f32 v181, v181, v98\n"
"v_mov_b32 v98, v181\n"
"s_nop 1\n"
"v_permlane16_swap_b32 v181, v98\n"
"v_max_f32 v181, v181, v98\n"
"v_sub_f32 v212, 0, v181\n"
"v_sub_f32 v213, 0, v181\n"
"v_sub_f32 v214, 0, v181\n"
"v_sub_f32 v215, 0, v181\n"
"s_mov_b32 s73, 0\n"
".Lf_slow2:\n"
"buffer_load_dwordx4 v[100:103], v96, s[36:39], s73 offen offset:0\n"
"buffer_load_dwordx4 v[104:107], v96, s[36:39], s73 offen offset:64\n"
"buffer_load_dwordx4 v[108:111], v96, s[36:39], s73 offen offset:128\n"
"buffer_load_dwordx4 v[112:115], v96, s[36:39], s73 offen offset:192\n"
"buffer_load_dwordx4 v[116:119], v96, s[36:39], s73 offen offset:1024\n"
"buffer_load_dwordx4 v[120:123], v96, s[36:39], s73 offen offset:1088\n"
"buffer_load_dwordx4 v[124:127], v96, s[36:39], s73 offen offset:1152\n"
"buffer_load_dwordx4 v[128:131], v96, s[36:39], s73 offen offset:1216\n"
"buffer_load_ushort v176, v97, s[40:43], s73 offen offset:0\n"
"buffer_load_ushort v177, v97, s[40:43], s73 offen offset:256\n"
"buffer_load_ushort v178, v97, s[40:43], s73 offen offset:512\n"
"buffer_load_ushort v179, v97, s[40:43], s73 offen offset:768\n"
"buffer_load_ushort v180, v97, s[40:43], s73 offen offset:1024\n"
"buffer_load_ushort v181, v97, s[40:43], s73 offen offset:1280\n"
"buffer_load_ushort v182, v97, s[40:43], s73 offen offset:1536\n"
"buffer_load_ushort v183, v97, s[40:43], s73 offen offset:1792\n"
"buffer_load_ushort v184, v97, s[40:43], s73 offen offset:32\n"
"buffer_load_ushort v185, v97, s[40:43], s73 offen offset:288\n"
"buffer_load_ushort v186, v97, s[40:43], s73 offen offset:544\n"
"buffer_load_ushort v187, v97, s[40:43], s73 offen offset:800\n"
"buffer_load_ushort v188, v97, s[40:43], s73 offen offset:1056\n"
"buffer_load_ushort v189, v97, s[40:43], s73 offen offset:1312\n"
"buffer_load_ushort v190, v97, s[40:43], s73 offen offset:1568\n"
"buffer_load_ushort v191, v97, s[40:43], s73 offen offset:1824\n"
"s_waitcnt vmcnt(0)\n"
"v_lshl_or_b32 v132, v177, 16, v176\n"
"v_lshl_or_b32 v133, v179, 16, v178\n"
"v_lshl_or_b32 v134, v181, 16, v180\n"
"v_lshl_or_b32 v135, v183, 16, v182\n"
"v_lshl_or_b32 v136, v185, 16, v184\n"
"v_lshl_or_b32 v137, v187, 16, v186\n"
"v_lshl_or_b32 v138, v189, 16, v188\n"
"v_lshl_or_b32 v139, v191, 16, v190\n"
"buffer_load_ushort v176, v97, s[40:43], s73 offen offset:64\n"
"buffer_load_ushort v177, v97, s[40:43], s73 offen offset:320\n"
"buffer_load_ushort v178, v97, s[40:43], s73 offen offset:576\n"
"buffer_load_ushort v179, v97, s[40:43], s73 offen offset:832\n"
"buffer_load_ushort v180, v97, s[40:43], s73 offen offset:1088\n"
"buffer_load_ushort v181, v97, s[40:43], s73 offen offset:1344\n"
"buffer_load_ushort v182, v97, s[40:43], s73 offen offset:1600\n"
"buffer_load_ushort v183, v97, s[40:43], s73 offen offset:1856\n"
"buffer_load_ushort v184, v97, s[40:43], s73 offen offset:96\n"
"buffer_load_ushort v185, v97, s[40:43], s73 offen offset:352\n"
"buffer_load_ushort v186, v97, s[40:43], s73 offen offset:608\n"
"buffer_load_ushort v187, v97, s[40:43], s73 offen offset:864\n"
"buffer_load_ushort v188, v97, s[40:43], s73 offen offset:1120\n"
"buffer_load_ushort v189, v97, s[40:43], s73 offen offset:1376\n"
"buffer_load_ushort v190, v97, s[40:43], s73 offen offset:1632\n"
"buffer_load_ushort v191, v97, s[40:43], s73 offen offset:1888\n"
"s_waitcnt vmcnt(0)\n"
"v_lshl_or_b32 v140, v177, 16, v176\n"
"v_lshl_or_b32 v141, v179, 16, v178\n"
"v_lshl_or_b32 v142, v181, 16, v180\n"
"v_lshl_or_b32 v143, v183, 16, v182\n"
"v_lshl_or_b32 v144, v185, 16, v184\n"
"v_lshl_or_b32 v145, v187, 16, v186\n"
"v_lshl_or_b32 v146, v189, 16, v188\n"
"v_lshl_or_b32 v147, v191, 16, v190\n"
"buffer_load_ushort v176, v97, s[40:43], s73 offen offset:128\n"
"buffer_load_ushort v177, v97, s[40:43], s73 offen offset:384\n"
"buffer_load_ushort v178, v97, s[40:43], s73 offen offset:640\n"
"buffer_load_ushort v179, v97, s[40:43], s73 offen offset:896\n"
"buffer_load_ushort v180, v97, s[40:43], s73 offen offset:1152\n"
"buffer_load_ushort v181, v97, s[40:43], s73 offen offset:1408\n"
"buffer_load_ushort v182, v97, s[40:43], s73 offen offset:1664\n"
"buffer_load_ushort v183, v97, s[40:43], s73 offen offset:1920\n"
"buffer_load_ushort v184, v97, s[40:43], s73 offen offset:160\n"
"buffer_load_ushort v185, v97, s[40:43], s73 offen offset:416\n"
"buffer_load_ushort v186, v97, s[40:43], s73 offen offset:672\n"
"buffer_load_ushort v187, v97, s[40:43], s73 offen offset:928\n"
"buffer_load_ushort v188, v97, s[40:43], s73 offen offset:1184\n"
"buffer_load_ushort v189, v97, s[40:43], s73 offen offset:1440\n"
"buffer_load_ushort v190, v97, s[40:43], s73 offen offset:1696\n"
"buffer_load_ushort v191, v97, s[40:43], s73 offen offset:1952\n"
"s_waitcnt vmcnt(0)\n"
"v_lshl_or_b32 v148, v177, 16, v176\n"
"v_lshl_or_b32 v149, v179, 16, v178\n"
"v_lshl_or_b32 v150, v181, 16, v180\n"
"v_lshl_or_b32 v151, v183, 16, v182\n"
"v_lshl_or_b32 v152, v185, 16, v184\n"
"v_lshl_or_b32 v153, v187, 16, v186\n"
"v_lshl_or_b32 v154, v189, 16, v188\n"
"v_lshl_or_b32 v155, v191, 16, v190\n"
"buffer_load_ushort v176, v97, s[40:43], s73 offen offset:192\n"
"buffer_load_ushort v177, v97, s[40:43], s73 offen offset:448\n"
"buffer_load_ushort v178, v97, s[40:43], s73 offen offset:704\n"
"buffer_load_ushort v179, v97, s[40:43], s73 offen offset:960\n"
"buffer_load_ushort v180, v97, s[40:43], s73 offen offset:1216\n"
"buffer_load_ushort v181, v97, s[40:43], s73 offen offset:1472\n"
"buffer_load_ushort v182, v97, s[40:43], s73 offen offset:1728\n"
"buffer_load_ushort v183, v97, s[40:43], s73 offen offset:1984\n"
"buffer_load_ushort v184, v97, s[40:43], s73 offen offset:224\n"
"buffer_load_ushort v185, v97, s[40:43], s73 offen offset:480\n"
"buffer_load_ushort v186, v97, s[40:43], s73 offen offset:736\n"
"buffer_load_ushort v187, v97, s[40:43], s73 offen offset:992\n"
"buffer_load_ushort v188, v97, s[40:43], s73 offen offset:1248\n"
"buffer_load_ushort v189, v97, s[40:43], s73 offen offset:1504\n"
"buffer_load_ushort v190, v97, s[40:43], s73 offen offset:1760\n"
"buffer_load_ushort v191, v97, s[40:43], s73 offen offset:2016\n"
"s_waitcnt vmcnt(0)\n"
"v_lshl_or_b32 v156, v177, 16, v176\n"
"v_lshl_or_b32 v157, v179, 16, v178\n"
"v_lshl_or_b32 v158, v181, 16, v180\n"
"v_lshl_or_b32 v159, v183, 16, v182\n"
"v_lshl_or_b32 v160, v185, 16, v184\n"
"v_lshl_or_b32 v161, v187, 16, v186\n"
"v_lshl_or_b32 v162, v189, 16, v188\n"
"v_lshl_or_b32 v163, v191, 16, v190\n"
"v_mfma_f32_16x16x32_bf16 v[164:167], v[100:103], v[0:3], v[192:195]\n"
"v_mfma_f32_16x16x32_bf16 v[164:167], v[104:107], v[4:7], v[164:167]\n"
"v_mfma_f32_16x16x32_bf16 v[164:167], v[108:111], v[8:11], v[164:167]\n"
"v_mfma_f32_16x16x32_bf16 v[164:167], v[112:115], v[12:15], v[164:167]\n"
"v_mfma_f32_16x16x32_bf16 v[168:171], v[116:119], v[0:3], v[192:195]\n"
"v_mfma_f32_16x16x32_bf16 v[168:171], v[120:123], v[4:7], v[168:171]\n"
"v_mfma_f32_16x16x32_bf16 v[168:171], v[124:127], v[8:11], v[168:171]\n"
"v_mfma_f32_16x16x32_bf16 v[168:171], v[128:131], v[12:15], v[168:171]\n"
"s_nop 3\n"
"v_exp_f32 v164, v164\n"
"v_exp_f32 v165, v165\n"
"v_exp_f32 v166, v166\n"
"v_exp_f32 v167, v167\n"
"v_exp_f32 v168, v168\n"
"v_exp_f32 v169, v169\n"
"v_exp_f32 v170, v170\n"
"v_exp_f32 v171, v171\n"
"v_add_f32 v216, v216, v164\n"
"v_add_f32 v216, v216, v165\n"
"v_add_f32 v216, v216, v166\n"
"v_add_f32 v216, v216, v167\n"
"v_add_f32 v216, v216, v168\n"
"v_add_f32 v216, v216, v169\n"
"v_add_f32 v216, v216, v170\n"
"v_add_f32 v216, v216, v171\n"
"v_cvt_pk_bf16_f32 v172, v164, v165\n"
"v_cvt_pk_bf16_f32 v173, v166, v167\n"
"v_cvt_pk_bf16_f32 v174, v168, v169\n"
"v_cvt_pk_bf16_f32 v175, v170, v171\n"
"s_nop 1\n"
"v_mfma_f32_16x16x32_bf16 a[0:3], v[132:135], v[172:175], a[0:3]\n"
"v_mfma_f32_16x16x32_bf16 a[24:27], v[136:139], v[172:175], a[24:27]\n"
"v_mfma_f32_16x16x32_bf16 a[48:51], v[140:143], v[172:175], a[48:51]\n"
"v_mfma_f32_16x16x32_bf16 a[72:75], v[144:147], v[172:175], a[72:75]\n"
"v_mfma_f32_16x16x32_bf16 a[96:99], v[148:151], v[172:175], a[96:99]\n"
"v_mfma_f32_16x16x32_bf16 a[120:123], v[152:155], v[172:175], a[120:123]\n"
"v_mfma_f32_16x16x32_bf16 a[144:147], v[156:159], v[172:175], a[144:147]\n"
"v_mfma_f32_16x16x32_bf16 a[168:171], v[160:163], v[172:175], a[168:171]\n"
"v_mfma_f32_16x16x32_bf16 v[164:167], v[100:103], v[16:19], v[196:199]\n"
"v_mfma_f32_16x16x32_bf16 v[164:167], v[104:107], v[20:23], v[164:167]\n"
"v_mfma_f32_16x16x32_bf16 v[164:167], v[108:111], v[24:27], v[164:167]\n"
"v_mfma_f32_16x16x32_bf16 v[164:167], v[112:115], v[28:31], v[164:167]\n"
"v_mfma_f32_16x16x32_bf16 v[168:171], v[116:119], v[16:19], v[196:199]\n"
"v_mfma_f32_16x16x32_bf16 v[168:171], v[120:123], v[20:23], v[168:171]\n"
"v_mfma_f32_16x16x32_bf16 v[168:171], v[124:127], v[24:27], v[168:171]\n"
"v_mfma_f32_16x16x32_bf16 v[168:171], v[128:131], v[28:31], v[168:171]\n"
"s_nop 3\n"
"v_exp_f32 v164, v164\n"
"v_exp_f32 v165, v165\n"
"v_exp_f32 v166, v166\n"
"v_exp_f32 v167, v167\n"
"v_exp_f32 v168, v168\n"
"v_exp_f32 v169, v169\n"
"v_exp_f32 v170, v170\n"
"v_exp_f32 v171, v171\n"
"v_add_f32 v217, v217, v164\n"
"v_add_f32 v217, v217, v165\n"
"v_add_f32 v217, v217, v166\n"
"v_add_f32 v217, v217, v167\n"
"v_add_f32 v217, v217, v168\n"
"v_add_f32 v217, v217, v169\n"
"v_add_f32 v217, v217, v170\n"
"v_add_f32 v217, v217, v171\n"
"v_cvt_pk_bf16_f32 v172, v164, v165\n"
"v_cvt_pk_bf16_f32 v173, v166, v167\n"
"v_cvt_pk_bf16_f32 v174, v168, v169\n"
"v_cvt_pk_bf16_f32 v175, v170, v171\n"
"s_nop 1\n"
"v_mfma_f32_16x16x32_bf16 a[4:7], v[132:135], v[172:175], a[4:7]\n"
"v_mfma_f32_16x16x32_bf16 a[28:31], v[136:139], v[172:175], a[28:31]\n"
"v_mfma_f32_16x16x32_bf16 a[52:55], v[140:143], v[172:175], a[52:55]\n"
"v_mfma_f32_16x16x32_bf16 a[76:79], v[144:147], v[172:175], a[76:79]\n"
"v_mfma_f32_16x16x32_bf16 a[100:103], v[148:151], v[172:175], a[100:103]\n"
"v_mfma_f32_16x16x32_bf16 a[124:127], v[152:155], v[172:175], a[124:127]\n"
"v_mfma_f32_16x16x32_bf16 a[148:151], v[156:159], v[172:175], a[148:151]\n"
"v_mfma_f32_16x16x32_bf16 a[172:175], v[160:163], v[172:175], a[172:175]\n"
"v_mfma_f32_16x16x32_bf16 v[164:167], v[100:103], v[32:35], v[200:203]\n"
"v_mfma_f32_16x16x32_bf16 v[164:167], v[104:107], v[36:39], v[164:167]\n"
"v_mfma_f32_16x16x32_bf16 v[164:167], v[108:111], v[40:43], v[164:167]\n"
"v_mfma_f32_16x16x32_bf16 v[164:167], v[112:115], v[44:47], v[164:167]\n"
"v_mfma_f32_16x16x32_bf16 v[168:171], v[116:119], v[32:35], v[200:203]\n"
"v_mfma_f32_16x16x32_bf16 v[168:171], v[120:123], v[36:39], v[168:171]\n"
"v_mfma_f32_16x16x32_bf16 v[168:171], v[124:127], v[40:43], v[168:171]\n"
"v_mfma_f32_16x16x32_bf16 v[168:171], v[128:131], v[44:47], v[168:171]\n"
"s_nop 3\n"
"v_exp_f32 v164, v164\n"
"v_exp_f32 v165, v165\n"
"v_exp_f32 v166, v166\n"
"v_exp_f32 v167, v167\n"
"v_exp_f32 v168, v168\n"
"v_exp_f32 v169, v169\n"
"v_exp_f32 v170, v170\n"
"v_exp_f32 v171, v171\n"
"v_add_f32 v218, v218, v164\n"
"v_add_f32 v218, v218, v165\n"
"v_add_f32 v218, v218, v166\n"
"v_add_f32 v218, v218, v167\n"
"v_add_f32 v218, v218, v168\n"
"v_add_f32 v218, v218, v169\n"
"v_add_f32 v218, v218, v170\n"
"v_add_f32 v218, v218, v171\n"
"v_cvt_pk_bf16_f32 v172, v164, v165\n"
"v_cvt_pk_bf16_f32 v173, v166, v167\n"
"v_cvt_pk_bf16_f32 v174, v168, v169\n"
"v_cvt_pk_bf16_f32 v175, v170, v171\n"
"s_nop 1\n"
"v_mfma_f32_16x16x32_bf16 a[8:11], v[132:135], v[172:175], a[8:11]\n"
"v_mfma_f32_16x16x32_bf16 a[32:35], v[136:139], v[172:175], a[32:35]\n"
"v_mfma_f32_16x16x32_bf16 a[56:59], v[140:143], v[172:175], a[56:59]\n"
"v_mfma_f32_16x16x32_bf16 a[80:83], v[144:147], v[172:175], a[80:83]\n"
"v_mfma_f32_16x16x32_bf16 a[104:107], v[148:151], v[172:175], a[104:107]\n"
"v_mfma_f32_16x16x32_bf16 a[128:131], v[152:155], v[172:175], a[128:131]\n"
"v_mfma_f32_16x16x32_bf16 a[152:155], v[156:159], v[172:175], a[152:155]\n"
"v_mfma_f32_16x16x32_bf16 a[176:179], v[160:163], v[172:175], a[176:179]\n"
"v_mfma_f32_16x16x32_bf16 v[164:167], v[100:103], v[48:51], v[204:207]\n"
"v_mfma_f32_16x16x32_bf16 v[164:167], v[104:107], v[52:55], v[164:167]\n"
"v_mfma_f32_16x16x32_bf16 v[164:167], v[108:111], v[56:59], v[164:167]\n"
"v_mfma_f32_16x16x32_bf16 v[164:167], v[112:115], v[60:63], v[164:167]\n"
"v_mfma_f32_16x16x32_bf16 v[168:171], v[116:119], v[48:51], v[204:207]\n"
"v_mfma_f32_16x16x32_bf16 v[168:171], v[120:123], v[52:55], v[168:171]\n"
"v_mfma_f32_16x16x32_bf16 v[168:171], v[124:127], v[56:59], v[168:171]\n"
"v_mfma_f32_16x16x32_bf16 v[168:171], v[128:131], v[60:63], v[168:171]\n"
"s_nop 3\n"
"v_exp_f32 v164, v164\n"
"v_exp_f32 v165, v165\n"
"v_exp_f32 v166, v166\n"
"v_exp_f32 v167, v167\n"
"v_exp_f32 v168, v168\n"
"v_exp_f32 v169, v169\n"
"v_exp_f32 v170, v170\n"
"v_exp_f32 v171, v171\n"
"v_add_f32 v219, v219, v164\n"
"v_add_f32 v219, v219, v165\n"
"v_add_f32 v219, v219, v166\n"
"v_add_f32 v219, v219, v167\n"
"v_add_f32 v219, v219, v168\n"
"v_add_f32 v219, v219, v169\n"
"v_add_f32 v219, v219, v170\n"
"v_add_f32 v219, v219, v171\n"
"v_cvt_pk_bf16_f32 v172, v164, v165\n"
"v_cvt_pk_bf16_f32 v173, v166, v167\n"
"v_cvt_pk_bf16_f32 v174, v168, v169\n"
"v_cvt_pk_bf16_f32 v175, v170, v171\n"
"s_nop 1\n"
"v_mfma_f32_16x16x32_bf16 a[12:15], v[132:135], v[172:175], a[12:15]\n"
"v_mfma_f32_16x16x32_bf16 a[36:39], v[136:139], v[172:175], a[36:39]\n"
"v_mfma_f32_16x16x32_bf16 a[60:63], v[140:143], v[172:175], a[60:63]\n"
"v_mfma_f32_16x16x32_bf16 a[84:87], v[144:147], v[172:175], a[84:87]\n"
"v_mfma_f32_16x16x32_bf16 a[108:111], v[148:151], v[172:175], a[108:111]\n"
"v_mfma_f32_16x16x32_bf16 a[132:135], v[152:155], v[172:175], a[132:135]\n"
"v_mfma_f32_16x16x32_bf16 a[156:159], v[156:159], v[172:175], a[156:159]\n"
"v_mfma_f32_16x16x32_bf16 a[180:183], v[160:163], v[172:175], a[180:183]\n"
"v_mfma_f32_16x16x32_bf16 v[164:167], v[100:103], v[64:67], v[208:211]\n"
"v_mfma_f32_16x16x32_bf16 v[164:167], v[104:107], v[68:71], v[164:167]\n"
"v_mfma_f32_16x16x32_bf16 v[164:167], v[108:111], v[72:75], v[164:167]\n"
"v_mfma_f32_16x16x32_bf16 v[164:167], v[112:115], v[76:79], v[164:167]\n"
"v_mfma_f32_16x16x32_bf16 v[168:171], v[116:119], v[64:67], v[208:211]\n"
"v_mfma_f32_16x16x32_bf16 v[168:171], v[120:123], v[68:71], v[168:171]\n"
"v_mfma_f32_16x16x32_bf16 v[168:171], v[124:127], v[72:75], v[168:171]\n"
"v_mfma_f32_16x16x32_bf16 v[168:171], v[128:131], v[76:79], v[168:171]\n"
"s_nop 3\n"
"v_exp_f32 v164, v164\n"
"v_exp_f32 v165, v165\n"
"v_exp_f32 v166, v166\n"
"v_exp_f32 v167, v167\n"
"v_exp_f32 v168, v168\n"
"v_exp_f32 v169, v169\n"
"v_exp_f32 v170, v170\n"
"v_exp_f32 v171, v171\n"
"v_add_f32 v220, v220, v164\n"
"v_add_f32 v220, v220, v165\n"
"v_add_f32 v220, v220, v166\n"
"v_add_f32 v220, v220, v167\n"
"v_add_f32 v220, v220, v168\n"
"v_add_f32 v220, v220, v169\n"
"v_add_f32 v220, v220, v170\n"
"v_add_f32 v220, v220, v171\n"
"v_cvt_pk_bf16_f32 v172, v164, v165\n"
"v_cvt_pk_bf16_f32 v173, v166, v167\n"
"v_cvt_pk_bf16_f32 v174, v168, v169\n"
"v_cvt_pk_bf16_f32 v175, v170, v171\n"
"s_nop 1\n"
"v_mfma_f32_16x16x32_bf16 a[16:19], v[132:135], v[172:175], a[16:19]\n"
"v_mfma_f32_16x16x32_bf16 a[40:43], v[136:139], v[172:175], a[40:43]\n"
"v_mfma_f32_16x16x32_bf16 a[64:67], v[140:143], v[172:175], a[64:67]\n"
"v_mfma_f32_16x16x32_bf16 a[88:91], v[144:147], v[172:175], a[88:91]\n"
"v_mfma_f32_16x16x32_bf16 a[112:115], v[148:151], v[172:175], a[112:115]\n"
"v_mfma_f32_16x16x32_bf16 a[136:139], v[152:155], v[172:175], a[136:139]\n"
"v_mfma_f32_16x16x32_bf16 a[160:163], v[156:159], v[172:175], a[160:163]\n"
"v_mfma_f32_16x16x32_bf16 a[184:187], v[160:163], v[172:175], a[184:187]\n"
"v_mfma_f32_16x16x32_bf16 v[164:167], v[100:103], v[80:83], v[212:215]\n"
"v_mfma_f32_16x16x32_bf16 v[164:167], v[104:107], v[84:87], v[164:167]\n"
"v_mfma_f32_16x16x32_bf16 v[164:167], v[108:111], v[88:91], v[164:167]\n"
"v_mfma_f32_16x16x32_bf16 v[164:167], v[112:115], v[92:95], v[164:167]\n"
"v_mfma_f32_16x16x32_bf16 v[168:171], v[116:119], v[80:83], v[212:215]\n"
"v_mfma_f32_16x16x32_bf16 v[168:171], v[120:123], v[84:87], v[168:171]\n"
"v_mfma_f32_16x16x32_bf16 v[168:171], v[124:127], v[88:91], v[168:171]\n"
"v_mfma_f32_16x16x32_bf16 v[168:171], v[128:131], v[92:95], v[168:171]\n"
"s_nop 3\n"
"v_exp_f32 v164, v164\n"
"v_exp_f32 v165, v165\n"
"v_exp_f32 v166, v166\n"
"v_exp_f32 v167, v167\n"
"v_exp_f32 v168, v168\n"
"v_exp_f32 v169, v169\n"
"v_exp_f32 v170, v170\n"
"v_exp_f32 v171, v171\n"
"v_add_f32 v221, v221, v164\n"
"v_add_f32 v221, v221, v165\n"
"v_add_f32 v221, v221, v166\n"
"v_add_f32 v221, v221, v167\n"
"v_add_f32 v221, v221, v168\n"
"v_add_f32 v221, v221, v169\n"
"v_add_f32 v221, v221, v170\n"
"v_add_f32 v221, v221, v171\n"
"v_cvt_pk_bf16_f32 v172, v164, v165\n"
"v_cvt_pk_bf16_f32 v173, v166, v167\n"
"v_cvt_pk_bf16_f32 v174, v168, v169\n"
"v_cvt_pk_bf16_f32 v175, v170, v171\n"
"s_nop 1\n"
"v_mfma_f32_16x16x32_bf16 a[20:23], v[132:135], v[172:175], a[20:23]\n"
"v_mfma_f32_16x16x32_bf16 a[44:47], v[136:139], v[172:175], a[44:47]\n"
"v_mfma_f32_16x16x32_bf16 a[68:71], v[140:143], v[172:175], a[68:71]\n"
"v_mfma_f32_16x16x32_bf16 a[92:95], v[144:147], v[172:175], a[92:95]\n"
"v_mfma_f32_16x16x32_bf16 a[116:119], v[148:151], v[172:175], a[116:119]\n"
"v_mfma_f32_16x16x32_bf16 a[140:143], v[152:155], v[172:175], a[140:143]\n"
"v_mfma_f32_16x16x32_bf16 a[164:167], v[156:159], v[172:175], a[164:167]\n"
"v_mfma_f32_16x16x32_bf16 a[188:191], v[160:163], v[172:175], a[188:191]\n"
"s_add_u32 s73, s73, 8192\n"
"s_cmp_lt_u32 s73, 0x20000\n"
"s_cbranch_scc1 .Lf_slow2\n"
"v_mov_b32 v98, v216\n"
"s_nop 1\n"
"v_permlane32_swap_b32 v216, v98\n"
"v_add_f32 v216, v216, v98\n"
"v_mov_b32 v98, v216\n"
"s_nop 1\n"
"v_permlane16_swap_b32 v216, v98\n"
"v_add_f32 v216, v216, v98\n"
"v_mov_b32 v98, v217\n"
"s_nop 1\n"
"v_permlane32_swap_b32 v217, v98\n"
"v_add_f32 v217, v217, v98\n"
"v_mov_b32 v98, v217\n"
"s_nop 1\n"
"v_permlane16_swap_b32 v217, v98\n"
"v_add_f32 v217, v217, v98\n"
"v_mov_b32 v98, v218\n"
"s_nop 1\n"
"v_permlane32_swap_b32 v218, v98\n"
"v_add_f32 v218, v218, v98\n"
"v_mov_b32 v98, v218\n"
"s_nop 1\n"
"v_permlane16_swap_b32 v218, v98\n"
"v_add_f32 v218, v218, v98\n"
"v_mov_b32 v98, v219\n"
"s_nop 1\n"
"v_permlane32_swap_b32 v219, v98\n"
"v_add_f32 v219, v219, v98\n"
"v_mov_b32 v98, v219\n"
"s_nop 1\n"
"v_permlane16_swap_b32 v219, v98\n"
"v_add_f32 v219, v219, v98\n"
"v_mov_b32 v98, v220\n"
"s_nop 1\n"
"v_permlane32_swap_b32 v220, v98\n"
"v_add_f32 v220, v220, v98\n"
"v_mov_b32 v98, v220\n"
"s_nop 1\n"
"v_permlane16_swap_b32 v220, v98\n"
"v_add_f32 v220, v220, v98\n"
"v_mov_b32 v98, v221\n"
"s_nop 1\n"
"v_permlane32_swap_b32 v221, v98\n"
"v_add_f32 v221, v221, v98\n"
"v_mov_b32 v98, v221\n"
"s_nop 1\n"
"v_permlane16_swap_b32 v221, v98\n"
"v_add_f32 v221, v221, v98\n"
"s_add_u32 s36, s36, 0x20000\n"
"s_addc_u32 s37, s37, 0\n"
"s_add_u32 s40, s40, 0x20000\n"
"s_addc_u32 s41, s41, 0\n"
"s_nop 15\n"
"s_setpc_b64 s[80:81]\n"
".Lf_end:\n"
  :: "s"(kp), "s"(vp), "s"(xp), "s"(wp), "s"(op), "s"(w), "s"(wrec), "s"(ldsb), "s"(wwarm), "s"(xwarm), "s"(ws), "s"(rot),
     "s"(wop), "s"(olp), "s"(outp), "s"(myflag), "s"(pollflag), "s"(wowarm), "s"(worec), "s"(xsend), "s"(fsend), "s"(kH), "s"(cwarm), "s"(eptr), "s"(kp2), "s"(w), "s"(xrecv), "s"(frecv)
  : "v0","v1","v2","v3","v4","v5","v6","v7","v8","v9","v10","v11","v12","v13","v14","v15","v16","v17","v18","v19","v20","v21","v22","v23","v24","v25","v26","v27","v28","v29","v30","v31","v32","v33","v34","v35","v36","v37","v38","v39","v40","v41","v42","v43","v44","v45","v46","v47","v48","v49","v50","v51","v52","v53","v54","v55","v56","v57","v58","v59","v60","v61","v62","v63","v64","v65","v66","v67","v68","v69","v70","v71","v72","v73","v74","v75","v76","v77","v78","v79","v80","v81","v82","v83","v84","v85","v86","v87","v88","v89","v90","v91","v92","v93","v94","v95","v96","v97","v98","v99","v100","v101","v102","v103","v104","v105","v106","v107","v108","v109","v110","v111","v112","v113","v114","v115","v116","v117","v118","v119","v120","v121","v122","v123","v124","v125","v126","v127","v128","v129","v130","v131","v132","v133","v134","v135","v136","v137","v138","v139","v140","v141","v142","v143","v144","v145","v146","v147","v148","v149","v150","v151","v152","v153","v154","v155","v156","v157","v158","v159","v160","v161","v162","v163","v164","v165","v166","v167","v168","v169","v170","v171","v172","v173","v174","v175","v176","v177","v178","v179","v180","v181","v182","v183","v184","v185","v186","v187","v188","v189","v190","v191","v192","v193","v194","v195","v196","v197","v198","v199","v200","v201","v202","v203","v204","v205","v206","v207","v208","v209","v210","v211","v212","v213","v214","v215","v216","v217","v218","v219","v220","v221","v222","v223","v224","v225","v226","v227","v228","v229","v230","v231","v232","v233","v234","v235","v236","v237","v238","v239","v240","v241","v242","v243","v244","v245","v246","v247","v248","v249","v250","v251","v252","v253","v254","v255","a0","a1","a2","a3","a4","a5","a6","a7","a8","a9","a10","a11","a12","a13","a14","a15","a16","a17","a18","a19","a20","a21","a22","a23","a24","a25","a26","a27","a28","a29","a30","a31","a32","a33","a34","a35","a36","a37","a38","a39","a40","a41","a42","a43","a44","a45","a46","a47","a48","a49","a50","a51","a52","a53","a54","a55","a56","a57","a58","a59","a60","a61","a62","a63","a64","a65","a66","a67","a68","a69","a70","a71","a72","a73","a74","a75","a76","a77","a78","a79","a80","a81","a82","a83","a84","a85","a86","a87","a88","a89","a90","a91","a92","a93","a94","a95","a96","a97","a98","a99","a100","a101","a102","a103","a104","a105","a106","a107","a108","a109","a110","a111","a112","a113","a114","a115","a116","a117","a118","a119","a120","a121","a122","a123","a124","a125","a126","a127","a128","a129","a130","a131","a132","a133","a134","a135","a136","a137","a138","a139","a140","a141","a142","a143","a144","a145","a146","a147","a148","a149","a150","a151","a152","a153","a154","a155","a156","a157","a158","a159","a160","a161","a162","a163","a164","a165","a166","a167","a168","a169","a170","a171","a172","a173","a174","a175","a176","a177","a178","a179","a180","a181","a182","a183","a184","a185","a186","a187","a188","a189","a190","a191","a192","a193","a194","a195","a196","a197","a198","a199","a200","a201","a202","a203","a204","a205","a206","a207","a208","a209","a210","a211","a212","a213","a214","a215","a216","a217","a218","a219","a220","a221","a222","a223","a224","a225","a226","a227","a228","a229","a230","a231","a232","a233","a234","a235","a236","a237","a238","a239","a240","a241","a242","a243","a244","a245","a246","a247","a248","a249","a250","a251","a252","a253","a254","a255","s36","s37","s38","s39","s40","s41","s42","s43","s44","s45","s46","s47","s48","s49","s50","s51","s52","s53","s54","s55","s56","s57","s58","s59","s60","s61","s62","s63","s64","s65","s66","s67","s68","s69","s70","s71","s72","s73","s74","s75","s76","s77","s78","s79","s80","s81","s82","s83","s84","s85","s86","s87","vcc","memory");
}

extern "C" void kernel_launch(void* const* d_in, const int* in_sizes, int n_in, void* d_out, int out_size, void* d_ws, size_t ws_size, hipStream_t stream) {
  (void)in_sizes; (void)n_in; (void)out_size; (void)ws_size;
  gqa_fused<<<256, 256, 0, stream>>>((cp)d_in[0], (cp)d_in[1], (cp)d_in[2], (cp)d_in[3], (cp)d_in[4], (cp)d_in[5], (cp)d_in[6], (cp)d_in[7], (cp)d_in[8], (cp)d_in[9],
                                     (char*)d_ws, (char*)d_out);
}
```
